```python
import torch

def _softmax(x, axis=-1):
    return torch.softmax(x, dim=axis)
def _gelu(x):
    return torch.nn.functional.gelu(x, approximate="tanh")
def _silu(x):
    return torch.nn.functional.silu(x)
def _sigmoid(x):
    return torch.sigmoid(x)
def _layernorm(x, axis=-1, eps=1e-5):
    m = x.mean(dim=axis, keepdim=True)
    v = x.var(dim=axis, keepdim=True, unbiased=False)
    return (x - m) / torch.sqrt(v + eps)
def _scatter_add(tgt, idx, src, axis=0):
    return tgt.clone().index_add_(axis, idx, src)
def _segment_sum(src, seg, n):
    out = torch.zeros((n,) + src.shape[1:], dtype=src.dtype)
    return out.index_add_(0, seg, src)
def _pad(x, pw):
    flat = tuple(v for pair in reversed(pw) for v in pair)
    return torch.nn.functional.pad(x, flat)

kernel_name = 'gated_mix__linear_attn_block__dense_activation_relu__prompt'

def setup_inputs():

    _w = (lambda a: (lambda t: t.to(torch.bfloat16) if t.is_floating_point() else t)(torch.as_tensor(a)))
    def _A_setup():
        B, S, D = 1, 2048, 640
        return {
            "x": _w((lambda *s: torch.randn(*s, dtype=torch.bfloat16) * 0.05)(B, S, D)),
            "wqkv": _w((lambda *s: torch.randn(*s, dtype=torch.bfloat16) * 0.8)(D, 3 * D)),
            "wo": _w((lambda *s: torch.randn(*s, dtype=torch.bfloat16) * 0.2)(D, D)),
        }
    _A_in = _A_setup()

    def _B_setup():
        B, S, D, Dff = 1, 2048, 640, 1536
        return {
            "x": _w((lambda *s: torch.randn(*s, dtype=torch.bfloat16) * 0.05)(B, S, D)),
            "w1": _w((lambda *s: torch.randn(*s, dtype=torch.bfloat16) * 0.05)(D, Dff)),
            "w2": _w((lambda *s: torch.randn(*s, dtype=torch.bfloat16) * 0.05)(Dff, D)),
        }
    _B_in = _B_setup()

    return {
        "A_x": _A_in["x"],
        "A_wqkv": _A_in["wqkv"],
        "A_wo": _A_in["wo"],
        "B_x": _B_in["x"],
        "B_w1": _B_in["w1"],
        "B_w2": _B_in["w2"],
        "gate_w": _w((lambda *s: torch.randn(*s, dtype=torch.bfloat16) * 0.05)(640) * 16.0),
    }

def reference(A_x, A_wqkv, A_wo, B_x, B_w1, B_w2, gate_w):
    def _A_ref(x, wqkv, wo):
        B, S, D = x.shape
        H = 32; Dh = D // H
        qkv = (lambda x, *d: x.permute(*d))((x @ wqkv).reshape(B, S, 3, H, Dh), 2, 0, 3, 1, 4)
        q, k, v = qkv[0], qkv[1], qkv[2]
        phi = lambda t: torch.where(t > 0, t + 1, torch.exp(t)) + 1e-6
        pq, pk = phi(q), phi(k)
        kv = torch.einsum("bhsd,bhse->bhde", pk, v)
        z = torch.einsum("bhsd->bhd", pk)
        num = torch.einsum("bhqd,bhde->bhqe", pq, kv)
        den = torch.einsum("bhqd,bhd->bhq", pq, z)[..., None] + 1e-6
        o = num / den
        return (lambda x, *d: x.permute(*d))(o, 0, 2, 1, 3).reshape(B, S, D) @ wo

    def _B_ref(x, w1, w2):
        h = torch.einsum("bsd,df->bsf", x, w1)
        zero = torch.zeros_like(h)
        h = torch.maximum(h, zero)
        return torch.einsum("bsf,fd->bsd", h, w2)

    x = A_x
    g = _sigmoid((x * gate_w).sum(-1, keepdims=True))
    a = _A_ref(x, A_wqkv, A_wo)
    b = _B_ref(x, B_w1, B_w2)
    return g * a + (1 - g) * b
```

```cpp
#include <hip/hip_runtime.h>
#include <hip/hip_bf16.h>
typedef unsigned short u16;
__device__ __forceinline__ float bf2f(u16 v){ return __uint_as_float(((unsigned)v)<<16); }
__device__ __forceinline__ u16 f2bf(float f){ unsigned u=__float_as_uint(f); u += 0x7fffu + ((u>>16)&1u); return (u16)(u>>16); }
__device__ __forceinline__ float rb(float f){ return bf2f(f2bf(f)); }

__global__ void gemm_naive(const u16* A, const u16* B, u16* C, int M, int N, int K, int relu){
  int n = blockIdx.x*blockDim.x+threadIdx.x; int m = blockIdx.y;
  if(n>=N) return;
  float acc=0.f;
  for(int k=0;k<K;++k) acc += bf2f(A[(size_t)m*K+k])*bf2f(B[(size_t)k*N+n]);
  if(relu) acc = fmaxf(acc,0.f);
  C[(size_t)m*N+n]=f2bf(acc);
}
__device__ __forceinline__ float phi(float t){ float r = t>0.f ? rb(t+1.f) : rb(__expf(t)); return rb(r+1e-6f); }
__global__ void kv_naive(const u16* qkv, float* kv, float* z){
  int h=blockIdx.x; int t=threadIdx.x; if(t>=420) return;
  int d=t/21, e=t%21; float acc=0.f;
  for(int s=0;s<2048;++s){ float pk=phi(bf2f(qkv[(size_t)s*1920+640+h*20+d])); float v = e<20? bf2f(qkv[(size_t)s*1920+1280+h*20+e]) : 1.f; acc+=pk*v; }
  if(e<20) kv[(h*20+d)*20+e]=rb(acc); else z[h*20+d]=rb(acc);
}
__global__ void o_naive(const u16* qkv, const float* kv, const float* z, u16* o){
  int s=blockIdx.x; int c=threadIdx.x; int h=c/20, e=c%20;
  float num=0.f, den=0.f;
  for(int d=0;d<20;++d){ float pq=phi(bf2f(qkv[(size_t)s*1920+h*20+d])); num+=pq*kv[(h*20+d)*20+e]; den+=pq*z[h*20+d]; }
  num=rb(num); den=rb(rb(den)+1e-6f);
  o[(size_t)s*640+c]=f2bf(num/den);
}
__global__ void final_naive(const u16* x, const u16* gw, const u16* a, const u16* b, u16* out){
  int s=blockIdx.x; int c=threadIdx.x;
  __shared__ float red[640];
  red[c]=rb(bf2f(x[(size_t)s*640+c])*bf2f(gw[c]));
  __syncthreads();
  float sum=0.f; for(int i=0;i<640;++i) sum+=red[i];
  sum=rb(sum);
  float g=rb(1.f/(1.f+__expf(-sum)));
  float r = rb(g*bf2f(a[(size_t)s*640+c])) + rb(rb(1.f-g)*bf2f(b[(size_t)s*640+c]));
  out[(size_t)s*640+c]=f2bf(r);
}
extern "C" void kernel_launch(void* const* d_in, const int* in_sizes, int n_in, void* d_out, int out_size, void* d_ws, size_t ws_size, hipStream_t stream){
  const u16* x=(const u16*)d_in[0]; const u16* wqkv=(const u16*)d_in[1]; const u16* wo=(const u16*)d_in[2];
  const u16* w1=(const u16*)d_in[4]; const u16* w2=(const u16*)d_in[5]; const u16* gw=(const u16*)d_in[6];
  char* ws=(char*)d_ws;
  u16* qkv=(u16*)ws; u16* h=(u16*)(ws+(8<<20)); u16* o=(u16*)(ws+(16<<20)); u16* a=(u16*)(ws+(20<<20)); u16* b=(u16*)(ws+(24<<20));
  float* kv=(float*)(ws+(28<<20)); float* z=(float*)(ws+(29<<20));
  gemm_naive<<<dim3(1920/128,2048),128,0,stream>>>(x,wqkv,qkv,2048,1920,640,0);
  gemm_naive<<<dim3(1536/128,2048),128,0,stream>>>(x,w1,h,2048,1536,640,1);
  kv_naive<<<32,448,0,stream>>>(qkv,kv,z);
  o_naive<<<2048,640,0,stream>>>(qkv,kv,z,o);
  gemm_naive<<<dim3(640/128,2048),128,0,stream>>>(o,wo,a,2048,640,640,0);
  gemm_naive<<<dim3(640/128,2048),128,0,stream>>>(h,w2,b,2048,640,1536,0);
  final_naive<<<2048,640,0,stream>>>(x,gw,a,b,(u16*)d_out);
}
```

```cpp
#include <hip/hip_runtime.h>

__global__ __launch_bounds__(256, 1) void gated_mix_fused(const void* x, unsigned nx, unsigned fx, const void* wqkv, unsigned nq,
                                                          unsigned fq, const void* w1, unsigned n1, unsigned f1, void* ws,
                                                          unsigned nw, unsigned fw, void* out, unsigned no, unsigned fo,
                                                          const void* gw, unsigned ng, unsigned fg, const void* wo,
                                                          const void* w2) {
  __shared__ __attribute__((aligned(16))) char smem[163840];
  unsigned tid = threadIdx.x;
  unsigned lds_base = (unsigned)(size_t)smem;
  asm volatile(
    "L_start_%=:\n"
    "s_getpc_b64 s[4:5]\n"
    "s_load_dwordx16 s[24:39], s[0:1], 0x0\n"
    "s_lshr_b32 s52, s2, 3\n"
    "s_lshl_b32 s6, s52, 12\n"
    "v_lshl_add_u32 v2, v0, 4, s6\n"
    "v_min_u32 v2, L_end_%=-L_start_%=-32, v2\n"
    "global_load_dwordx4 a[200:203], v2, s[4:5]\n"
    "v_lshrrev_b32 v2, 6, v0\n"
    "v_and_b32 v0, 63, v0\n"
    "v_readfirstlane_b32 s3, v2\n"
    "v_lshlrev_b32 v1, 4, v0\n"
    "s_bfe_u32 s6, s2, 0x10002\n"
    "s_lshr_b32 s7, s52, 2\n"
    "s_lshl3_add_u32 s4, s6, s7\n"
    "s_and_b32 s53, s7, 7\n"
    "s_mul_i32 s53, s53, 10\n"
    "s_lshr_b32 s53, s53, 3\n"
    "s_add_u32 s53, s53, s6\n"
    "s_and_b32 s6, s2, 3\n"
    "s_mul_i32 s7, s6, 5\n"
    "s_lshr_b32 s7, s7, 1\n"
    "s_add_u32 s53, s53, s7\n"
    "s_sub_u32 s7, s53, 10\n"
    "s_cselect_b32 s53, s53, s7\n"
    "s_and_b32 s5, s52, 1\n"
    "s_lshl1_add_u32 s5, s6, s5\n"
    "s_lshr_b32 s56, s3, 1\n"
    "s_and_b32 s57, s3, 1\n"
    "s_lshl1_add_u32 s62, s53, s3\n"
    "v_lshrrev_b32 v38, 3, v0\n"
    "v_and_b32_e64 v39, 7, v0\n"
    "v_lshrrev_b32 v40, 4, v0\n"
    "s_lshl2_add_u32 s6, s4, s3\n"
    "s_mul_i32 s6, s6, 40960\n"
    "v_xor_b32_e64 v41, v39, v40\n"
    "v_lshl_add_u32 v41, v41, 4, s6\n"
    "s_movk_i32 s7, 1280\n"
    "v_mad_u32_u24 v16, v38, s7, v41\n"
    "v_xor_b32 v17, 64, v16\n"
    "v_add_u32 v17, 9216, v17\n"
    "s_lshl_b32 s58, s3, 12\n"
    "v_and_b32 v40, 1, v40\n"
    "s_lshl_b32 s6, s57, 1\n"
    "v_or_b32 v40, s6, v40\n"
    "v_lshrrev_b32 v42, 1, v39\n"
    "v_xor_b32 v42, v42, v40\n"
    "v_and_b32 v43, 1, v0\n"
    "v_lshlrev_b32 v43, 4, v43\n"
    "v_lshl_add_u32 v43, v42, 5, v43\n"
    "s_lshl_b32 s6, s3, 3\n"
    "v_add_u32 v38, s6, v38\n"
    "s_bitcmp1_b32 s52, 1\n"
    "s_cbranch_scc0 L_kv0_%=\n"
    "s_movk_i32 s6, 3072\n"
    "v_mad_u32_u24 v43, v38, s6, v43\n"
    "s_mul_i32 s17, s5, 3\n"
    "s_and_b32 s6, s5, 1\n"
    "s_sub_u32 s17, s17, s6\n"
    "s_lshl_b32 s6, s17, 7\n"
    "s_add_u32 s6, s6, 2048\n"
    "v_add_u32 v18, s6, v43\n"
    "v_add_u32 v19, -896, v18\n"
    "s_mul_i32 s59, s3, 10240\n"
    "s_add_u32 s59, s59, 49152\n"
    "s_lshl_b32 s60, s53, 7\n"
    "s_add_u32 m0, s58, 0\n"
    "s_waitcnt lgkmcnt(0)\n"
    "s_nop 0\n"
    "buffer_load_dwordx4 v16, s[24:27], s60 offen lds\n"
    "buffer_load_dwordx4 v17, s[24:27], s60 offen offset:1024 lds\n"
    "s_sub_u32 s32, s32, 2048\n"
    "s_subb_u32 s33, s33, 0\n"
    "s_add_u32 m0, s59, 0\n"
    "s_mul_i32 s61, s53, 196608\n"
    "buffer_load_dwordx4 v18, s[32:35], s61 offen lds\n"
    "buffer_load_dwordx4 v19, s[32:35], s61 offen offset:1024 lds\n"
    "s_add_u32 s60, s60, 20480\n"
    "s_add_u32 m0, s58, 2048\n"
    "s_nop 0\n"
    "s_nop 0\n"
    "buffer_load_dwordx4 v16, s[24:27], s60 offen lds\n"
    "buffer_load_dwordx4 v17, s[24:27], s60 offen offset:1024 lds\n"
    "s_add_u32 m0, s59, 2048\n"
    "s_add_u32 s61, s61, 98304\n"
    "buffer_load_dwordx4 v18, s[32:35], s61 offen lds\n"
    "buffer_load_dwordx4 v19, s[32:35], s61 offen offset:1024 lds\n"
    "s_load_dwordx8 s[40:47], s[0:1], 0x40\n"
    "s_load_dwordx4 s[12:15], s[0:1], 0x60\n"
    "s_branch L_q_%=\n"
    "L_kv0_%=:\n"
    "s_movk_i32 s6, 3840\n"
    "v_mad_u32_u24 v43, v38, s6, v43\n"
    "s_mul_i32 s6, s5, 160\n"
    "s_add_u32 s6, s6, 3328\n"
    "v_add_u32_e64 v18, s6, v43\n"
    "v_add_u32 v19, -896, v18\n"
    "v_cmp_lt_u32_e64 vcc, 0, v42\n"
    "v_add_u32 v44, 1120, v19\n"
    "v_add_u32 v20, -672, v18\n"
    "v_cndmask_b32 v19, v19, v44, vcc\n"
    "v_cmp_gt_u32 vcc, 2, v42\n"
    "v_mov_b32 v44, 2147483648\n"
    "s_mul_i32 s59, s3, 12288\n"
    "v_cndmask_b32_e64 v20, v44, v20, vcc\n"
    "s_add_u32 s59, s59, 49152\n"
    "s_lshl_b32 s60, s53, 7\n"
    "s_add_u32 m0, s58, 0\n"
    "s_waitcnt lgkmcnt(0)\n"
    "s_nop 0\n"
    "buffer_load_dwordx4 v16, s[24:27], s60 offen lds\n"
    "buffer_load_dwordx4 v17, s[24:27], s60 offen offset:1024 lds\n"
    "s_sub_u32 s28, s28, 2048\n"
    "s_subb_u32 s29, s29, 0\n"
    "s_add_u32 m0, s59, 0\n"
    "s_mul_i32 s61, s53, 245760\n"
    "buffer_load_dwordx4 v18, s[28:31], s61 offen lds\n"
    "buffer_load_dwordx4 v19, s[28:31], s61 offen offset:1024 lds\n"
    "buffer_load_dwordx4 v20, s[28:31], s61 offen offset:2048 lds\n"
    "s_add_u32 s60, s60, 20480\n"
    "s_add_u32 m0, s58, 2048\n"
    "s_nop 0\n"
    "s_nop 0\n"
    "buffer_load_dwordx4 v16, s[24:27], s60 offen lds\n"
    "buffer_load_dwordx4 v17, s[24:27], s60 offen offset:1024 lds\n"
    "s_load_dwordx8 s[40:47], s[0:1], 0x40\n"
    "s_load_dwordx4 s[12:15], s[0:1], 0x60\n"
    "s_lshl3_add_u32 s6, s4, s5\n"
    "s_lshl1_add_u32 s6, s6, 0\n"
    "s_lshl2_add_u32 s66, s6, s3\n"
    "s_mul_i32 s7, s66, 2560\n"
    "v_and_b32 v46, 31, v0\n"
    "v_lshlrev_b32_e64 v46, 3, v46\n"
    "v_lshrrev_b32 v47, 5, v0\n"
    "s_movk_i32 s6, 1280\n"
    "v_mad_u32_u24 v47, v47, s6, v46\n"
    "buffer_load_dwordx2 v[76:77], v47, s[24:27], s7 offen offset:0\n"
    "buffer_load_dwordx2 v[78:79], v47, s[24:27], s7 offen offset:256\n"
    "buffer_load_dwordx2 v[80:81], v47, s[24:27], s7 offen offset:512\n"
    "buffer_load_dwordx2 v[82:83], v47, s[24:27], s7 offen offset:768\n"
    "buffer_load_dwordx2 v[84:85], v47, s[24:27], s7 offen offset:1024\n"
    "s_waitcnt lgkmcnt(0)\n"
    "s_nop 0\n"
    "buffer_load_dwordx2 v[86:87], v46, s[44:47], 0 offen offset:0\n"
    "buffer_load_dwordx2 v[88:89], v46, s[44:47], 0 offen offset:256\n"
    "buffer_load_dwordx2 v[90:91], v46, s[44:47], 0 offen offset:512\n"
    "buffer_load_dwordx2 v[92:93], v46, s[44:47], 0 offen offset:768\n"
    "buffer_load_dwordx2 v[94:95], v46, s[44:47], 0 offen offset:1024\n"
    "s_load_dword s63, s[36:37], 0x0\n"
    "v_and_b32 v38, 15, v0\n"
    "v_lshrrev_b32 v39, 4, v0\n"
    "v_lshrrev_b32 v40, 1, v38\n"
    "v_xor_b32 v40, v40, v39\n"
    "v_lshlrev_b32_e64 v40, 4, v40\n"
    "v_lshl_add_u32 v40, v38, 7, v40\n"
    "v_add_u32 v21, s58, v40\n"
    "v_xor_b32 v22, 64, v21\n"
    "v_xor_b32 v23, 0x1000, v21\n"
    "v_xor_b32 v24, 0x1040, v21\n"
    "v_bfe_u32 v40, v0, 2, 2\n"
    "v_bfe_u32 v41, v0, 3, 2\n"
    "v_and_b32 v38, 3, v0\n"
    "v_lshlrev_b32 v38, 3, v38\n"
    "v_lshl_add_u32 v38, v41, 5, v38\n"
    "v_lshl_add_u32 v38, v40, 7, v38\n"
    "s_movk_i32 s6, 12288\n"
    "v_mad_u32_u24 v38, v39, s6, v38\n"
    "v_add_u32 v38, 49152, v38\n"
    "s_lshl_b32 s6, s57, 5\n"
    "v_xor_b32 v25, s6, v38\n"
    "v_xor_b32 v26, 64, v25\n"
    "s_mov_b32 s70, 0x358637bd\n"
    "s_cmp_eq_u32 s57, 0\n"
    "s_cselect_b32 s6, s70, 0\n"
    "v_mov_b32 v152, s70\n"
    "v_mov_b32 v153, s70\n"
    "v_mov_b32 v154, s70\n"
    "v_mov_b32 v155, s70\n"
    "v_mov_b32 v156, s6\n"
    "v_mov_b32 v157, s6\n"
    "v_mov_b32 v158, s6\n"
    "v_mov_b32 v159, s6\n"
    "s_add_u32 m0, s59, 3072\n"
    "s_add_u32 s61, s61, 122880\n"
    "buffer_load_dwordx4 v18, s[28:31], s61 offen lds\n"
    "buffer_load_dwordx4 v19, s[28:31], s61 offen offset:1024 lds\n"
    "buffer_load_dwordx4 v20, s[28:31], s61 offen offset:2048 lds\n"
    "s_waitcnt vmcnt(13)\n"
    "s_barrier\n"
    "ds_read_b128 a[0:3], v21 offset:0\n"
    "ds_read_b64_tr_b16 v[104:105], v25 offset:0\n"
    "ds_read_b64_tr_b16 v[106:107], v25 offset:512\n"
    "ds_read_b128 a[4:7], v21 offset:2048\n"
    "ds_read_b128 v[96:99], v23 offset:0\n"
    "ds_read_b128 v[100:103], v23 offset:2048\n"
    "ds_read_b64_tr_b16 v[108:109], v26 offset:0\n"
    "ds_read_b64_tr_b16 v[110:111], v26 offset:512\n"
    "ds_read_b64_tr_b16 v[112:113], v25 offset:1024\n"
    "ds_read_b64_tr_b16 v[114:115], v25 offset:1536\n"
    "ds_read_b64_tr_b16 v[116:117], v26 offset:1024\n"
    "ds_read_b64_tr_b16 v[118:119], v26 offset:1536\n"
    "ds_read_b64_tr_b16 v[120:121], v25 offset:2048\n"
    "ds_read_b64_tr_b16 v[122:123], v25 offset:2560\n"
    "s_add_u32 s53, s53, 1\n"
    "s_cmp_eq_u32 s53, 10\n"
    "s_cselect_b32 s53, 0, s53\n"
    "s_lshl_b32 s60, s53, 7\n"
    "s_add_u32 m0, s58, 16384\n"
    "s_nop 0\n"
    "s_nop 0\n"
    "buffer_load_dwordx4 v16, s[24:27], s60 offen lds\n"
    "buffer_load_dwordx4 v17, s[24:27], s60 offen offset:1024 lds\n"
    "s_add_u32 m0, s59, 6144\n"
    "s_mul_i32 s61, s53, 245760\n"
    "buffer_load_dwordx4 v18, s[28:31], s61 offen lds\n"
    "buffer_load_dwordx4 v19, s[28:31], s61 offen offset:1024 lds\n"
    "buffer_load_dwordx4 v20, s[28:31], s61 offen offset:2048 lds\n"
    "s_waitcnt lgkmcnt(0)\n"
    "v_mfma_f32_16x16x32_bf16 v[160:163], v[104:107], a[0:3], v[152:155]\n"
    "s_waitcnt vmcnt(5)\n"
    "s_barrier\n"
    "v_mfma_f32_16x16x32_bf16 v[164:167], v[104:107], a[4:7], v[152:155]\n"
    "ds_read_b128 a[8:11], v22 offset:0\n"
    "s_add_u32 s60, s60, 20480\n"
    "v_mfma_f32_16x16x32_bf16 v[168:171], v[104:107], v[96:99], v[152:155]\n"
    "s_add_u32 m0, s58, 18432\n"
    "v_mfma_f32_16x16x32_bf16 v[172:175], v[104:107], v[100:103], v[152:155]\n"
    "buffer_load_dwordx4 v16, s[24:27], s60 offen lds\n"
    "v_mfma_f32_16x16x32_bf16 v[188:191], v[108:111], v[100:103], v[152:155]\n"
    "buffer_load_dwordx4 v17, s[24:27], s60 offen offset:1024 lds\n"
    "v_mfma_f32_16x16x32_bf16 v[184:187], v[108:111], v[96:99], v[152:155]\n"
    "ds_read_b64_tr_b16 v[132:133], v25 offset:3072\n"
    "s_add_u32 m0, s59, 9216\n"
    "v_mfma_f32_16x16x32_bf16 v[180:183], v[108:111], a[4:7], v[152:155]\n"
    "ds_read_b64_tr_b16 v[134:135], v25 offset:3584\n"
    "s_add_u32 s61, s61, 122880\n"
    "v_mfma_f32_16x16x32_bf16 v[176:179], v[108:111], a[0:3], v[152:155]\n"
    "ds_read_b128 a[12:15], v22 offset:2048\n"
    "buffer_load_dwordx4 v18, s[28:31], s61 offen lds\n"
    "v_mfma_f32_16x16x32_bf16 v[192:195], v[112:115], a[0:3], v[156:159]\n"
    "ds_read_b128 v[124:127], v24 offset:0\n"
    "buffer_load_dwordx4 v19, s[28:31], s61 offen offset:1024 lds\n"
    "v_mfma_f32_16x16x32_bf16 v[196:199], v[112:115], a[4:7], v[156:159]\n"
    "ds_read_b128 v[128:131], v24 offset:2048\n"
    "buffer_load_dwordx4 v20, s[28:31], s61 offen offset:2048 lds\n"
    "v_mfma_f32_16x16x32_bf16 v[200:203], v[112:115], v[96:99], v[156:159]\n"
    "ds_read_b64_tr_b16 v[136:137], v26 offset:3072\n"
    "s_add_u32 s53, s53, 1\n"
    "v_mfma_f32_16x16x32_bf16 v[204:207], v[112:115], v[100:103], v[156:159]\n"
    "ds_read_b64_tr_b16 v[138:139], v26 offset:3584\n"
    "s_cmp_eq_u32 s53, 10\n"
    "v_mfma_f32_16x16x32_bf16 v[220:223], v[116:119], v[100:103], 0\n"
    "ds_read_b64_tr_b16 v[140:141], v25 offset:4096\n"
    "s_cselect_b32 s53, 0, s53\n"
    "s_lshl_b32 s60, s53, 7\n"
    "v_mfma_f32_16x16x32_bf16 v[216:219], v[116:119], v[96:99], 0\n"
    "ds_read_b64_tr_b16 v[142:143], v25 offset:4608\n"
    "s_add_u32 m0, s58, 32768\n"
    "s_nop 0\n"
    "buffer_load_dwordx4 v16, s[24:27], s60 offen lds\n"
    "v_mfma_f32_16x16x32_bf16 v[212:215], v[116:119], a[4:7], 0\n"
    "ds_read_b64_tr_b16 v[144:145], v26 offset:4096\n"
    "buffer_load_dwordx4 v17, s[24:27], s60 offen offset:1024 lds\n"
    "v_mfma_f32_16x16x32_bf16 v[208:211], v[116:119], a[0:3], 0\n"
    "ds_read_b64_tr_b16 v[146:147], v26 offset:4608\n"
    "s_add_u32 m0, s59, 0\n"
    "v_mfma_f32_16x16x32_bf16 v[224:227], v[120:123], a[0:3], 0\n"
    "ds_read_b64_tr_b16 v[148:149], v25 offset:5120\n"
    "s_mul_i32 s61, s53, 245760\n"
    "v_mfma_f32_16x16x32_bf16 v[228:231], v[120:123], a[4:7], 0\n"
    "ds_read_b64_tr_b16 v[150:151], v25 offset:5632\n"
    "buffer_load_dwordx4 v18, s[28:31], s61 offen lds\n"
    "v_mfma_f32_16x16x32_bf16 v[232:235], v[120:123], v[96:99], 0\n"
    "buffer_load_dwordx4 v19, s[28:31], s61 offen offset:1024 lds\n"
    "v_mfma_f32_16x16x32_bf16 v[236:239], v[120:123], v[100:103], 0\n"
    "buffer_load_dwordx4 v20, s[28:31], s61 offen offset:2048 lds\n"
    "s_waitcnt lgkmcnt(0)\n"
    "v_mfma_f32_16x16x32_bf16 v[160:163], v[132:135], a[8:11], v[160:163]\n"
    "s_waitcnt vmcnt(8)\n"
    "s_barrier\n"
    "v_mfma_f32_16x16x32_bf16 v[164:167], v[132:135], a[12:15], v[164:167]\n"
    "ds_read_b128 a[16:19], v21 offset:16384\n"
    "s_add_u32 s60, s60, 20480\n"
    "v_mfma_f32_16x16x32_bf16 v[168:171], v[132:135], v[124:127], v[168:171]\n"
    "s_add_u32 m0, s58, 34816\n"
    "v_mfma_f32_16x16x32_bf16 v[172:175], v[132:135], v[128:131], v[172:175]\n"
    "buffer_load_dwordx4 v16, s[24:27], s60 offen lds\n"
    "v_mfma_f32_16x16x32_bf16 v[188:191], v[136:139], v[128:131], v[188:191]\n"
    "ds_read_b64_tr_b16 v[104:105], v25 offset:6144\n"
    "v_mfma_f32_16x16x32_bf16 v[184:187], v[136:139], v[124:127], v[184:187]\n"
    "ds_read_b64_tr_b16 v[106:107], v25 offset:6656\n"
    "v_mfma_f32_16x16x32_bf16 v[180:183], v[136:139], a[12:15], v[180:183]\n"
    "buffer_load_dwordx4 v17, s[24:27], s60 offen offset:1024 lds\n"
    "v_mfma_f32_16x16x32_bf16 v[176:179], v[136:139], a[8:11], v[176:179]\n"
    "ds_read_b128 a[20:23], v21 offset:18432\n"
    "v_mfma_f32_16x16x32_bf16 v[192:195], v[140:143], a[8:11], v[192:195]\n"
    "ds_read_b128 v[96:99], v23 offset:16384\n"
    "s_add_u32 m0, s59, 3072\n"
    "v_mfma_f32_16x16x32_bf16 v[196:199], v[140:143], a[12:15], v[196:199]\n"
    "ds_read_b128 v[100:103], v23 offset:18432\n"
    "s_add_u32 s61, s61, 122880\n"
    "v_mfma_f32_16x16x32_bf16 v[200:203], v[140:143], v[124:127], v[200:203]\n"
    "ds_read_b64_tr_b16 v[108:109], v26 offset:6144\n"
    "v_mfma_f32_16x16x32_bf16 v[204:207], v[140:143], v[128:131], v[204:207]\n"
    "ds_read_b64_tr_b16 v[110:111], v26 offset:6656\n"
    "v_mfma_f32_16x16x32_bf16 v[220:223], v[144:147], v[128:131], v[220:223]\n"
    "ds_read_b64_tr_b16 v[112:113], v25 offset:7168\n"
    "v_mfma_f32_16x16x32_bf16 v[216:219], v[144:147], v[124:127], v[216:219]\n"
    "ds_read_b64_tr_b16 v[114:115], v25 offset:7680\n"
    "v_mfma_f32_16x16x32_bf16 v[212:215], v[144:147], a[12:15], v[212:215]\n"
    "ds_read_b64_tr_b16 v[116:117], v26 offset:7168\n"
    "buffer_load_dwordx4 v18, s[28:31], s61 offen lds\n"
    "v_mfma_f32_16x16x32_bf16 v[208:211], v[144:147], a[8:11], v[208:211]\n"
    "ds_read_b64_tr_b16 v[118:119], v26 offset:7680\n"
    "v_mfma_f32_16x16x32_bf16 v[224:227], v[148:151], a[8:11], v[224:227]\n"
    "ds_read_b64_tr_b16 v[120:121], v25 offset:8192\n"
    "v_mfma_f32_16x16x32_bf16 v[228:231], v[148:151], a[12:15], v[228:231]\n"
    "ds_read_b64_tr_b16 v[122:123], v25 offset:8704\n"
    "buffer_load_dwordx4 v19, s[28:31], s61 offen offset:1024 lds\n"
    "v_mfma_f32_16x16x32_bf16 v[232:235], v[148:151], v[124:127], v[232:235]\n"
    "buffer_load_dwordx4 v20, s[28:31], s61 offen offset:2048 lds\n"
    "v_mfma_f32_16x16x32_bf16 v[236:239], v[148:151], v[128:131], v[236:239]\n"
    "s_waitcnt lgkmcnt(0)\n"
    "v_mfma_f32_16x16x32_bf16 v[160:163], v[104:107], a[16:19], v[160:163]\n"
    "s_waitcnt vmcnt(10)\n"
    "s_barrier\n"
    "v_mfma_f32_16x16x32_bf16 v[164:167], v[104:107], a[20:23], v[164:167]\n"
    "ds_read_b128 a[24:27], v22 offset:16384\n"
    "s_waitcnt vmcnt(23)\n"
    "v_mfma_f32_16x16x32_bf16 v[168:171], v[104:107], v[96:99], v[168:171]\n"
    "v_dot2_f32_bf16 v44, v76, v86, 0\n"
    "v_dot2_f32_bf16 v44, v77, v87, v44\n"
    "s_add_u32 s53, s53, 1\n"
    "v_mfma_f32_16x16x32_bf16 v[172:175], v[104:107], v[100:103], v[172:175]\n"
    "ds_read_b64_tr_b16 v[132:133], v25 offset:9216\n"
    "s_cmp_eq_u32 s53, 10\n"
    "s_cselect_b32 s53, 0, s53\n"
    "v_dot2_f32_bf16 v44, v78, v88, v44\n"
    "v_mfma_f32_16x16x32_bf16 v[188:191], v[108:111], v[100:103], v[188:191]\n"
    "s_nop 0\n"
    "ds_read_b64_tr_b16 v[134:135], v25 offset:9728\n"
    "v_dot2_f32_bf16 v44, v79, v89, v44\n"
    "v_dot2_f32_bf16 v44, v80, v90, v44\n"
    "s_lshl_b32 s60, s53, 7\n"
    "s_add_u32 m0, s58, 0\n"
    "v_mfma_f32_16x16x32_bf16 v[184:187], v[108:111], v[96:99], v[184:187]\n"
    "ds_read_b128 a[28:31], v22 offset:18432\n"
    "v_dot2_f32_bf16 v44, v81, v91, v44\n"
    "v_dot2_f32_bf16 v44, v82, v92, v44\n"
    "v_dot2_f32_bf16 v44, v83, v93, v44\n"
    "v_mfma_f32_16x16x32_bf16 v[180:183], v[108:111], a[20:23], v[180:183]\n"
    "ds_read_b128 v[124:127], v24 offset:16384\n"
    "v_dot2_f32_bf16 v44, v84, v94, v44\n"
    "buffer_load_dwordx4 v16, s[24:27], s60 offen lds\n"
    "v_mfma_f32_16x16x32_bf16 v[176:179], v[108:111], a[16:19], v[176:179]\n"
    "v_dot2_f32_bf16 v44, v85, v95, v44\n"
    "s_add_u32 s63, s63, 1\n"
    "v_mfma_f32_16x16x32_bf16 v[192:195], v[112:115], a[16:19], v[192:195]\n"
    "ds_read_b128 v[128:131], v24 offset:18432\n"
    "v_and_b32 v46, 31, v0\n"
    "v_lshrrev_b32 v47, 5, v0\n"
    "v_mov_b32 v248, s63\n"
    "buffer_load_dwordx4 v17, s[24:27], s60 offen offset:1024 lds\n"
    "v_mfma_f32_16x16x32_bf16 v[196:199], v[112:115], a[20:23], v[196:199]\n"
    "ds_read_b64_tr_b16 v[136:137], v26 offset:9216\n"
    "v_add_f32_dpp v44, v44, v44 row_shr:1 row_mask:0xf bank_mask:0xf\n"
    "v_mfma_f32_16x16x32_bf16 v[200:203], v[112:115], v[96:99], v[200:203]\n"
    "ds_read_b64_tr_b16 v[138:139], v26 offset:9728\n"
    "v_add_f32_dpp v44, v44, v44 row_shr:2 row_mask:0xf bank_mask:0xf\n"
    "s_nop 1\n"
    "v_add_f32_dpp v44, v44, v44 row_shr:4 row_mask:0xf bank_mask:0xf\n"
    "s_nop 1\n"
    "v_add_f32_dpp v44, v44, v44 row_shr:8 row_mask:0xf bank_mask:0xf\n"
    "v_mfma_f32_16x16x32_bf16 v[204:207], v[112:115], v[100:103], v[204:207]\n"
    "ds_read_b64_tr_b16 v[140:141], v25 offset:10240\n"
    "s_add_u32 m0, s59, 6144\n"
    "s_mul_i32 s61, s53, 245760\n"
    "v_add_f32_dpp v44, v44, v44 row_bcast:15 row_mask:0xa bank_mask:0xf\n"
    "v_mfma_f32_16x16x32_bf16 v[220:223], v[116:119], v[100:103], v[220:223]\n"
    "ds_read_b64_tr_b16 v[142:143], v25 offset:10752\n"
    "v_mul_f32 v44, 0xbfb8aa3b, v44\n"
    "v_exp_f32_e64 v44, v44\n"
    "buffer_load_dwordx4 v18, s[28:31], s61 offen lds\n"
    "v_mfma_f32_16x16x32_bf16 v[216:219], v[116:119], v[96:99], v[216:219]\n"
    "v_cmp_eq_u32 vcc, 31, v46\n"
    "v_lshlrev_b32 v47, 3, v47\n"
    "v_mfma_f32_16x16x32_bf16 v[212:215], v[116:119], a[20:23], v[212:215]\n"
    "ds_read_b64_tr_b16 v[144:145], v26 offset:10240\n"
    "v_mov_b32 v46, 2147483648\n"
    "v_cndmask_b32_e64 v47, v46, v47, vcc\n"
    "v_mfma_f32_16x16x32_bf16 v[208:211], v[116:119], a[16:19], v[208:211]\n"
    "ds_read_b64_tr_b16 v[146:147], v26 offset:10752\n"
    "v_add_f32 v44, 1.0, v44\n"
    "v_rcp_f32 v44, v44\n"
    "v_mfma_f32_16x16x32_bf16 v[224:227], v[120:123], a[16:19], v[224:227]\n"
    "ds_read_b64_tr_b16 v[148:149], v25 offset:11264\n"
    "v_mov_b32_e64 v45, s63\n"
    "buffer_load_dwordx4 v19, s[28:31], s61 offen offset:1024 lds\n"
    "v_mfma_f32_16x16x32_bf16 v[228:231], v[120:123], a[20:23], v[228:231]\n"
    "ds_read_b64_tr_b16 v[150:151], v25 offset:11776\n"
    "s_lshl4_add_u32 s65, s66, 16384\n"
    "s_lshl4_add_u32 s89, s5, s4\n"
    "s_lshl2_add_u32 s89, s89, s3\n"
    "v_mfma_f32_16x16x32_bf16 v[232:235], v[120:123], v[96:99], v[232:235]\n"
    "s_lshl_b32 s89, s89, 11\n"
    "s_add_u32 s89, s89, 655360\n"
    "buffer_load_dwordx4 v20, s[28:31], s61 offen offset:2048 lds\n"
    "v_mfma_f32_16x16x32_bf16 v[236:239], v[120:123], v[100:103], v[236:239]\n"
    "v_mov_b32 v76, -1\n"
    "v_mov_b32 v77, -1\n"
    "v_mov_b32 v78, -1\n"
    "v_mov_b32_e64 v79, -1\n"
    "buffer_store_dwordx2 v[44:45], v47, s[36:39], s65 offen sc1\n"
    "buffer_store_dwordx4 v[76:79], v1, s[36:39], s89 offen offset:0 sc1\n"
    "buffer_store_dwordx4 v[76:79], v1, s[36:39], s89 offen offset:1024 sc1\n"
    "s_waitcnt lgkmcnt(0)\n"
    "v_mfma_f32_16x16x32_bf16 v[160:163], v[132:135], a[24:27], v[160:163]\n"
    "s_waitcnt vmcnt(11)\n"
    "s_barrier\n"
    "v_mfma_f32_16x16x32_bf16 v[164:167], v[132:135], a[28:31], v[164:167]\n"
    "ds_read_b128 a[32:35], v21 offset:32768\n"
    "s_add_u32 s60, s60, 20480\n"
    "v_mfma_f32_16x16x32_bf16 v[168:171], v[132:135], v[124:127], v[168:171]\n"
    "s_sub_u32 s6, s62, 20\n"
    "s_cselect_b32 s62, s62, s6\n"
    "s_add_u32 m0, s58, 2048\n"
    "v_mfma_f32_16x16x32_bf16 v[172:175], v[132:135], v[128:131], v[172:175]\n"
    "ds_read_b64_tr_b16 v[104:105], v25 offset:0\n"
    "s_mul_i32 s71, s3, 4096\n"
    "v_mfma_f32_16x16x32_bf16 v[188:191], v[136:139], v[128:131], v[188:191]\n"
    "buffer_load_dwordx4 v16, s[24:27], s60 offen lds\n"
    "v_mfma_f32_16x16x32_bf16 v[184:187], v[136:139], v[124:127], v[184:187]\n"
    "ds_read_b64_tr_b16 v[106:107], v25 offset:512\n"
    "v_lshrrev_b32 v38, 3, v0\n"
    "v_mfma_f32_16x16x32_bf16 v[180:183], v[136:139], a[28:31], v[180:183]\n"
    "ds_read_b128 a[36:39], v21 offset:34816\n"
    "v_bfe_u32 v39, v0, 1, 2\n"
    "v_mfma_f32_16x16x32_bf16 v[176:179], v[136:139], a[24:27], v[176:179]\n"
    "buffer_load_dwordx4 v17, s[24:27], s60 offen offset:1024 lds\n"
    "v_mfma_f32_16x16x32_bf16 v[192:195], v[140:143], a[24:27], v[192:195]\n"
    "ds_read_b128 v[96:99], v23 offset:32768\n"
    "v_bfe_u32 v40, v0, 4, 1\n"
    "v_mfma_f32_16x16x32_bf16 v[196:199], v[140:143], a[28:31], v[196:199]\n"
    "ds_read_b128 v[100:103], v23 offset:34816\n"
    "v_xor_b32_e64 v39, v39, v40\n"
    "v_mfma_f32_16x16x32_bf16 v[200:203], v[140:143], v[124:127], v[200:203]\n"
    "ds_read_b64_tr_b16 v[108:109], v26 offset:0\n"
    "s_add_u32 m0, s59, 9216\n"
    "v_mfma_f32_16x16x32_bf16 v[204:207], v[140:143], v[128:131], v[204:207]\n"
    "ds_read_b64_tr_b16 v[110:111], v26 offset:512\n"
    "s_add_u32 s61, s61, 122880\n"
    "v_mfma_f32_16x16x32_bf16 v[220:223], v[144:147], v[128:131], v[220:223]\n"
    "ds_read_b64_tr_b16 v[112:113], v25 offset:1024\n"
    "v_and_b32_e64 v40, 1, v0\n"
    "v_mfma_f32_16x16x32_bf16 v[216:219], v[144:147], v[124:127], v[216:219]\n"
    "ds_read_b64_tr_b16 v[114:115], v25 offset:1536\n"
    "v_lshlrev_b32_e64 v40, 4, v40\n"
    "buffer_load_dwordx4 v18, s[28:31], s61 offen lds\n"
    "v_mfma_f32_16x16x32_bf16 v[212:215], v[144:147], a[28:31], v[212:215]\n"
    "ds_read_b64_tr_b16 v[116:117], v26 offset:1024\n"
    "v_lshl_add_u32 v39, v39, 5, v40\n"
    "v_mfma_f32_16x16x32_bf16 v[208:211], v[144:147], a[24:27], v[208:211]\n"
    "ds_read_b64_tr_b16 v[118:119], v26 offset:1536\n"
    "v_mul_u32_u24 v38, 0xc00, v38\n"
    "v_mfma_f32_16x16x32_bf16 v[224:227], v[148:151], a[24:27], v[224:227]\n"
    "ds_read_b64_tr_b16 v[120:121], v25 offset:2048\n"
    "buffer_load_dwordx4 v19, s[28:31], s61 offen offset:1024 lds\n"
    "v_mfma_f32_16x16x32_bf16 v[228:231], v[148:151], a[28:31], v[228:231]\n"
    "ds_read_b64_tr_b16 v[122:123], v25 offset:2560\n"
    "s_lshr_b32 s6, s5, 1\n"
    "v_mfma_f32_16x16x32_bf16 v[232:235], v[148:151], v[124:127], v[232:235]\n"
    "s_lshl2_add_u32 s17, s6, s5\n"
    "v_mfma_f32_16x16x32_bf16 v[236:239], v[148:151], v[128:131], v[236:239]\n"
    "buffer_load_dwordx4 v20, s[28:31], s61 offen offset:2048 lds\n"
    "s_add_u32 s17, s17, 4\n"
    "s_waitcnt lgkmcnt(0)\n"
    "v_mfma_f32_16x16x32_bf16 v[160:163], v[104:107], a[32:35], v[160:163]\n"
    "s_waitcnt vmcnt(13)\n"
    "s_barrier\n"
    "v_mfma_f32_16x16x32_bf16 v[164:167], v[104:107], a[36:39], v[164:167]\n"
    "ds_read_b128 a[40:43], v22 offset:32768\n"
    "s_lshl_b32 s6, s17, 7\n"
    "v_mfma_f32_16x16x32_bf16 v[168:171], v[104:107], v[96:99], v[168:171]\n"
    "v_add_u32 v38, v38, v39\n"
    "s_add_u32 s53, s53, 1\n"
    "v_mfma_f32_16x16x32_bf16 v[172:175], v[104:107], v[100:103], v[172:175]\n"
    "ds_read_b64_tr_b16 v[132:133], v25 offset:3072\n"
    "s_cmp_eq_u32 s53, 10\n"
    "v_mfma_f32_16x16x32_bf16 v[188:191], v[108:111], v[100:103], v[188:191]\n"
    "ds_read_b64_tr_b16 v[134:135], v25 offset:3584\n"
    "s_cselect_b32 s53, 0, s53\n"
    "v_mfma_f32_16x16x32_bf16 v[184:187], v[108:111], v[96:99], v[184:187]\n"
    "ds_read_b128 a[44:47], v22 offset:34816\n"
    "v_add_u32 v29, s6, v38\n"
    "s_lshl_b32 s60, s53, 7\n"
    "v_mfma_f32_16x16x32_bf16 v[180:183], v[108:111], a[36:39], v[180:183]\n"
    "ds_read_b128 v[124:127], v24 offset:32768\n"
    "s_add_u32 m0, s58, 16384\n"
    "v_xor_b32 v30, 64, v29\n"
    "v_mfma_f32_16x16x32_bf16 v[176:179], v[108:111], a[32:35], v[176:179]\n"
    "buffer_load_dwordx4 v16, s[24:27], s60 offen lds\n"
    "v_mfma_f32_16x16x32_bf16 v[192:195], v[112:115], a[32:35], v[192:195]\n"
    "ds_read_b128 v[128:131], v24 offset:34816\n"
    "v_add_u32 v30, 23552, v30\n"
    "v_mfma_f32_16x16x32_bf16 v[196:199], v[112:115], a[36:39], v[196:199]\n"
    "buffer_load_dwordx4 v17, s[24:27], s60 offen offset:1024 lds\n"
    "v_mfma_f32_16x16x32_bf16 v[200:203], v[112:115], v[96:99], v[200:203]\n"
    "ds_read_b64_tr_b16 v[136:137], v26 offset:3072\n"
    "v_add_u32 v31, 47104, v29\n"
    "v_mfma_f32_16x16x32_bf16 v[204:207], v[112:115], v[100:103], v[204:207]\n"
    "ds_read_b64_tr_b16 v[138:139], v26 offset:3584\n"
    "s_add_u32 m0, s59, 0\n"
    "v_mfma_f32_16x16x32_bf16 v[220:223], v[116:119], v[100:103], v[220:223]\n"
    "ds_read_b64_tr_b16 v[140:141], v25 offset:4096\n"
    "s_mul_i32 s61, s53, 245760\n"
    "v_add_u32 v32, 47104, v30\n"
    "v_mfma_f32_16x16x32_bf16 v[216:219], v[116:119], v[96:99], v[216:219]\n"
    "ds_read_b64_tr_b16 v[142:143], v25 offset:4608\n"
    "buffer_load_dwordx4 v18, s[28:31], s61 offen lds\n"
    "v_mfma_f32_16x16x32_bf16 v[212:215], v[116:119], a[36:39], v[212:215]\n"
    "ds_read_b64_tr_b16 v[144:145], v26 offset:4096\n"
    "v_lshrrev_b32 v38, 4, v0\n"
    "v_mfma_f32_16x16x32_bf16 v[208:211], v[116:119], a[32:35], v[208:211]\n"
    "ds_read_b64_tr_b16 v[146:147], v26 offset:4608\n"
    "v_bfe_u32 v39, v0, 2, 2\n"
    "v_mfma_f32_16x16x32_bf16 v[224:227], v[120:123], a[32:35], v[224:227]\n"
    "ds_read_b64_tr_b16 v[148:149], v25 offset:5120\n"
    "buffer_load_dwordx4 v19, s[28:31], s61 offen offset:1024 lds\n"
    "v_mfma_f32_16x16x32_bf16 v[228:231], v[120:123], a[36:39], v[228:231]\n"
    "ds_read_b64_tr_b16 v[150:151], v25 offset:5632\n"
    "v_bfe_u32 v40, v0, 3, 2\n"
    "v_mfma_f32_16x16x32_bf16 v[232:235], v[120:123], v[96:99], v[232:235]\n"
    "v_and_b32_e64 v41, 3, v0\n"
    "buffer_load_dwordx4 v20, s[28:31], s61 offen offset:2048 lds\n"
    "v_mfma_f32_16x16x32_bf16 v[236:239], v[120:123], v[100:103], v[236:239]\n"
    "v_lshlrev_b32_e64 v41, 3, v41\n"
    "v_lshl_add_u32 v41, v40, 5, v41\n"
    "s_waitcnt lgkmcnt(0)\n"
    "v_mfma_f32_16x16x32_bf16 v[160:163], v[132:135], a[40:43], v[160:163]\n"
    "s_waitcnt vmcnt(8)\n"
    "s_barrier\n"
    "v_mfma_f32_16x16x32_bf16 v[164:167], v[132:135], a[44:47], v[164:167]\n"
    "ds_read_b128 a[48:51], v21 offset:0\n"
    "v_lshl_add_u32 v41, v39, 7, v41\n"
    "v_mfma_f32_16x16x32_bf16 v[168:171], v[132:135], v[124:127], v[168:171]\n"
    "v_lshl_add_u32 v41, v38, 10, v41\n"
    "s_add_u32 s60, s60, 20480\n"
    "v_mfma_f32_16x16x32_bf16 v[172:175], v[132:135], v[128:131], v[172:175]\n"
    "ds_read_b64_tr_b16 v[104:105], v25 offset:6144\n"
    "v_add_u32 v34, 98304, v41\n"
    "v_mfma_f32_16x16x32_bf16 v[188:191], v[136:139], v[128:131], v[188:191]\n"
    "ds_read_b64_tr_b16 v[106:107], v25 offset:6656\n"
    "s_add_u32 m0, s58, 18432\n"
    "v_mfma_f32_16x16x32_bf16 v[184:187], v[136:139], v[124:127], v[184:187]\n"
    "ds_read_b128 a[52:55], v21 offset:2048\n"
    "v_xor_b32 v35, 32, v34\n"
    "v_mfma_f32_16x16x32_bf16 v[180:183], v[136:139], a[44:47], v[180:183]\n"
    "ds_read_b128 v[96:99], v23 offset:0\n"
    "v_xor_b32_e64 v36, 64, v34\n"
    "buffer_load_dwordx4 v16, s[24:27], s60 offen lds\n"
    "v_mfma_f32_16x16x32_bf16 v[176:179], v[136:139], a[40:43], v[176:179]\n"
    "v_xor_b32 v37, 0x60, v34\n"
    "v_mfma_f32_16x16x32_bf16 v[192:195], v[140:143], a[40:43], v[192:195]\n"
    "buffer_load_dwordx4 v17, s[24:27], s60 offen offset:1024 lds\n"
    "v_mfma_f32_16x16x32_bf16 v[196:199], v[140:143], a[44:47], v[196:199]\n"
    "ds_read_b128 v[100:103], v23 offset:2048\n"
    "s_lshl1_add_u32 s6, s4, s56\n"
    "v_mfma_f32_16x16x32_bf16 v[200:203], v[140:143], v[124:127], v[200:203]\n"
    "ds_read_b64_tr_b16 v[108:109], v26 offset:6144\n"
    "s_mul_i32 s6, s6, 24\n"
    "v_mfma_f32_16x16x32_bf16 v[204:207], v[140:143], v[128:131], v[204:207]\n"
    "ds_read_b64_tr_b16 v[110:111], v26 offset:6656\n"
    "s_add_u32 m0, s59, 3072\n"
    "v_mfma_f32_16x16x32_bf16 v[220:223], v[144:147], v[128:131], v[220:223]\n"
    "ds_read_b64_tr_b16 v[112:113], v25 offset:7168\n"
    "s_add_u32 s61, s61, 122880\n"
    "v_mfma_f32_16x16x32_bf16 v[216:219], v[144:147], v[124:127], v[216:219]\n"
    "ds_read_b64_tr_b16 v[114:115], v25 offset:7680\n"
    "s_add_u32 s17, s17, s6\n"
    "buffer_load_dwordx4 v18, s[28:31], s61 offen lds\n"
    "v_mfma_f32_16x16x32_bf16 v[212:215], v[144:147], a[44:47], v[212:215]\n"
    "ds_read_b64_tr_b16 v[116:117], v26 offset:7168\n"
    "s_lshl2_add_u32 s17, s17, s57\n"
    "v_mfma_f32_16x16x32_bf16 v[208:211], v[144:147], a[40:43], v[208:211]\n"
    "ds_read_b64_tr_b16 v[118:119], v26 offset:7680\n"
    "s_lshl_b32 s17, s17, 11\n"
    "v_mfma_f32_16x16x32_bf16 v[224:227], v[148:151], a[40:43], v[224:227]\n"
    "ds_read_b64_tr_b16 v[120:121], v25 offset:8192\n"
    "buffer_load_dwordx4 v19, s[28:31], s61 offen offset:1024 lds\n"
    "v_mfma_f32_16x16x32_bf16 v[228:231], v[148:151], a[44:47], v[228:231]\n"
    "ds_read_b64_tr_b16 v[122:123], v25 offset:8704\n"
    "s_xor_b32 s45, s17, 0x800\n"
    "v_mfma_f32_16x16x32_bf16 v[232:235], v[148:151], v[124:127], v[232:235]\n"
    "s_add_u32 s17, s17, 7110656\n"
    "v_mfma_f32_16x16x32_bf16 v[236:239], v[148:151], v[128:131], v[236:239]\n"
    "buffer_load_dwordx4 v20, s[28:31], s61 offen offset:2048 lds\n"
    "s_add_u32 s45, s45, 7110656\n"
    "s_waitcnt lgkmcnt(0)\n"
    "v_mfma_f32_16x16x32_bf16 v[160:163], v[104:107], a[48:51], v[160:163]\n"
    "s_waitcnt vmcnt(10)\n"
    "s_barrier\n"
    "v_mfma_f32_16x16x32_bf16 v[164:167], v[104:107], a[52:55], v[164:167]\n"
    "ds_read_b128 a[56:59], v22 offset:0\n"
    "s_mul_i32 s67, s2, 16\n"
    "v_mfma_f32_16x16x32_bf16 v[168:171], v[104:107], v[96:99], v[168:171]\n"
    "s_add_u32 s67, s67, 65536\n"
    "s_add_u32 s53, s53, 1\n"
    "s_cmp_eq_u32 s53, 10\n"
    "v_mfma_f32_16x16x32_bf16 v[172:175], v[104:107], v[100:103], v[172:175]\n"
    "ds_read_b64_tr_b16 v[132:133], v25 offset:9216\n"
    "s_cselect_b32 s53, 0, s53\n"
    "v_mfma_f32_16x16x32_bf16 v[188:191], v[108:111], v[100:103], v[188:191]\n"
    "ds_read_b64_tr_b16 v[134:135], v25 offset:9728\n"
    "v_cmp_gt_u32_e64 vcc, 4, v0\n"
    "v_mov_b32 v47, 2147483648\n"
    "v_lshlrev_b32_e64 v46, 2, v0\n"
    "v_mfma_f32_16x16x32_bf16 v[184:187], v[108:111], v[96:99], v[184:187]\n"
    "ds_read_b128 a[60:63], v22 offset:2048\n"
    "v_cndmask_b32 v46, v47, v46, vcc\n"
    "s_lshl_b32 s60, s53, 7\n"
    "v_mfma_f32_16x16x32_bf16 v[180:183], v[108:111], a[52:55], v[180:183]\n"
    "ds_read_b128 v[124:127], v24 offset:0\n"
    "s_add_u32 m0, s58, 32768\n"
    "s_cmp_eq_u32 s3, 0\n"
    "s_cbranch_scc0 L_f1s_kv_%=\n"
    "buffer_store_dword v248, v46, s[36:39], s67 offen sc1\n"
    "L_f1s_kv_%=:\n"
    "v_mfma_f32_16x16x32_bf16 v[176:179], v[108:111], a[48:51], v[176:179]\n"
    "buffer_load_dwordx4 v16, s[24:27], s60 offen lds\n"
    "v_mfma_f32_16x16x32_bf16 v[192:195], v[112:115], a[48:51], v[192:195]\n"
    "ds_read_b128 v[128:131], v24 offset:2048\n"
    "v_add_u32 v247, 4096, v1\n"
    "v_mfma_f32_16x16x32_bf16 v[196:199], v[112:115], a[52:55], v[196:199]\n"
    "ds_read_b64_tr_b16 v[136:137], v26 offset:9216\n"
    "v_mfma_f32_16x16x32_bf16 v[200:203], v[112:115], v[96:99], v[200:203]\n"
    "buffer_load_dwordx4 v17, s[24:27], s60 offen offset:1024 lds\n"
    "v_mfma_f32_16x16x32_bf16 v[204:207], v[112:115], v[100:103], v[204:207]\n"
    "ds_read_b64_tr_b16 v[138:139], v26 offset:9728\n"
    "v_cmp_gt_u32_e64 vcc, 32, v0\n"
    "v_mfma_f32_16x16x32_bf16 v[220:223], v[116:119], v[100:103], v[220:223]\n"
    "ds_read_b64_tr_b16 v[140:141], v25 offset:10240\n"
    "v_mov_b32 v38, 2147483648\n"
    "v_lshlrev_b32_e64 v249, 2, v0\n"
    "v_mfma_f32_16x16x32_bf16 v[216:219], v[116:119], v[96:99], v[216:219]\n"
    "ds_read_b64_tr_b16 v[142:143], v25 offset:10752\n"
    "v_cndmask_b32_e64 v249, v38, v249, vcc\n"
    "s_add_u32 m0, s59, 6144\n"
    "v_mfma_f32_16x16x32_bf16 v[212:215], v[116:119], a[52:55], v[212:215]\n"
    "ds_read_b64_tr_b16 v[144:145], v26 offset:10240\n"
    "s_mul_i32 s61, s53, 245760\n"
    "s_bfe_u32 s6, s2, 0x20001\n"
    "s_lshr_b32 s54, s52, 3\n"
    "v_mfma_f32_16x16x32_bf16 v[208:211], v[116:119], a[48:51], v[208:211]\n"
    "ds_read_b64_tr_b16 v[146:147], v26 offset:10752\n"
    "buffer_load_dwordx4 v18, s[28:31], s61 offen lds\n"
    "v_mfma_f32_16x16x32_bf16 v[224:227], v[120:123], a[48:51], v[224:227]\n"
    "ds_read_b64_tr_b16 v[148:149], v25 offset:11264\n"
    "s_lshl2_add_u32 s54, s6, s54\n"
    "s_bfe_u32 s6, s52, 0x10001\n"
    "v_mfma_f32_16x16x32_bf16 v[228:231], v[120:123], a[52:55], v[228:231]\n"
    "ds_read_b64_tr_b16 v[150:151], v25 offset:11776\n"
    "buffer_load_dwordx4 v19, s[28:31], s61 offen offset:1024 lds\n"
    "v_mfma_f32_16x16x32_bf16 v[232:235], v[120:123], v[96:99], v[232:235]\n"
    "s_and_b32 s55, s52, 1\n"
    "s_xor_b32 s6, s6, s55\n"
    "v_mfma_f32_16x16x32_bf16 v[236:239], v[120:123], v[100:103], v[236:239]\n"
    "buffer_load_dwordx4 v20, s[28:31], s61 offen offset:2048 lds\n"
    "s_lshl1_add_u32 s54, s54, s6\n"
    "s_bfe_u32 s6, s52, 0x10002\n"
    "s_waitcnt lgkmcnt(0)\n"
    "v_mfma_f32_16x16x32_bf16 v[160:163], v[132:135], a[56:59], v[160:163]\n"
    "s_waitcnt vmcnt(8)\n"
    "s_barrier\n"
    "v_mfma_f32_16x16x32_bf16 v[164:167], v[132:135], a[60:63], v[164:167]\n"
    "ds_read_b128 a[64:67], v21 offset:16384\n"
    "s_add_u32 s60, s60, 20480\n"
    "v_mfma_f32_16x16x32_bf16 v[168:171], v[132:135], v[124:127], v[168:171]\n"
    "s_lshl1_add_u32 s55, s6, s55\n"
    "s_and_b32 s6, s2, 1\n"
    "s_add_u32 m0, s58, 34816\n"
    "v_mfma_f32_16x16x32_bf16 v[172:175], v[132:135], v[128:131], v[172:175]\n"
    "ds_read_b64_tr_b16 v[104:105], v25 offset:0\n"
    "s_lshl2_add_u32 s55, s6, s55\n"
    "v_mfma_f32_16x16x32_bf16 v[188:191], v[136:139], v[128:131], v[188:191]\n"
    "ds_read_b64_tr_b16 v[106:107], v25 offset:512\n"
    "s_and_b32 s47, s54, 7\n"
    "v_mfma_f32_16x16x32_bf16 v[184:187], v[136:139], v[124:127], v[184:187]\n"
    "buffer_load_dwordx4 v16, s[24:27], s60 offen lds\n"
    "v_mfma_f32_16x16x32_bf16 v[180:183], v[136:139], a[60:63], v[180:183]\n"
    "ds_read_b128 a[68:71], v21 offset:18432\n"
    "s_lshl2_add_u32 s64, s55, s3\n"
    "v_mfma_f32_16x16x32_bf16 v[176:179], v[136:139], a[56:59], v[176:179]\n"
    "ds_read_b128 v[96:99], v23 offset:16384\n"
    "s_mul_i32 s64, s64, 87040\n"
    "v_mfma_f32_16x16x32_bf16 v[192:195], v[140:143], a[56:59], v[192:195]\n"
    "buffer_load_dwordx4 v17, s[24:27], s60 offen offset:1024 lds\n"
    "v_mfma_f32_16x16x32_bf16 v[196:199], v[140:143], a[60:63], v[196:199]\n"
    "ds_read_b128 v[100:103], v23 offset:18432\n"
    "s_add_u32 s64, s64, 1703936\n"
    "v_mfma_f32_16x16x32_bf16 v[200:203], v[140:143], v[124:127], v[200:203]\n"
    "ds_read_b64_tr_b16 v[108:109], v26 offset:0\n"
    "s_add_u32 s72, s64, 61440\n"
    "v_mfma_f32_16x16x32_bf16 v[204:207], v[140:143], v[128:131], v[204:207]\n"
    "ds_read_b64_tr_b16 v[110:111], v26 offset:512\n"
    "s_add_u32 m0, s59, 9216\n"
    "v_mfma_f32_16x16x32_bf16 v[220:223], v[144:147], v[128:131], v[220:223]\n"
    "ds_read_b64_tr_b16 v[112:113], v25 offset:1024\n"
    "s_add_u32 s61, s61, 122880\n"
    "s_mov_b32 s68, 0x3fb8aa3b\n"
    "v_mfma_f32_16x16x32_bf16 v[216:219], v[144:147], v[124:127], v[216:219]\n"
    "ds_read_b64_tr_b16 v[114:115], v25 offset:1536\n"
    "s_mov_b32 s69, 0xb5c1a2a0\n"
    "buffer_load_dwordx4 v18, s[28:31], s61 offen lds\n"
    "v_mfma_f32_16x16x32_bf16 v[212:215], v[144:147], a[60:63], v[212:215]\n"
    "ds_read_b64_tr_b16 v[116:117], v26 offset:1024\n"
    "s_lshr_b32 s6, s47, 1\n"
    "v_mfma_f32_16x16x32_bf16 v[208:211], v[144:147], a[56:59], v[208:211]\n"
    "ds_read_b64_tr_b16 v[118:119], v26 offset:1536\n"
    "v_mfma_f32_16x16x32_bf16 v[224:227], v[148:151], a[56:59], v[224:227]\n"
    "ds_read_b64_tr_b16 v[120:121], v25 offset:2048\n"
    "buffer_load_dwordx4 v19, s[28:31], s61 offen offset:1024 lds\n"
    "v_mfma_f32_16x16x32_bf16 v[228:231], v[148:151], a[60:63], v[228:231]\n"
    "ds_read_b64_tr_b16 v[122:123], v25 offset:2560\n"
    "s_mul_i32 s6, s6, 5120\n"
    "v_mfma_f32_16x16x32_bf16 v[232:235], v[148:151], v[124:127], v[232:235]\n"
    "s_add_u32 s64, s64, s6\n"
    "buffer_load_dwordx4 v20, s[28:31], s61 offen offset:2048 lds\n"
    "v_mfma_f32_16x16x32_bf16 v[236:239], v[148:151], v[128:131], v[236:239]\n"
    "s_add_u32 s64, s64, 40960\n"
    "s_mul_i32 s73, s3, 25600\n"
    "s_waitcnt lgkmcnt(0)\n"
    "v_mfma_f32_16x16x32_bf16 v[160:163], v[104:107], a[64:67], v[160:163]\n"
    "s_waitcnt vmcnt(10)\n"
    "s_barrier\n"
    "v_mfma_f32_16x16x32_bf16 v[164:167], v[104:107], a[68:71], v[164:167]\n"
    "ds_read_b128 a[72:75], v22 offset:16384\n"
    "s_add_u32 s53, s53, 1\n"
    "v_mfma_f32_16x16x32_bf16 v[168:171], v[104:107], v[96:99], v[168:171]\n"
    "s_cmp_eq_u32 s53, 10\n"
    "s_cselect_b32 s53, 0, s53\n"
    "v_and_b32 v38, 15, v0\n"
    "v_mfma_f32_16x16x32_bf16 v[172:175], v[104:107], v[100:103], v[172:175]\n"
    "ds_read_b64_tr_b16 v[132:133], v25 offset:3072\n"
    "v_lshrrev_b32_e64 v39, 4, v0\n"
    "v_mfma_f32_16x16x32_bf16 v[188:191], v[108:111], v[100:103], v[188:191]\n"
    "ds_read_b64_tr_b16 v[134:135], v25 offset:3584\n"
    "s_lshl_b32 s60, s53, 7\n"
    "v_mfma_f32_16x16x32_bf16 v[184:187], v[108:111], v[96:99], v[184:187]\n"
    "ds_read_b128 a[76:79], v22 offset:18432\n"
    "s_add_u32 m0, s58, 0\n"
    "v_mfma_f32_16x16x32_bf16 v[180:183], v[108:111], a[68:71], v[180:183]\n"
    "ds_read_b128 v[124:127], v24 offset:16384\n"
    "s_mul_i32 s6, s3, 11520\n"
    "v_mfma_f32_16x16x32_bf16 v[176:179], v[108:111], a[64:67], v[176:179]\n"
    "s_lshl_b32 s7, s57, 5\n"
    "buffer_load_dwordx4 v16, s[24:27], s60 offen lds\n"
    "v_mfma_f32_16x16x32_bf16 v[192:195], v[112:115], a[64:67], v[192:195]\n"
    "ds_read_b128 v[128:131], v24 offset:18432\n"
    "s_add_u32 s6, s6, s7\n"
    "v_mfma_f32_16x16x32_bf16 v[196:199], v[112:115], a[68:71], v[196:199]\n"
    "ds_read_b64_tr_b16 v[136:137], v26 offset:3072\n"
    "v_mul_u32_u24 v41, 0x168, v38\n"
    "v_mfma_f32_16x16x32_bf16 v[200:203], v[112:115], v[96:99], v[200:203]\n"
    "buffer_load_dwordx4 v17, s[24:27], s60 offen offset:1024 lds\n"
    "v_mfma_f32_16x16x32_bf16 v[204:207], v[112:115], v[100:103], v[204:207]\n"
    "ds_read_b64_tr_b16 v[138:139], v26 offset:3584\n"
    "v_lshl_add_u32 v40, v39, 3, s6\n"
    "v_mfma_f32_16x16x32_bf16 v[220:223], v[116:119], v[100:103], v[220:223]\n"
    "ds_read_b64_tr_b16 v[140:141], v25 offset:4096\n"
    "s_lshl_b32 s7, s57, 2\n"
    "v_mfma_f32_16x16x32_bf16 v[216:219], v[116:119], v[96:99], v[216:219]\n"
    "ds_read_b64_tr_b16 v[142:143], v25 offset:4608\n"
    "s_add_u32 m0, s59, 0\n"
    "s_mul_i32 s61, s53, 245760\n"
    "v_mfma_f32_16x16x32_bf16 v[212:215], v[116:119], a[68:71], v[212:215]\n"
    "ds_read_b64_tr_b16 v[144:145], v26 offset:4096\n"
    "v_add_u32_e64 v48, v40, v41\n"
    "buffer_load_dwordx4 v18, s[28:31], s61 offen lds\n"
    "v_mfma_f32_16x16x32_bf16 v[208:211], v[116:119], a[64:67], v[208:211]\n"
    "ds_read_b64_tr_b16 v[146:147], v26 offset:4608\n"
    "v_add_u32_e64 v49, 64, v48\n"
    "v_mfma_f32_16x16x32_bf16 v[224:227], v[120:123], a[64:67], v[224:227]\n"
    "ds_read_b64_tr_b16 v[148:149], v25 offset:5120\n"
    "v_mfma_f32_16x16x32_bf16 v[228:231], v[120:123], a[68:71], v[228:231]\n"
    "ds_read_b64_tr_b16 v[150:151], v25 offset:5632\n"
    "buffer_load_dwordx4 v19, s[28:31], s61 offen offset:1024 lds\n"
    "v_mfma_f32_16x16x32_bf16 v[232:235], v[120:123], v[96:99], v[232:235]\n"
    "v_add_u32 v40, -4, v39\n"
    "v_add_u32 v40, s7, v40\n"
    "v_mfma_f32_16x16x32_bf16 v[236:239], v[120:123], v[100:103], v[236:239]\n"
    "buffer_load_dwordx4 v20, s[28:31], s61 offen offset:2048 lds\n"
    "s_waitcnt lgkmcnt(0)\n"
    "v_mfma_f32_16x16x32_bf16 v[160:163], v[132:135], a[72:75], v[160:163]\n"
    "s_waitcnt vmcnt(8)\n"
    "s_barrier\n"
    "v_mfma_f32_16x16x32_bf16 v[164:167], v[132:135], a[76:79], v[164:167]\n"
    "ds_read_b128 a[80:83], v21 offset:32768\n"
    "s_add_u32 s60, s60, 20480\n"
    "v_mfma_f32_16x16x32_bf16 v[168:171], v[132:135], v[124:127], v[168:171]\n"
    "v_max_i32 v40, 0, v40\n"
    "v_mul_u32_u24_e64 v40, 52, v40\n"
    "v_mfma_f32_16x16x32_bf16 v[172:175], v[132:135], v[128:131], v[172:175]\n"
    "ds_read_b64_tr_b16 v[104:105], v25 offset:6144\n"
    "s_add_u32 m0, s58, 2048\n"
    "v_mfma_f32_16x16x32_bf16 v[188:191], v[136:139], v[128:131], v[188:191]\n"
    "ds_read_b64_tr_b16 v[106:107], v25 offset:6656\n"
    "v_lshrrev_b32_e64 v40, 8, v40\n"
    "v_mfma_f32_16x16x32_bf16 v[184:187], v[136:139], v[124:127], v[184:187]\n"
    "ds_read_b128 a[84:87], v21 offset:34816\n"
    "v_lshl_add_u32 v40, v40, 3, v48\n"
    "v_mfma_f32_16x16x32_bf16 v[180:183], v[136:139], a[76:79], v[180:183]\n"
    "buffer_load_dwordx4 v16, s[24:27], s60 offen lds\n"
    "v_mfma_f32_16x16x32_bf16 v[176:179], v[136:139], a[72:75], v[176:179]\n"
    "ds_read_b128 v[96:99], v23 offset:32768\n"
    "v_add_u32 v50, 128, v40\n"
    "v_mfma_f32_16x16x32_bf16 v[192:195], v[140:143], a[72:75], v[192:195]\n"
    "buffer_load_dwordx4 v17, s[24:27], s60 offen offset:1024 lds\n"
    "v_mfma_f32_16x16x32_bf16 v[196:199], v[140:143], a[76:79], v[196:199]\n"
    "ds_read_b128 v[100:103], v23 offset:34816\n"
    "v_add_u32_e64 v40, 4, v39\n"
    "v_mfma_f32_16x16x32_bf16 v[200:203], v[140:143], v[124:127], v[200:203]\n"
    "ds_read_b64_tr_b16 v[108:109], v26 offset:6144\n"
    "v_add_u32_e64 v40, s7, v40\n"
    "v_mfma_f32_16x16x32_bf16 v[204:207], v[140:143], v[128:131], v[204:207]\n"
    "ds_read_b64_tr_b16 v[110:111], v26 offset:6656\n"
    "s_add_u32 m0, s59, 3072\n"
    "s_add_u32 s61, s61, 122880\n"
    "v_mfma_f32_16x16x32_bf16 v[220:223], v[144:147], v[128:131], v[220:223]\n"
    "ds_read_b64_tr_b16 v[112:113], v25 offset:7168\n"
    "v_max_i32_e64 v40, 0, v40\n"
    "v_mfma_f32_16x16x32_bf16 v[216:219], v[144:147], v[124:127], v[216:219]\n"
    "ds_read_b64_tr_b16 v[114:115], v25 offset:7680\n"
    "v_mul_u32_u24_e64 v40, 52, v40\n"
    "buffer_load_dwordx4 v18, s[28:31], s61 offen lds\n"
    "v_mfma_f32_16x16x32_bf16 v[212:215], v[144:147], a[76:79], v[212:215]\n"
    "ds_read_b64_tr_b16 v[116:117], v26 offset:7168\n"
    "v_lshrrev_b32_e64 v40, 8, v40\n"
    "v_mfma_f32_16x16x32_bf16 v[208:211], v[144:147], a[72:75], v[208:211]\n"
    "ds_read_b64_tr_b16 v[118:119], v26 offset:7680\n"
    "v_lshl_add_u32 v40, v40, 3, v48\n"
    "v_mfma_f32_16x16x32_bf16 v[224:227], v[148:151], a[72:75], v[224:227]\n"
    "ds_read_b64_tr_b16 v[120:121], v25 offset:8192\n"
    "buffer_load_dwordx4 v19, s[28:31], s61 offen offset:1024 lds\n"
    "v_mfma_f32_16x16x32_bf16 v[228:231], v[148:151], a[76:79], v[228:231]\n"
    "ds_read_b64_tr_b16 v[122:123], v25 offset:8704\n"
    "v_add_u32 v51, 192, v40\n"
    "v_mfma_f32_16x16x32_bf16 v[232:235], v[148:151], v[124:127], v[232:235]\n"
    "buffer_load_dwordx4 v20, s[28:31], s61 offen offset:2048 lds\n"
    "v_mfma_f32_16x16x32_bf16 v[236:239], v[148:151], v[128:131], v[236:239]\n"
    "v_add_u32_e64 v40, 12, v39\n"
    "v_add_u32 v40, s7, v40\n"
    "s_waitcnt lgkmcnt(0)\n"
    "v_mfma_f32_16x16x32_bf16 v[160:163], v[104:107], a[80:83], v[160:163]\n"
    "s_waitcnt vmcnt(10)\n"
    "s_barrier\n"
    "v_mfma_f32_16x16x32_bf16 v[164:167], v[104:107], a[84:87], v[164:167]\n"
    "ds_read_b128 a[88:91], v22 offset:32768\n"
    "v_max_i32_e64 v40, 0, v40\n"
    "v_mfma_f32_16x16x32_bf16 v[168:171], v[104:107], v[96:99], v[168:171]\n"
    "v_mul_u32_u24 v40, 52, v40\n"
    "s_add_u32 s53, s53, 1\n"
    "v_mfma_f32_16x16x32_bf16 v[172:175], v[104:107], v[100:103], v[172:175]\n"
    "ds_read_b64_tr_b16 v[132:133], v25 offset:9216\n"
    "s_cmp_eq_u32 s53, 10\n"
    "v_mfma_f32_16x16x32_bf16 v[188:191], v[108:111], v[100:103], v[188:191]\n"
    "ds_read_b64_tr_b16 v[134:135], v25 offset:9728\n"
    "s_cselect_b32 s53, 0, s53\n"
    "v_mfma_f32_16x16x32_bf16 v[184:187], v[108:111], v[96:99], v[184:187]\n"
    "ds_read_b128 a[92:95], v22 offset:34816\n"
    "v_lshrrev_b32_e64 v40, 8, v40\n"
    "v_lshl_add_u32 v40, v40, 3, v48\n"
    "v_mfma_f32_16x16x32_bf16 v[180:183], v[108:111], a[84:87], v[180:183]\n"
    "ds_read_b128 v[124:127], v24 offset:32768\n"
    "s_lshl_b32 s60, s53, 7\n"
    "s_add_u32 m0, s58, 16384\n"
    "v_mfma_f32_16x16x32_bf16 v[176:179], v[108:111], a[80:83], v[176:179]\n"
    "v_add_u32 v52, 256, v40\n"
    "buffer_load_dwordx4 v16, s[24:27], s60 offen lds\n"
    "v_mfma_f32_16x16x32_bf16 v[192:195], v[112:115], a[80:83], v[192:195]\n"
    "ds_read_b128 v[128:131], v24 offset:34816\n"
    "s_mul_i32 s6, s57, 23040\n"
    "v_mfma_f32_16x16x32_bf16 v[196:199], v[112:115], a[84:87], v[196:199]\n"
    "ds_read_b64_tr_b16 v[136:137], v26 offset:9216\n"
    "s_sub_u32 s6, 11520, s6\n"
    "v_mfma_f32_16x16x32_bf16 v[200:203], v[112:115], v[96:99], v[200:203]\n"
    "buffer_load_dwordx4 v17, s[24:27], s60 offen offset:1024 lds\n"
    "v_mfma_f32_16x16x32_bf16 v[204:207], v[112:115], v[100:103], v[204:207]\n"
    "ds_read_b64_tr_b16 v[138:139], v26 offset:9728\n"
    "v_add_u32 v53, s6, v48\n"
    "v_mfma_f32_16x16x32_bf16 v[220:223], v[116:119], v[100:103], v[220:223]\n"
    "ds_read_b64_tr_b16 v[140:141], v25 offset:10240\n"
    "v_add_u32_e64 v54, s6, v49\n"
    "v_mfma_f32_16x16x32_bf16 v[216:219], v[116:119], v[96:99], v[216:219]\n"
    "ds_read_b64_tr_b16 v[142:143], v25 offset:10752\n"
    "s_add_u32 m0, s59, 6144\n"
    "s_mul_i32 s61, s53, 245760\n"
    "v_mfma_f32_16x16x32_bf16 v[212:215], v[116:119], a[84:87], v[212:215]\n"
    "ds_read_b64_tr_b16 v[144:145], v26 offset:10240\n"
    "v_add_u32_e64 v55, s6, v50\n"
    "buffer_load_dwordx4 v18, s[28:31], s61 offen lds\n"
    "v_mfma_f32_16x16x32_bf16 v[208:211], v[116:119], a[80:83], v[208:211]\n"
    "ds_read_b64_tr_b16 v[146:147], v26 offset:10752\n"
    "v_add_u32_e64 v56, s6, v51\n"
    "v_mfma_f32_16x16x32_bf16 v[224:227], v[120:123], a[80:83], v[224:227]\n"
    "ds_read_b64_tr_b16 v[148:149], v25 offset:11264\n"
    "buffer_load_dwordx4 v19, s[28:31], s61 offen offset:1024 lds\n"
    "v_mfma_f32_16x16x32_bf16 v[228:231], v[120:123], a[84:87], v[228:231]\n"
    "ds_read_b64_tr_b16 v[150:151], v25 offset:11776\n"
    "v_add_u32_e64 v57, s6, v52\n"
    "v_mfma_f32_16x16x32_bf16 v[232:235], v[120:123], v[96:99], v[232:235]\n"
    "buffer_load_dwordx4 v20, s[28:31], s61 offen offset:2048 lds\n"
    "v_mfma_f32_16x16x32_bf16 v[236:239], v[120:123], v[100:103], v[236:239]\n"
    "s_mul_i32 s6, s3, 48\n"
    "s_add_u32 s6, s6, 200\n"
    "s_waitcnt lgkmcnt(0)\n"
    "v_mfma_f32_16x16x32_bf16 v[160:163], v[132:135], a[88:91], v[160:163]\n"
    "s_waitcnt vmcnt(8)\n"
    "s_barrier\n"
    "v_mfma_f32_16x16x32_bf16 v[164:167], v[132:135], a[92:95], v[164:167]\n"
    "ds_read_b128 a[96:99], v21 offset:0\n"
    "s_add_u32 s60, s60, 20480\n"
    "v_mfma_f32_16x16x32_bf16 v[168:171], v[132:135], v[124:127], v[168:171]\n"
    "v_mul_u32_u24 v40, 0x168, v0\n"
    "v_add_u32_e64 v62, s6, v40\n"
    "v_mfma_f32_16x16x32_bf16 v[172:175], v[132:135], v[128:131], v[172:175]\n"
    "ds_read_b64_tr_b16 v[104:105], v25 offset:0\n"
    "s_add_u32 m0, s58, 18432\n"
    "v_mfma_f32_16x16x32_bf16 v[188:191], v[136:139], v[128:131], v[188:191]\n"
    "ds_read_b64_tr_b16 v[106:107], v25 offset:512\n"
    "v_lshrrev_b32_e64 v40, 2, v0\n"
    "v_mfma_f32_16x16x32_bf16 v[184:187], v[136:139], v[124:127], v[184:187]\n"
    "ds_read_b128 a[100:103], v21 offset:2048\n"
    "v_and_b32_e64 v41, 3, v0\n"
    "v_mfma_f32_16x16x32_bf16 v[180:183], v[136:139], a[92:95], v[180:183]\n"
    "buffer_load_dwordx4 v16, s[24:27], s60 offen lds\n"
    "v_mfma_f32_16x16x32_bf16 v[176:179], v[136:139], a[88:91], v[176:179]\n"
    "ds_read_b128 v[96:99], v23 offset:0\n"
    "s_mul_i32 s6, s3, 40\n"
    "v_mfma_f32_16x16x32_bf16 v[192:195], v[140:143], a[88:91], v[192:195]\n"
    "buffer_load_dwordx4 v17, s[24:27], s60 offen offset:1024 lds\n"
    "v_mfma_f32_16x16x32_bf16 v[196:199], v[140:143], a[92:95], v[196:199]\n"
    "ds_read_b128 v[100:103], v23 offset:2048\n"
    "v_lshl_add_u32 v42, v41, 3, s6\n"
    "v_mfma_f32_16x16x32_bf16 v[200:203], v[140:143], v[124:127], v[200:203]\n"
    "ds_read_b64_tr_b16 v[108:109], v26 offset:0\n"
    "v_mul_u32_u24 v40, 0x168, v40\n"
    "v_mfma_f32_16x16x32_bf16 v[204:207], v[140:143], v[128:131], v[204:207]\n"
    "ds_read_b64_tr_b16 v[110:111], v26 offset:512\n"
    "s_add_u32 m0, s59, 9216\n"
    "s_add_u32 s61, s61, 122880\n"
    "v_mfma_f32_16x16x32_bf16 v[220:223], v[144:147], v[128:131], v[220:223]\n"
    "ds_read_b64_tr_b16 v[112:113], v25 offset:1024\n"
    "v_add_u32 v58, v40, v42\n"
    "v_mfma_f32_16x16x32_bf16 v[216:219], v[144:147], v[124:127], v[216:219]\n"
    "ds_read_b64_tr_b16 v[114:115], v25 offset:1536\n"
    "s_lshl3_add_u32 s6, s3, 160\n"
    "buffer_load_dwordx4 v18, s[28:31], s61 offen lds\n"
    "v_mfma_f32_16x16x32_bf16 v[212:215], v[144:147], a[92:95], v[212:215]\n"
    "ds_read_b64_tr_b16 v[116:117], v26 offset:1024\n"
    "v_add_u32_e64 v59, s6, v58\n"
    "v_mfma_f32_16x16x32_bf16 v[208:211], v[144:147], a[88:91], v[208:211]\n"
    "ds_read_b64_tr_b16 v[118:119], v26 offset:1536\n"
    "buffer_load_dwordx4 v19, s[28:31], s61 offen offset:1024 lds\n"
    "v_mfma_f32_16x16x32_bf16 v[224:227], v[148:151], a[88:91], v[224:227]\n"
    "ds_read_b64_tr_b16 v[120:121], v25 offset:2048\n"
    "v_cmp_eq_u32_e64 vcc, 0, v41\n"
    "v_mfma_f32_16x16x32_bf16 v[228:231], v[148:151], a[92:95], v[228:231]\n"
    "ds_read_b64_tr_b16 v[122:123], v25 offset:2560\n"
    "v_add_u32_e64 v40, 24, v59\n"
    "v_mfma_f32_16x16x32_bf16 v[232:235], v[148:151], v[124:127], v[232:235]\n"
    "v_add_u32 v42, 32, v58\n"
    "v_cndmask_b32 v60, v40, v42, vcc\n"
    "v_mfma_f32_16x16x32_bf16 v[236:239], v[148:151], v[128:131], v[236:239]\n"
    "buffer_load_dwordx4 v20, s[28:31], s61 offen offset:2048 lds\n"
    "s_waitcnt lgkmcnt(0)\n"
    "v_mfma_f32_16x16x32_bf16 v[160:163], v[104:107], a[96:99], v[160:163]\n"
    "s_waitcnt vmcnt(10)\n"
    "s_barrier\n"
    "v_mfma_f32_16x16x32_bf16 v[164:167], v[104:107], a[100:103], v[164:167]\n"
    "ds_read_b128 a[104:107], v22 offset:0\n"
    "s_add_u32 s53, s53, 1\n"
    "v_mfma_f32_16x16x32_bf16 v[168:171], v[104:107], v[96:99], v[168:171]\n"
    "s_cmp_eq_u32 s53, 10\n"
    "s_cselect_b32 s53, 0, s53\n"
    "v_mad_u32_u24 v61, v39, 5, v38\n"
    "v_mfma_f32_16x16x32_bf16 v[172:175], v[104:107], v[100:103], v[172:175]\n"
    "ds_read_b64_tr_b16 v[132:133], v25 offset:3072\n"
    "s_lshl_b32 s60, s53, 7\n"
    "v_mfma_f32_16x16x32_bf16 v[188:191], v[108:111], v[100:103], v[188:191]\n"
    "ds_read_b64_tr_b16 v[134:135], v25 offset:3584\n"
    "s_add_u32 m0, s58, 32768\n"
    "v_mfma_f32_16x16x32_bf16 v[184:187], v[108:111], v[96:99], v[184:187]\n"
    "ds_read_b128 a[108:111], v22 offset:2048\n"
    "v_lshlrev_b32 v61, 4, v61\n"
    "v_mfma_f32_16x16x32_bf16 v[180:183], v[108:111], a[100:103], v[180:183]\n"
    "buffer_load_dwordx4 v16, s[24:27], s60 offen lds\n"
    "v_mfma_f32_16x16x32_bf16 v[176:179], v[108:111], a[96:99], v[176:179]\n"
    "ds_read_b128 v[124:127], v24 offset:0\n"
    "s_lshr_b32 s6, s54, 1\n"
    "v_mfma_f32_16x16x32_bf16 v[192:195], v[112:115], a[96:99], v[192:195]\n"
    "ds_read_b128 v[128:131], v24 offset:2048\n"
    "s_lshl2_add_u32 s16, s6, s3\n"
    "v_mfma_f32_16x16x32_bf16 v[196:199], v[112:115], a[100:103], v[196:199]\n"
    "buffer_load_dwordx4 v17, s[24:27], s60 offen offset:1024 lds\n"
    "v_mfma_f32_16x16x32_bf16 v[200:203], v[112:115], v[96:99], v[200:203]\n"
    "ds_read_b64_tr_b16 v[136:137], v26 offset:3072\n"
    "s_and_b32 s6, s54, 1\n"
    "v_mfma_f32_16x16x32_bf16 v[204:207], v[112:115], v[100:103], v[204:207]\n"
    "ds_read_b64_tr_b16 v[138:139], v26 offset:3584\n"
    "s_lshl3_add_u32 s16, s16, s6\n"
    "v_mfma_f32_16x16x32_bf16 v[220:223], v[116:119], v[100:103], v[220:223]\n"
    "ds_read_b64_tr_b16 v[140:141], v25 offset:4096\n"
    "s_add_u32 m0, s59, 0\n"
    "s_mul_i32 s61, s53, 245760\n"
    "v_mfma_f32_16x16x32_bf16 v[216:219], v[116:119], v[96:99], v[216:219]\n"
    "ds_read_b64_tr_b16 v[142:143], v25 offset:4608\n"
    "s_lshl_b32 s16, s16, 8\n"
    "s_add_u32 s16, s16, 131072\n"
    "v_mfma_f32_16x16x32_bf16 v[212:215], v[116:119], a[100:103], v[212:215]\n"
    "ds_read_b64_tr_b16 v[144:145], v26 offset:4096\n"
    "buffer_load_dwordx4 v18, s[28:31], s61 offen lds\n"
    "v_mfma_f32_16x16x32_bf16 v[208:211], v[116:119], a[96:99], v[208:211]\n"
    "ds_read_b64_tr_b16 v[146:147], v26 offset:4608\n"
    "v_min_u32_e64 v6, 3, v0\n"
    "v_mfma_f32_16x16x32_bf16 v[224:227], v[120:123], a[96:99], v[224:227]\n"
    "ds_read_b64_tr_b16 v[148:149], v25 offset:5120\n"
    "v_lshrrev_b32_e64 v38, 1, v6\n"
    "v_mfma_f32_16x16x32_bf16 v[228:231], v[120:123], a[100:103], v[228:231]\n"
    "ds_read_b64_tr_b16 v[150:151], v25 offset:5632\n"
    "buffer_load_dwordx4 v19, s[28:31], s61 offen offset:1024 lds\n"
    "v_mfma_f32_16x16x32_bf16 v[232:235], v[120:123], v[96:99], v[232:235]\n"
    "v_and_b32_e64 v6, 1, v6\n"
    "v_lshl_add_u32 v6, v38, 3, v6\n"
    "v_mfma_f32_16x16x32_bf16 v[236:239], v[120:123], v[100:103], v[236:239]\n"
    "buffer_load_dwordx4 v20, s[28:31], s61 offen offset:2048 lds\n"
    "s_waitcnt lgkmcnt(0)\n"
    "v_mfma_f32_16x16x32_bf16 v[160:163], v[132:135], a[104:107], v[160:163]\n"
    "s_waitcnt vmcnt(8)\n"
    "s_barrier\n"
    "v_mfma_f32_16x16x32_bf16 v[164:167], v[132:135], a[108:111], v[164:167]\n"
    "ds_read_b128 a[112:115], v21 offset:16384\n"
    "s_add_u32 s60, s60, 20480\n"
    "v_mfma_f32_16x16x32_bf16 v[168:171], v[132:135], v[124:127], v[168:171]\n"
    "v_lshlrev_b32 v6, 7, v6\n"
    "s_add_u32 m0, s58, 34816\n"
    "v_mfma_f32_16x16x32_bf16 v[172:175], v[132:135], v[128:131], v[172:175]\n"
    "ds_read_b64_tr_b16 v[104:105], v25 offset:6144\n"
    "v_lshrrev_b32_e64 v12, 3, v0\n"
    "v_mfma_f32_16x16x32_bf16 v[188:191], v[136:139], v[128:131], v[188:191]\n"
    "ds_read_b64_tr_b16 v[106:107], v25 offset:6656\n"
    "v_and_b32_e64 v38, 7, v0\n"
    "v_mfma_f32_16x16x32_bf16 v[184:187], v[136:139], v[124:127], v[184:187]\n"
    "buffer_load_dwordx4 v16, s[24:27], s60 offen lds\n"
    "v_mfma_f32_16x16x32_bf16 v[180:183], v[136:139], a[108:111], v[180:183]\n"
    "ds_read_b128 a[116:119], v21 offset:18432\n"
    "v_lshlrev_b32_e64 v39, 4, v38\n"
    "v_mfma_f32_16x16x32_bf16 v[176:179], v[136:139], a[104:107], v[176:179]\n"
    "ds_read_b128 v[96:99], v23 offset:16384\n"
    "v_lshl_add_u32 v12, v12, 13, v39\n"
    "v_mfma_f32_16x16x32_bf16 v[192:195], v[140:143], a[104:107], v[192:195]\n"
    "buffer_load_dwordx4 v17, s[24:27], s60 offen offset:1024 lds\n"
    "v_mfma_f32_16x16x32_bf16 v[196:199], v[140:143], a[108:111], v[196:199]\n"
    "ds_read_b128 v[100:103], v23 offset:18432\n"
    "s_lshl2_add_u32 s6, s4, s3\n"
    "v_mfma_f32_16x16x32_bf16 v[200:203], v[140:143], v[124:127], v[200:203]\n"
    "ds_read_b64_tr_b16 v[108:109], v26 offset:6144\n"
    "s_lshl_b32 s19, s5, 10\n"
    "v_mfma_f32_16x16x32_bf16 v[204:207], v[140:143], v[128:131], v[204:207]\n"
    "ds_read_b64_tr_b16 v[110:111], v26 offset:6656\n"
    "s_add_u32 m0, s59, 3072\n"
    "v_mfma_f32_16x16x32_bf16 v[220:223], v[144:147], v[128:131], v[220:223]\n"
    "ds_read_b64_tr_b16 v[112:113], v25 offset:7168\n"
    "s_add_u32 s61, s61, 122880\n"
    "s_add_u32 s19, s19, s6\n"
    "v_mfma_f32_16x16x32_bf16 v[216:219], v[144:147], v[124:127], v[216:219]\n"
    "ds_read_b64_tr_b16 v[114:115], v25 offset:7680\n"
    "s_lshl_b32 s19, s19, 7\n"
    "v_mfma_f32_16x16x32_bf16 v[212:215], v[144:147], a[108:111], v[212:215]\n"
    "ds_read_b64_tr_b16 v[116:117], v26 offset:7168\n"
    "buffer_load_dwordx4 v18, s[28:31], s61 offen lds\n"
    "v_mfma_f32_16x16x32_bf16 v[208:211], v[144:147], a[104:107], v[208:211]\n"
    "ds_read_b64_tr_b16 v[118:119], v26 offset:7680\n"
    "s_add_u32 s19, s19, 655360\n"
    "v_mfma_f32_16x16x32_bf16 v[224:227], v[148:151], a[104:107], v[224:227]\n"
    "ds_read_b64_tr_b16 v[120:121], v25 offset:8192\n"
    "s_add_u32 s20, s19, 65536\n"
    "v_mfma_f32_16x16x32_bf16 v[228:231], v[148:151], a[108:111], v[228:231]\n"
    "ds_read_b64_tr_b16 v[122:123], v25 offset:8704\n"
    "buffer_load_dwordx4 v19, s[28:31], s61 offen offset:1024 lds\n"
    "v_mfma_f32_16x16x32_bf16 v[232:235], v[148:151], v[124:127], v[232:235]\n"
    "s_and_b32 s6, s6, 15\n"
    "s_lshl_b32 s6, s6, 3\n"
    "v_mfma_f32_16x16x32_bf16 v[236:239], v[148:151], v[128:131], v[236:239]\n"
    "buffer_load_dwordx4 v20, s[28:31], s61 offen offset:2048 lds\n"
    "s_waitcnt lgkmcnt(0)\n"
    "v_mfma_f32_16x16x32_bf16 v[160:163], v[104:107], a[112:115], v[160:163]\n"
    "s_waitcnt vmcnt(10)\n"
    "s_barrier\n"
    "v_mfma_f32_16x16x32_bf16 v[164:167], v[104:107], a[116:119], v[164:167]\n"
    "ds_read_b128 a[120:123], v22 offset:16384\n"
    "s_add_u32 s53, s53, 1\n"
    "v_mfma_f32_16x16x32_bf16 v[168:171], v[104:107], v[96:99], v[168:171]\n"
    "s_cmp_eq_u32 s53, 10\n"
    "s_cselect_b32 s53, 0, s53\n"
    "v_add_u32_e64 v39, s6, v38\n"
    "v_mfma_f32_16x16x32_bf16 v[172:175], v[104:107], v[100:103], v[172:175]\n"
    "ds_read_b64_tr_b16 v[132:133], v25 offset:9216\n"
    "v_lshrrev_b32_e64 v13, 5, v0\n"
    "v_mfma_f32_16x16x32_bf16 v[188:191], v[108:111], v[100:103], v[188:191]\n"
    "ds_read_b64_tr_b16 v[134:135], v25 offset:9728\n"
    "s_lshl_b32 s60, s53, 7\n"
    "v_mfma_f32_16x16x32_bf16 v[184:187], v[108:111], v[96:99], v[184:187]\n"
    "ds_read_b128 a[124:127], v22 offset:18432\n"
    "s_add_u32 m0, s58, 0\n"
    "v_mfma_f32_16x16x32_bf16 v[180:183], v[108:111], a[116:119], v[180:183]\n"
    "ds_read_b128 v[124:127], v24 offset:16384\n"
    "v_cmp_gt_u32 vcc, 105, v39\n"
    "v_lshlrev_b32_e64 v13, 2, v13\n"
    "v_mfma_f32_16x16x32_bf16 v[176:179], v[108:111], a[112:115], v[176:179]\n"
    "v_lshl_add_u32 v13, v38, 3, v13\n"
    "s_mov_b64 s[22:23], vcc\n"
    "v_mfma_f32_16x16x32_bf16 v[192:195], v[112:115], a[112:115], v[192:195]\n"
    "buffer_load_dwordx4 v16, s[24:27], s60 offen lds\n"
    "v_mfma_f32_16x16x32_bf16 v[196:199], v[112:115], a[116:119], v[196:199]\n"
    "ds_read_b128 v[128:131], v24 offset:18432\n"
    "v_mfma_f32_16x16x32_bf16 v[200:203], v[112:115], v[96:99], v[200:203]\n"
    "ds_read_b64_tr_b16 v[136:137], v26 offset:9216\n"
    "buffer_load_dwordx4 v17, s[24:27], s60 offen offset:1024 lds\n"
    "v_mfma_f32_16x16x32_bf16 v[204:207], v[112:115], v[100:103], v[204:207]\n"
    "ds_read_b64_tr_b16 v[138:139], v26 offset:9728\n"
    "s_and_b32 vcc_lo, s22, 0xff\n"
    "v_mfma_f32_16x16x32_bf16 v[220:223], v[116:119], v[100:103], v[220:223]\n"
    "ds_read_b64_tr_b16 v[140:141], v25 offset:10240\n"
    "s_and_b32 vcc_hi, s23, 0xff\n"
    "v_mov_b32 v39, 2147483648\n"
    "v_mfma_f32_16x16x32_bf16 v[216:219], v[116:119], v[96:99], v[216:219]\n"
    "ds_read_b64_tr_b16 v[142:143], v25 offset:10752\n"
    "v_cndmask_b32 v13, v39, v13, vcc\n"
    "v_mfma_f32_16x16x32_bf16 v[212:215], v[116:119], a[116:119], v[212:215]\n"
    "ds_read_b64_tr_b16 v[144:145], v26 offset:10240\n"
    "s_add_u32 m0, s59, 6144\n"
    "s_mul_i32 s61, s53, 245760\n"
    "v_mfma_f32_16x16x32_bf16 v[208:211], v[116:119], a[112:115], v[208:211]\n"
    "ds_read_b64_tr_b16 v[146:147], v26 offset:10752\n"
    "buffer_load_dwordx4 v18, s[28:31], s61 offen lds\n"
    "v_mfma_f32_16x16x32_bf16 v[224:227], v[120:123], a[112:115], v[224:227]\n"
    "ds_read_b64_tr_b16 v[148:149], v25 offset:11264\n"
    "s_sub_u32 s88, s89, 655360\n"
    "v_mfma_f32_16x16x32_bf16 v[228:231], v[120:123], a[116:119], v[228:231]\n"
    "ds_read_b64_tr_b16 v[150:151], v25 offset:11776\n"
    "s_lshr_b32 s88, s88, 5\n"
    "buffer_load_dwordx4 v19, s[28:31], s61 offen offset:1024 lds\n"
    "v_mfma_f32_16x16x32_bf16 v[232:235], v[120:123], v[96:99], v[232:235]\n"
    "s_add_u32 s88, s88, 32768\n"
    "v_mfma_f32_16x16x32_bf16 v[236:239], v[120:123], v[100:103], v[236:239]\n"
    "buffer_load_dwordx4 v20, s[28:31], s61 offen offset:2048 lds\n"
    "s_waitcnt lgkmcnt(0)\n"
    "v_mfma_f32_16x16x32_bf16 v[160:163], v[132:135], a[120:123], v[160:163]\n"
    "s_waitcnt vmcnt(8)\n"
    "s_barrier\n"
    "v_mfma_f32_16x16x32_bf16 v[164:167], v[132:135], a[124:127], v[164:167]\n"
    "ds_read_b128 a[128:131], v21 offset:32768\n"
    "s_add_u32 s60, s60, 20480\n"
    "v_mfma_f32_16x16x32_bf16 v[168:171], v[132:135], v[124:127], v[168:171]\n"
    "s_lshl1_add_u32 s65, s4, s56\n"
    "s_lshl_b32 s65, s65, 9\n"
    "s_add_u32 m0, s58, 2048\n"
    "v_mfma_f32_16x16x32_bf16 v[172:175], v[132:135], v[128:131], v[172:175]\n"
    "ds_read_b64_tr_b16 v[104:105], v25 offset:0\n"
    "s_add_u32 s65, s65, 16384\n"
    "v_mfma_f32_16x16x32_bf16 v[188:191], v[136:139], v[128:131], v[188:191]\n"
    "ds_read_b64_tr_b16 v[106:107], v25 offset:512\n"
    "v_and_b32_e64 v45, 15, v0\n"
    "v_mfma_f32_16x16x32_bf16 v[184:187], v[136:139], v[124:127], v[184:187]\n"
    "buffer_load_dwordx4 v16, s[24:27], s60 offen lds\n"
    "v_mfma_f32_16x16x32_bf16 v[180:183], v[136:139], a[124:127], v[180:183]\n"
    "ds_read_b128 a[132:135], v21 offset:34816\n"
    "v_lshlrev_b32_e64 v45, 3, v45\n"
    "v_mfma_f32_16x16x32_bf16 v[176:179], v[136:139], a[120:123], v[176:179]\n"
    "ds_read_b128 v[96:99], v23 offset:32768\n"
    "v_mfma_f32_16x16x32_bf16 v[192:195], v[140:143], a[120:123], v[192:195]\n"
    "buffer_load_dwordx4 v17, s[24:27], s60 offen offset:1024 lds\n"
    "v_mfma_f32_16x16x32_bf16 v[196:199], v[140:143], a[124:127], v[196:199]\n"
    "ds_read_b128 v[100:103], v23 offset:34816\n"
    "v_lshl_add_u32 v45, s57, 8, v45\n"
    "v_mfma_f32_16x16x32_bf16 v[200:203], v[140:143], v[124:127], v[200:203]\n"
    "ds_read_b64_tr_b16 v[108:109], v26 offset:0\n"
    "s_add_u32 m0, s59, 9216\n"
    "v_mfma_f32_16x16x32_bf16 v[204:207], v[140:143], v[128:131], v[204:207]\n"
    "ds_read_b64_tr_b16 v[110:111], v26 offset:512\n"
    "s_add_u32 s61, s61, 122880\n"
    "v_mfma_f32_16x16x32_bf16 v[220:223], v[144:147], v[128:131], v[220:223]\n"
    "ds_read_b64_tr_b16 v[112:113], v25 offset:1024\n"
    "v_xor_b32 v46, 0x100, v45\n"
    "v_mfma_f32_16x16x32_bf16 v[216:219], v[144:147], v[124:127], v[216:219]\n"
    "ds_read_b64_tr_b16 v[114:115], v25 offset:1536\n"
    "v_mfma_f32_16x16x32_bf16 v[212:215], v[144:147], a[124:127], v[212:215]\n"
    "ds_read_b64_tr_b16 v[116:117], v26 offset:1024\n"
    "buffer_load_dwordx4 v18, s[28:31], s61 offen lds\n"
    "v_mfma_f32_16x16x32_bf16 v[208:211], v[144:147], a[120:123], v[208:211]\n"
    "ds_read_b64_tr_b16 v[118:119], v26 offset:1536\n"
    "s_mul_i32 s44, s3, 1024\n"
    "v_mfma_f32_16x16x32_bf16 v[224:227], v[148:151], a[120:123], v[224:227]\n"
    "ds_read_b64_tr_b16 v[120:121], v25 offset:2048\n"
    "v_mul_u32_u24_e64 v47, 16, v0\n"
    "v_mfma_f32_16x16x32_bf16 v[228:231], v[148:151], a[124:127], v[228:231]\n"
    "ds_read_b64_tr_b16 v[122:123], v25 offset:2560\n"
    "buffer_load_dwordx4 v19, s[28:31], s61 offen offset:1024 lds\n"
    "v_mfma_f32_16x16x32_bf16 v[232:235], v[148:151], v[124:127], v[232:235]\n"
    "s_add_u32 s44, s44, 65536\n"
    "v_mfma_f32_16x16x32_bf16 v[236:239], v[148:151], v[128:131], v[236:239]\n"
    "buffer_load_dwordx4 v20, s[28:31], s61 offen offset:2048 lds\n"
    "buffer_load_dwordx2 v[64:65], v45, s[36:39], s65 offen offset:0 sc1\n"
    "buffer_load_dwordx2 v[66:67], v45, s[36:39], s65 offen offset:128 sc1\n"
    "buffer_load_dword v63, v47, s[36:39], s44 offen sc1\n"
    "s_waitcnt lgkmcnt(0)\n"
    "v_mfma_f32_16x16x32_bf16 v[160:163], v[104:107], a[128:131], v[160:163]\n"
    "s_waitcnt vmcnt(13)\n"
    "s_barrier\n"
    "v_mfma_f32_16x16x32_bf16 v[164:167], v[104:107], a[132:135], v[164:167]\n"
    "ds_read_b128 a[136:139], v22 offset:32768\n"
    "s_mul_i32 s21, s62, 98304\n"
    "v_mfma_f32_16x16x32_bf16 v[168:171], v[104:107], v[96:99], v[168:171]\n"
    "s_add_u32 m0, s71, 98304\n"
    "v_mfma_f32_16x16x32_bf16 v[172:175], v[104:107], v[100:103], v[172:175]\n"
    "buffer_load_dwordx4 v29, s[32:35], s21 offen lds\n"
    "v_mfma_f32_16x16x32_bf16 v[188:191], v[108:111], v[100:103], v[188:191]\n"
    "ds_read_b64_tr_b16 v[132:133], v25 offset:3072\n"
    "v_mfma_f32_16x16x32_bf16 v[184:187], v[108:111], v[96:99], v[184:187]\n"
    "ds_read_b64_tr_b16 v[134:135], v25 offset:3584\n"
    "v_mfma_f32_16x16x32_bf16 v[180:183], v[108:111], a[132:135], v[180:183]\n"
    "ds_read_b128 a[140:143], v22 offset:34816\n"
    "v_mfma_f32_16x16x32_bf16 v[176:179], v[108:111], a[128:131], v[176:179]\n"
    "buffer_load_dwordx4 v30, s[32:35], s21 offen offset:1024 lds\n"
    "v_mfma_f32_16x16x32_bf16 v[192:195], v[112:115], a[128:131], v[192:195]\n"
    "ds_read_b128 v[124:127], v24 offset:32768\n"
    "v_mfma_f32_16x16x32_bf16 v[196:199], v[112:115], a[132:135], v[196:199]\n"
    "ds_read_b128 v[128:131], v24 offset:34816\n"
    "v_mfma_f32_16x16x32_bf16 v[200:203], v[112:115], v[96:99], v[200:203]\n"
    "ds_read_b64_tr_b16 v[136:137], v26 offset:3072\n"
    "v_mfma_f32_16x16x32_bf16 v[204:207], v[112:115], v[100:103], v[204:207]\n"
    "ds_read_b64_tr_b16 v[138:139], v26 offset:3584\n"
    "v_mfma_f32_16x16x32_bf16 v[220:223], v[116:119], v[100:103], v[220:223]\n"
    "ds_read_b64_tr_b16 v[140:141], v25 offset:4096\n"
    "v_mfma_f32_16x16x32_bf16 v[216:219], v[116:119], v[96:99], v[216:219]\n"
    "ds_read_b64_tr_b16 v[142:143], v25 offset:4608\n"
    "buffer_load_dwordx4 v31, s[32:35], s21 offen offset:2048 lds\n"
    "v_mfma_f32_16x16x32_bf16 v[212:215], v[116:119], a[132:135], v[212:215]\n"
    "ds_read_b64_tr_b16 v[144:145], v26 offset:4096\n"
    "v_mfma_f32_16x16x32_bf16 v[208:211], v[116:119], a[128:131], v[208:211]\n"
    "ds_read_b64_tr_b16 v[146:147], v26 offset:4608\n"
    "v_mfma_f32_16x16x32_bf16 v[224:227], v[120:123], a[128:131], v[224:227]\n"
    "ds_read_b64_tr_b16 v[148:149], v25 offset:5120\n"
    "buffer_load_dwordx4 v32, s[32:35], s21 offen offset:3072 lds\n"
    "v_mfma_f32_16x16x32_bf16 v[228:231], v[120:123], a[132:135], v[228:231]\n"
    "ds_read_b64_tr_b16 v[150:151], v25 offset:5632\n"
    "s_add_u32 s62, s62, 4\n"
    "v_mfma_f32_16x16x32_bf16 v[232:235], v[120:123], v[96:99], v[232:235]\n"
    "s_sub_u32 s67, s62, 20\n"
    "s_cselect_b32 s62, s62, s67\n"
    "v_mfma_f32_16x16x32_bf16 v[236:239], v[120:123], v[100:103], v[236:239]\n"
    "s_waitcnt lgkmcnt(0)\n"
    "v_mfma_f32_16x16x32_bf16 v[160:163], v[132:135], a[136:139], v[160:163]\n"
    "s_waitcnt vmcnt(10)\n"
    "s_barrier\n"
    "v_mfma_f32_16x16x32_bf16 v[164:167], v[132:135], a[140:143], v[164:167]\n"
    "ds_read_b128 a[144:147], v21 offset:0\n"
    "v_mfma_f32_16x16x32_bf16 v[168:171], v[132:135], v[124:127], v[168:171]\n"
    "v_mfma_f32_16x16x32_bf16 v[172:175], v[132:135], v[128:131], v[172:175]\n"
    "ds_read_b64_tr_b16 v[104:105], v25 offset:6144\n"
    "v_mfma_f32_16x16x32_bf16 v[188:191], v[136:139], v[128:131], v[188:191]\n"
    "ds_read_b64_tr_b16 v[106:107], v25 offset:6656\n"
    "v_mfma_f32_16x16x32_bf16 v[184:187], v[136:139], v[124:127], v[184:187]\n"
    "ds_read_b128 a[148:151], v21 offset:2048\n"
    "v_mfma_f32_16x16x32_bf16 v[180:183], v[136:139], a[140:143], v[180:183]\n"
    "ds_read_b128 v[96:99], v23 offset:0\n"
    "v_mfma_f32_16x16x32_bf16 v[176:179], v[136:139], a[136:139], v[176:179]\n"
    "v_mfma_f32_16x16x32_bf16 v[192:195], v[140:143], a[136:139], v[192:195]\n"
    "ds_read_b128 v[100:103], v23 offset:2048\n"
    "v_mfma_f32_16x16x32_bf16 v[196:199], v[140:143], a[140:143], v[196:199]\n"
    "ds_read_b64_tr_b16 v[108:109], v26 offset:6144\n"
    "v_mfma_f32_16x16x32_bf16 v[200:203], v[140:143], v[124:127], v[200:203]\n"
    "ds_read_b64_tr_b16 v[110:111], v26 offset:6656\n"
    "v_mfma_f32_16x16x32_bf16 v[204:207], v[140:143], v[128:131], v[204:207]\n"
    "ds_read_b64_tr_b16 v[112:113], v25 offset:7168\n"
    "v_mfma_f32_16x16x32_bf16 v[220:223], v[144:147], v[128:131], v[220:223]\n"
    "ds_read_b64_tr_b16 v[114:115], v25 offset:7680\n"
    "v_mfma_f32_16x16x32_bf16 v[216:219], v[144:147], v[124:127], v[216:219]\n"
    "v_mfma_f32_16x16x32_bf16 v[212:215], v[144:147], a[140:143], v[212:215]\n"
    "ds_read_b64_tr_b16 v[116:117], v26 offset:7168\n"
    "v_mfma_f32_16x16x32_bf16 v[208:211], v[144:147], a[136:139], v[208:211]\n"
    "ds_read_b64_tr_b16 v[118:119], v26 offset:7680\n"
    "v_mfma_f32_16x16x32_bf16 v[224:227], v[148:151], a[136:139], v[224:227]\n"
    "ds_read_b64_tr_b16 v[120:121], v25 offset:8192\n"
    "v_mfma_f32_16x16x32_bf16 v[228:231], v[148:151], a[140:143], v[228:231]\n"
    "ds_read_b64_tr_b16 v[122:123], v25 offset:8704\n"
    "v_mfma_f32_16x16x32_bf16 v[232:235], v[148:151], v[124:127], v[232:235]\n"
    "v_mfma_f32_16x16x32_bf16 v[236:239], v[148:151], v[128:131], v[236:239]\n"
    "s_waitcnt lgkmcnt(0)\n"
    "v_mfma_f32_16x16x32_bf16 v[160:163], v[104:107], a[144:147], v[160:163]\n"
    "s_waitcnt vmcnt(7)\n"
    "s_barrier\n"
    "v_mfma_f32_16x16x32_bf16 v[164:167], v[104:107], a[148:151], v[164:167]\n"
    "ds_read_b128 a[152:155], v22 offset:0\n"
    "s_mul_i32 s21, s62, 98304\n"
    "v_mfma_f32_16x16x32_bf16 v[168:171], v[104:107], v[96:99], v[168:171]\n"
    "s_add_u32 m0, s71, 114688\n"
    "v_mfma_f32_16x16x32_bf16 v[172:175], v[104:107], v[100:103], v[172:175]\n"
    "buffer_load_dwordx4 v29, s[32:35], s21 offen lds\n"
    "v_mfma_f32_16x16x32_bf16 v[188:191], v[108:111], v[100:103], v[188:191]\n"
    "ds_read_b64_tr_b16 v[132:133], v25 offset:9216\n"
    "v_mfma_f32_16x16x32_bf16 v[184:187], v[108:111], v[96:99], v[184:187]\n"
    "ds_read_b64_tr_b16 v[134:135], v25 offset:9728\n"
    "v_mfma_f32_16x16x32_bf16 v[180:183], v[108:111], a[148:151], v[180:183]\n"
    "ds_read_b128 a[156:159], v22 offset:2048\n"
    "v_mfma_f32_16x16x32_bf16 v[176:179], v[108:111], a[144:147], v[176:179]\n"
    "buffer_load_dwordx4 v30, s[32:35], s21 offen offset:1024 lds\n"
    "v_mfma_f32_16x16x32_bf16 v[192:195], v[112:115], a[144:147], v[192:195]\n"
    "ds_read_b128 v[124:127], v24 offset:0\n"
    "v_mfma_f32_16x16x32_bf16 v[196:199], v[112:115], a[148:151], v[196:199]\n"
    "ds_read_b128 v[128:131], v24 offset:2048\n"
    "v_mfma_f32_16x16x32_bf16 v[200:203], v[112:115], v[96:99], v[200:203]\n"
    "ds_read_b64_tr_b16 v[136:137], v26 offset:9216\n"
    "v_mfma_f32_16x16x32_bf16 v[204:207], v[112:115], v[100:103], v[204:207]\n"
    "ds_read_b64_tr_b16 v[138:139], v26 offset:9728\n"
    "v_mfma_f32_16x16x32_bf16 v[220:223], v[116:119], v[100:103], v[220:223]\n"
    "ds_read_b64_tr_b16 v[140:141], v25 offset:10240\n"
    "v_mfma_f32_16x16x32_bf16 v[216:219], v[116:119], v[96:99], v[216:219]\n"
    "ds_read_b64_tr_b16 v[142:143], v25 offset:10752\n"
    "buffer_load_dwordx4 v31, s[32:35], s21 offen offset:2048 lds\n"
    "v_mfma_f32_16x16x32_bf16 v[212:215], v[116:119], a[148:151], v[212:215]\n"
    "ds_read_b64_tr_b16 v[144:145], v26 offset:10240\n"
    "v_mfma_f32_16x16x32_bf16 v[208:211], v[116:119], a[144:147], v[208:211]\n"
    "ds_read_b64_tr_b16 v[146:147], v26 offset:10752\n"
    "v_mfma_f32_16x16x32_bf16 v[224:227], v[120:123], a[144:147], v[224:227]\n"
    "ds_read_b64_tr_b16 v[148:149], v25 offset:11264\n"
    "buffer_load_dwordx4 v32, s[32:35], s21 offen offset:3072 lds\n"
    "v_mfma_f32_16x16x32_bf16 v[228:231], v[120:123], a[148:151], v[228:231]\n"
    "ds_read_b64_tr_b16 v[150:151], v25 offset:11776\n"
    "s_add_u32 s62, s62, 4\n"
    "v_mfma_f32_16x16x32_bf16 v[232:235], v[120:123], v[96:99], v[232:235]\n"
    "s_sub_u32 s67, s62, 20\n"
    "s_cselect_b32 s62, s62, s67\n"
    "v_mfma_f32_16x16x32_bf16 v[236:239], v[120:123], v[100:103], v[236:239]\n"
    "s_waitcnt lgkmcnt(0)\n"
    "v_mfma_f32_16x16x32_bf16 v[160:163], v[132:135], a[152:155], v[160:163]\n"
    "s_barrier\n"
    "v_mfma_f32_16x16x32_bf16 v[164:167], v[132:135], a[156:159], v[164:167]\n"
    "v_mov_b32 v38, 0x3f80\n"
    "v_mfma_f32_16x16x32_bf16 v[168:171], v[132:135], v[124:127], v[168:171]\n"
    "v_mov_b32 v39, 0\n"
    "v_mfma_f32_16x16x32_bf16 v[172:175], v[132:135], v[128:131], v[172:175]\n"
    "v_mfma_f32_16x16x32_bf16 v[188:191], v[136:139], v[128:131], v[188:191]\n"
    "v_mfma_f32_16x16x32_bf16 v[184:187], v[136:139], v[124:127], v[184:187]\n"
    "ds_write_b64 v62, v[38:39]\n"
    "v_mfma_f32_16x16x32_bf16 v[180:183], v[136:139], a[156:159], v[180:183]\n"
    "v_mfma_f32_16x16x32_bf16 v[176:179], v[136:139], a[152:155], v[176:179]\n"
    "v_mfma_f32_16x16x32_bf16 v[192:195], v[140:143], a[152:155], v[192:195]\n"
    "v_mfma_f32_16x16x32_bf16 v[196:199], v[140:143], a[156:159], v[196:199]\n"
    "v_mfma_f32_16x16x32_bf16 v[200:203], v[140:143], v[124:127], v[200:203]\n"
    "v_mfma_f32_16x16x32_bf16 v[204:207], v[140:143], v[128:131], v[204:207]\n"
    "v_mfma_f32_16x16x32_bf16 v[220:223], v[144:147], v[128:131], v[220:223]\n"
    "ds_write_b64 v62, v[38:39] offset:23040\n"
    "v_mfma_f32_16x16x32_bf16 v[216:219], v[144:147], v[124:127], v[216:219]\n"
    "v_mfma_f32_16x16x32_bf16 v[212:215], v[144:147], a[156:159], v[212:215]\n"
    "v_mfma_f32_16x16x32_bf16 v[208:211], v[144:147], a[152:155], v[208:211]\n"
    "v_mfma_f32_16x16x32_bf16 v[224:227], v[148:151], a[152:155], v[224:227]\n"
    "v_mfma_f32_16x16x32_bf16 v[228:231], v[148:151], a[156:159], v[228:231]\n"
    "v_mfma_f32_16x16x32_bf16 v[232:235], v[148:151], v[124:127], v[232:235]\n"
    "v_mfma_f32_16x16x32_bf16 v[236:239], v[148:151], v[128:131], v[236:239]\n"
    "s_waitcnt lgkmcnt(0)\n"
    "s_nop 0\n"
    "v_pk_fma_f32 v[76:77], v[160:161], s[68:69], s[68:69] op_sel:[0,0,1] op_sel_hi:[1,0,1]\n"
    "v_pk_fma_f32 v[78:79], v[162:163], s[68:69], s[68:69] op_sel:[0,0,1] op_sel_hi:[1,0,1]\n"
    "v_pk_fma_f32 v[80:81], v[164:165], s[68:69], s[68:69] op_sel:[0,0,1] op_sel_hi:[1,0,1]\n"
    "v_pk_fma_f32 v[82:83], v[166:167], s[68:69], s[68:69] op_sel:[0,0,1] op_sel_hi:[1,0,1]\n"
    "v_pk_fma_f32 v[84:85], v[168:169], s[68:69], s[68:69] op_sel:[0,0,1] op_sel_hi:[1,0,1]\n"
    "v_pk_fma_f32 v[86:87], v[170:171], s[68:69], s[68:69] op_sel:[0,0,1] op_sel_hi:[1,0,1]\n"
    "v_pk_fma_f32 v[88:89], v[172:173], s[68:69], s[68:69] op_sel:[0,0,1] op_sel_hi:[1,0,1]\n"
    "v_pk_fma_f32 v[90:91], v[174:175], s[68:69], s[68:69] op_sel:[0,0,1] op_sel_hi:[1,0,1]\n"
    "v_cvt_pk_bf16_f32 v112, v208, v209\n"
    "v_cvt_pk_bf16_f32 v113, v210, v211\n"
    "v_cvt_pk_bf16_f32 v114, v212, v213\n"
    "v_cvt_pk_bf16_f32 v115, v214, v215\n"
    "v_cvt_pk_bf16_f32 v116, v216, v217\n"
    "v_cvt_pk_bf16_f32 v117, v218, v219\n"
    "v_cvt_pk_bf16_f32 v118, v220, v221\n"
    "v_cvt_pk_bf16_f32 v119, v222, v223\n"
    "v_cvt_pk_bf16_f32 v144, v224, v225\n"
    "v_cvt_pk_bf16_f32 v145, v226, v227\n"
    "v_cvt_pk_bf16_f32 v146, v228, v229\n"
    "v_cvt_pk_bf16_f32 v147, v230, v231\n"
    "v_cvt_pk_bf16_f32 v148, v232, v233\n"
    "v_cvt_pk_bf16_f32 v149, v234, v235\n"
    "v_cvt_pk_bf16_f32 v150, v236, v237\n"
    "v_cvt_pk_bf16_f32 v151, v238, v239\n"
    "v_exp_f32_e64 v76, v76 clamp\n"
    "v_exp_f32_e64 v77, v77 clamp\n"
    "ds_write_b64 v51, v[112:113] offset:0\n"
    "v_max_f32 v160, s70, v160\n"
    "v_max_f32 v161, s70, v161\n"
    "v_exp_f32_e64 v78, v78 clamp\n"
    "v_exp_f32_e64 v79, v79 clamp\n"
    "s_mul_i32 s21, s62, 98304\n"
    "s_add_u32 m0, s71, 131072\n"
    "v_max_f32 v162, s70, v162\n"
    "v_max_f32 v163, s70, v163\n"
    "v_exp_f32_e64 v80, v80 clamp\n"
    "ds_write_b64 v51, v[114:115] offset:5760\n"
    "v_exp_f32_e64 v81, v81 clamp\n"
    "v_max_f32 v164, s70, v164\n"
    "v_max_f32 v165, s70, v165\n"
    "buffer_load_dwordx4 v29, s[32:35], s21 offen lds\n"
    "v_exp_f32_e64 v82, v82 clamp\n"
    "v_exp_f32_e64 v83, v83 clamp\n"
    "v_max_f32 v166, s70, v166\n"
    "v_max_f32 v167, s70, v167\n"
    "ds_write_b64 v56, v[116:117] offset:0\n"
    "v_exp_f32_e64 v84, v84 clamp\n"
    "v_exp_f32_e64 v85, v85 clamp\n"
    "v_max_f32_e64 v168, s70, v168\n"
    "buffer_load_dwordx4 v30, s[32:35], s21 offen offset:1024 lds\n"
    "v_max_f32_e64 v169, s70, v169\n"
    "v_exp_f32_e64 v86, v86 clamp\n"
    "v_exp_f32_e64 v87, v87 clamp\n"
    "v_max_f32_e64 v170, s70, v170\n"
    "ds_write_b64 v56, v[118:119] offset:5760\n"
    "v_max_f32_e64 v171, s70, v171\n"
    "v_exp_f32_e64 v88, v88 clamp\n"
    "v_exp_f32_e64 v89, v89 clamp\n"
    "ds_write_b64 v52, v[144:145] offset:0\n"
    "v_max_f32 v172, s70, v172\n"
    "v_max_f32 v173, s70, v173\n"
    "v_exp_f32_e64 v90, v90 clamp\n"
    "v_exp_f32_e64 v91, v91 clamp\n"
    "buffer_load_dwordx4 v31, s[32:35], s21 offen offset:2048 lds\n"
    "v_max_f32 v174, s70, v174\n"
    "v_max_f32 v175, s70, v175\n"
    "v_pk_add_f32 v[160:161], v[160:161], v[76:77]\n"
    "ds_write_b64 v52, v[146:147] offset:5760\n"
    "v_pk_add_f32 v[162:163], v[162:163], v[78:79]\n"
    "v_pk_add_f32 v[164:165], v[164:165], v[80:81]\n"
    "v_pk_add_f32 v[166:167], v[166:167], v[82:83]\n"
    "buffer_load_dwordx4 v32, s[32:35], s21 offen offset:3072 lds\n"
    "v_pk_add_f32 v[168:169], v[168:169], v[84:85]\n"
    "v_pk_add_f32 v[170:171], v[170:171], v[86:87]\n"
    "v_pk_add_f32 v[172:173], v[172:173], v[88:89]\n"
    "v_pk_add_f32 v[174:175], v[174:175], v[90:91]\n"
    "ds_write_b64 v57, v[148:149] offset:0\n"
    "v_cvt_pk_bf16_f32 v96, v160, v161\n"
    "v_cvt_pk_bf16_f32 v97, v162, v163\n"
    "v_cvt_pk_bf16_f32 v98, v164, v165\n"
    "s_add_u32 s62, s62, 4\n"
    "s_sub_u32 s67, s62, 20\n"
    "s_cselect_b32 s62, s62, s67\n"
    "s_nop 0\n"
    "v_cvt_pk_bf16_f32 v99, v166, v167\n"
    "v_cvt_pk_bf16_f32 v100, v168, v169\n"
    "v_cvt_pk_bf16_f32 v101, v170, v171\n"
    "v_cvt_pk_bf16_f32 v102, v172, v173\n"
    "ds_write_b64 v57, v[150:151] offset:5760\n"
    "v_cvt_pk_bf16_f32 v103, v174, v175\n"
    "v_pk_fma_f32 v[76:77], v[176:177], s[68:69], s[68:69] op_sel:[0,0,1] op_sel_hi:[1,0,1]\n"
    "v_pk_fma_f32 v[78:79], v[178:179], s[68:69], s[68:69] op_sel:[0,0,1] op_sel_hi:[1,0,1]\n"
    "v_pk_fma_f32 v[80:81], v[180:181], s[68:69], s[68:69] op_sel:[0,0,1] op_sel_hi:[1,0,1]\n"
    "s_mul_i32 s21, s62, 98304\n"
    "s_add_u32 m0, s71, 147456\n"
    "v_pk_fma_f32 v[82:83], v[182:183], s[68:69], s[68:69] op_sel:[0,0,1] op_sel_hi:[1,0,1]\n"
    "v_pk_fma_f32 v[84:85], v[184:185], s[68:69], s[68:69] op_sel:[0,0,1] op_sel_hi:[1,0,1]\n"
    "v_pk_fma_f32 v[86:87], v[186:187], s[68:69], s[68:69] op_sel:[0,0,1] op_sel_hi:[1,0,1]\n"
    "v_pk_fma_f32 v[88:89], v[188:189], s[68:69], s[68:69] op_sel:[0,0,1] op_sel_hi:[1,0,1]\n"
    "v_pk_fma_f32 v[90:91], v[190:191], s[68:69], s[68:69] op_sel:[0,0,1] op_sel_hi:[1,0,1]\n"
    "v_exp_f32_e64 v76, v76 clamp\n"
    "ds_write_b64 v48, v[96:97] offset:0\n"
    "v_exp_f32_e64 v77, v77 clamp\n"
    "v_max_f32 v176, s70, v176\n"
    "v_max_f32 v177, s70, v177\n"
    "v_exp_f32_e64 v78, v78 clamp\n"
    "v_exp_f32_e64 v79, v79 clamp\n"
    "buffer_load_dwordx4 v29, s[32:35], s21 offen lds\n"
    "v_max_f32 v178, s70, v178\n"
    "v_max_f32 v179, s70, v179\n"
    "v_exp_f32_e64 v80, v80 clamp\n"
    "v_exp_f32_e64 v81, v81 clamp\n"
    "v_max_f32 v180, s70, v180\n"
    "v_max_f32 v181, s70, v181\n"
    "ds_write_b64 v48, v[98:99] offset:5760\n"
    "v_exp_f32_e64 v82, v82 clamp\n"
    "v_exp_f32_e64 v83, v83 clamp\n"
    "v_max_f32 v182, s70, v182\n"
    "v_max_f32 v183, s70, v183\n"
    "v_exp_f32_e64 v84, v84 clamp\n"
    "v_exp_f32_e64 v85, v85 clamp\n"
    "buffer_load_dwordx4 v30, s[32:35], s21 offen offset:1024 lds\n"
    "v_max_f32 v184, s70, v184\n"
    "v_max_f32 v185, s70, v185\n"
    "v_exp_f32_e64 v86, v86 clamp\n"
    "v_exp_f32_e64 v87, v87 clamp\n"
    "v_max_f32_e64 v186, s70, v186\n"
    "buffer_load_dwordx4 v31, s[32:35], s21 offen offset:2048 lds\n"
    "v_max_f32_e64 v187, s70, v187\n"
    "v_exp_f32_e64 v88, v88 clamp\n"
    "v_exp_f32_e64 v89, v89 clamp\n"
    "v_max_f32 v188, s70, v188\n"
    "v_max_f32 v189, s70, v189\n"
    "v_exp_f32_e64 v90, v90 clamp\n"
    "ds_write_b64 v53, v[100:101] offset:0\n"
    "v_exp_f32_e64 v91, v91 clamp\n"
    "v_max_f32 v190, s70, v190\n"
    "v_max_f32 v191, s70, v191\n"
    "v_pk_add_f32 v[176:177], v[176:177], v[76:77]\n"
    "v_pk_add_f32 v[178:179], v[178:179], v[78:79]\n"
    "buffer_load_dwordx4 v32, s[32:35], s21 offen offset:3072 lds\n"
    "v_pk_add_f32 v[180:181], v[180:181], v[80:81]\n"
    "v_pk_add_f32 v[182:183], v[182:183], v[82:83]\n"
    "v_pk_add_f32 v[184:185], v[184:185], v[84:85]\n"
    "v_pk_add_f32 v[186:187], v[186:187], v[86:87]\n"
    "v_pk_add_f32 v[188:189], v[188:189], v[88:89]\n"
    "v_pk_add_f32 v[190:191], v[190:191], v[90:91]\n"
    "ds_write_b64 v53, v[102:103] offset:5760\n"
    "v_cvt_pk_bf16_f32 v104, v176, v177\n"
    "v_cvt_pk_bf16_f32 v105, v178, v179\n"
    "v_cvt_pk_bf16_f32 v106, v180, v181\n"
    "v_cvt_pk_bf16_f32 v107, v182, v183\n"
    "v_cvt_pk_bf16_f32 v108, v184, v185\n"
    "v_cvt_pk_bf16_f32 v109, v186, v187\n"
    "s_add_u32 s62, s62, 4\n"
    "s_sub_u32 s67, s62, 20\n"
    "s_cselect_b32 s62, s62, s67\n"
    "v_cvt_pk_bf16_f32 v110, v188, v189\n"
    "v_cvt_pk_bf16_f32 v111, v190, v191\n"
    "s_cmp_eq_u32 s57, 0\n"
    "s_cbranch_scc0 L_kv_p4v_%=\n"
    "s_nop 0\n"
    "v_pk_fma_f32 v[76:77], v[192:193], s[68:69], s[68:69] op_sel:[0,0,1] op_sel_hi:[1,0,1]\n"
    "v_pk_fma_f32 v[78:79], v[194:195], s[68:69], s[68:69] op_sel:[0,0,1] op_sel_hi:[1,0,1]\n"
    "v_pk_fma_f32 v[80:81], v[196:197], s[68:69], s[68:69] op_sel:[0,0,1] op_sel_hi:[1,0,1]\n"
    "v_pk_fma_f32 v[82:83], v[198:199], s[68:69], s[68:69] op_sel:[0,0,1] op_sel_hi:[1,0,1]\n"
    "ds_write_b64 v49, v[104:105] offset:0\n"
    "v_exp_f32_e64 v76, v76 clamp\n"
    "v_exp_f32_e64 v77, v77 clamp\n"
    "v_max_f32 v192, s70, v192\n"
    "v_max_f32 v193, s70, v193\n"
    "v_exp_f32_e64 v78, v78 clamp\n"
    "v_exp_f32_e64 v79, v79 clamp\n"
    "v_max_f32_e64 v194, s70, v194\n"
    "ds_write_b64 v49, v[106:107] offset:5760\n"
    "v_max_f32_e64 v195, s70, v195\n"
    "v_exp_f32_e64 v80, v80 clamp\n"
    "v_exp_f32_e64 v81, v81 clamp\n"
    "v_max_f32 v196, s70, v196\n"
    "v_max_f32 v197, s70, v197\n"
    "v_exp_f32_e64 v82, v82 clamp\n"
    "v_exp_f32_e64 v83, v83 clamp\n"
    "ds_write_b64 v54, v[108:109] offset:0\n"
    "v_max_f32 v198, s70, v198\n"
    "v_max_f32 v199, s70, v199\n"
    "v_pk_add_f32 v[192:193], v[192:193], v[76:77]\n"
    "v_pk_add_f32 v[194:195], v[194:195], v[78:79]\n"
    "v_pk_add_f32 v[196:197], v[196:197], v[80:81]\n"
    "v_pk_add_f32 v[198:199], v[198:199], v[82:83]\n"
    "v_cvt_pk_bf16_f32 v120, v192, v193\n"
    "ds_write_b64 v54, v[110:111] offset:5760\n"
    "v_cvt_pk_bf16_f32 v121, v194, v195\n"
    "v_cvt_pk_bf16_f32 v122, v196, v197\n"
    "v_cvt_pk_bf16_f32 v123, v198, v199\n"
    "v_pk_fma_f32 v[84:85], v[200:201], s[68:69], s[68:69] op_sel:[0,0,1] op_sel_hi:[1,0,1]\n"
    "v_pk_fma_f32 v[86:87], v[202:203], s[68:69], s[68:69] op_sel:[0,0,1] op_sel_hi:[1,0,1]\n"
    "v_pk_fma_f32 v[88:89], v[204:205], s[68:69], s[68:69] op_sel:[0,0,1] op_sel_hi:[1,0,1]\n"
    "v_pk_fma_f32 v[90:91], v[206:207], s[68:69], s[68:69] op_sel:[0,0,1] op_sel_hi:[1,0,1]\n"
    "v_exp_f32_e64 v84, v84 clamp\n"
    "v_exp_f32_e64 v85, v85 clamp\n"
    "v_max_f32_e64 v200, s70, v200\n"
    "ds_write_b64 v50, v[120:121] offset:0\n"
    "v_max_f32_e64 v201, s70, v201\n"
    "v_exp_f32_e64 v86, v86 clamp\n"
    "v_exp_f32_e64 v87, v87 clamp\n"
    "v_max_f32 v202, s70, v202\n"
    "v_max_f32 v203, s70, v203\n"
    "v_exp_f32_e64 v88, v88 clamp\n"
    "v_exp_f32_e64 v89, v89 clamp\n"
    "v_max_f32 v204, s70, v204\n"
    "v_max_f32 v205, s70, v205\n"
    "v_exp_f32_e64 v90, v90 clamp\n"
    "v_exp_f32_e64 v91, v91 clamp\n"
    "v_max_f32 v206, s70, v206\n"
    "v_max_f32 v207, s70, v207\n"
    "v_pk_add_f32 v[200:201], v[200:201], v[84:85]\n"
    "ds_write_b64 v50, v[122:123] offset:5760\n"
    "v_pk_add_f32 v[202:203], v[202:203], v[86:87]\n"
    "v_pk_add_f32 v[204:205], v[204:205], v[88:89]\n"
    "v_pk_add_f32 v[206:207], v[206:207], v[90:91]\n"
    "v_cvt_pk_bf16_f32 v124, v200, v201\n"
    "v_cvt_pk_bf16_f32 v125, v202, v203\n"
    "v_cvt_pk_bf16_f32 v126, v204, v205\n"
    "v_cvt_pk_bf16_f32 v127, v206, v207\n"
    "ds_write_b64 v55, v[124:125] offset:0\n"
    "ds_write_b64 v55, v[126:127] offset:5760\n"
    "s_branch L_kv_p4done_%=\n"
    "L_kv_p4v_%=:\n"
    "s_nop 0\n"
    "v_cvt_pk_bf16_f32 v120, v192, v193\n"
    "v_cvt_pk_bf16_f32 v121, v194, v195\n"
    "v_cvt_pk_bf16_f32 v122, v196, v197\n"
    "v_cvt_pk_bf16_f32 v123, v198, v199\n"
    "v_cvt_pk_bf16_f32 v124, v200, v201\n"
    "v_cvt_pk_bf16_f32 v125, v202, v203\n"
    "v_cvt_pk_bf16_f32 v126, v204, v205\n"
    "v_cvt_pk_bf16_f32 v127, v206, v207\n"
    "ds_write_b64 v49, v[104:105] offset:0\n"
    "ds_write_b64 v49, v[106:107] offset:5760\n"
    "ds_write_b64 v54, v[108:109] offset:0\n"
    "ds_write_b64 v54, v[110:111] offset:5760\n"
    "ds_write_b64 v50, v[120:121] offset:0\n"
    "ds_write_b64 v50, v[122:123] offset:5760\n"
    "ds_write_b64 v55, v[124:125] offset:0\n"
    "ds_write_b64 v55, v[126:127] offset:5760\n"
    "L_kv_p4done_%=:\n"
    "s_waitcnt vmcnt(12)\n"
    "s_waitcnt vmcnt(16)\n"
    "v_cmp_ne_u32 vcc, s63, v63\n"
    "s_nop 1\n"
    "s_cmp_eq_u64 vcc, 0\n"
    "s_cbranch_scc0 L_f1kv_slow_%=\n"
    "L_f1kv_ok_%=:\n"
    "s_waitcnt lgkmcnt(0)\n"
    "s_barrier\n"
    "ds_read_b64_tr_b16 v[96:97], v58 offset:0\n"
    "ds_read_b64_tr_b16 v[98:99], v58 offset:5760\n"
    "ds_read_b64_tr_b16 v[100:101], v59 offset:0\n"
    "ds_read_b64_tr_b16 v[102:103], v59 offset:5760\n"
    "ds_read_b64_tr_b16 v[104:105], v60 offset:0\n"
    "ds_read_b64_tr_b16 v[106:107], v60 offset:5760\n"
    "ds_read_b64_tr_b16 v[108:109], v58 offset:11520\n"
    "ds_read_b64_tr_b16 v[110:111], v58 offset:17280\n"
    "ds_read_b64_tr_b16 v[112:113], v59 offset:11520\n"
    "ds_read_b64_tr_b16 v[114:115], v59 offset:17280\n"
    "ds_read_b64_tr_b16 v[116:117], v60 offset:11520\n"
    "ds_read_b64_tr_b16 v[118:119], v60 offset:17280\n"
    "ds_read_b64_tr_b16 v[120:121], v58 offset:23040\n"
    "ds_read_b64_tr_b16 v[122:123], v58 offset:28800\n"
    "s_mul_i32 s21, s62, 98304\n"
    "s_add_u32 m0, s71, 49152\n"
    "s_nop 0\n"
    "s_nop 0\n"
    "buffer_load_dwordx4 v29, s[32:35], s21 offen lds\n"
    "buffer_load_dwordx4 v30, s[32:35], s21 offen offset:1024 lds\n"
    "buffer_load_dwordx4 v31, s[32:35], s21 offen offset:2048 lds\n"
    "buffer_load_dwordx4 v32, s[32:35], s21 offen offset:3072 lds\n"
    "buffer_load_dwordx4 a[160:163], v1, s[36:39], s64 offen sc1\n"
    "buffer_load_dwordx4 a[164:167], v1, s[36:39], s64 offen offset:1024 sc1\n"
    "buffer_load_dwordx4 a[168:171], v1, s[36:39], s64 offen offset:2048 sc1\n"
    "buffer_load_dwordx4 a[172:175], v1, s[36:39], s64 offen offset:3072 sc1\n"
    "buffer_load_dwordx4 a[176:179], v247, s[36:39], s64 offen sc1\n"
    "s_add_u32 s64, s64, 5120\n"
    "s_cmp_eq_u32 s64, s72\n"
    "s_cselect_b32 s67, 20480, 0\n"
    "s_sub_u32 s64, s64, s67\n"
    "s_waitcnt lgkmcnt(8)\n"
    "v_mfma_f32_16x16x32_bf16 v[160:163], v[96:99], v[100:103], 0\n"
    "ds_read_b64_tr_b16 v[124:125], v59 offset:23040\n"
    "v_mfma_f32_16x16x32_bf16 v[164:167], v[96:99], v[104:107], 0\n"
    "ds_read_b64_tr_b16 v[126:127], v59 offset:28800\n"
    "v_mfma_f32_16x16x32_bf16 v[168:171], v[104:107], v[100:103], 0\n"
    "ds_read_b64_tr_b16 v[128:129], v60 offset:23040\n"
    "v_mfma_f32_16x16x32_bf16 v[172:175], v[104:107], v[104:107], 0\n"
    "ds_read_b64_tr_b16 v[130:131], v60 offset:28800\n"
    "ds_read_b64_tr_b16 v[132:133], v58 offset:34560\n"
    "s_waitcnt lgkmcnt(7)\n"
    "v_mfma_f32_16x16x32_bf16 v[160:163], v[108:111], v[112:115], v[160:163]\n"
    "ds_read_b64_tr_b16 v[134:135], v58 offset:40320\n"
    "v_mfma_f32_16x16x32_bf16 v[164:167], v[108:111], v[116:119], v[164:167]\n"
    "ds_read_b64_tr_b16 v[136:137], v59 offset:34560\n"
    "v_mfma_f32_16x16x32_bf16 v[168:171], v[116:119], v[112:115], v[168:171]\n"
    "ds_read_b64_tr_b16 v[138:139], v59 offset:40320\n"
    "v_mfma_f32_16x16x32_bf16 v[172:175], v[116:119], v[116:119], v[172:175]\n"
    "ds_read_b64_tr_b16 v[140:141], v60 offset:34560\n"
    "ds_read_b64_tr_b16 v[142:143], v60 offset:40320\n"
    "s_waitcnt lgkmcnt(6)\n"
    "v_mfma_f32_16x16x32_bf16 v[160:163], v[120:123], v[124:127], v[160:163]\n"
    "v_mfma_f32_16x16x32_bf16 v[164:167], v[120:123], v[128:131], v[164:167]\n"
    "v_mfma_f32_16x16x32_bf16 v[168:171], v[128:131], v[124:127], v[168:171]\n"
    "v_mfma_f32_16x16x32_bf16 v[172:175], v[128:131], v[128:131], v[172:175]\n"
    "s_waitcnt lgkmcnt(0)\n"
    "v_mfma_f32_16x16x32_bf16 v[160:163], v[132:135], v[136:139], v[160:163]\n"
    "v_mfma_f32_16x16x32_bf16 v[164:167], v[132:135], v[140:143], v[164:167]\n"
    "v_mfma_f32_16x16x32_bf16 v[168:171], v[140:143], v[136:139], v[168:171]\n"
    "v_mfma_f32_16x16x32_bf16 v[172:175], v[140:143], v[140:143], v[172:175]\n"
    "ds_read_b64_tr_b16 v[224:225], v34 offset:0\n"
    "ds_read_b64_tr_b16 v[226:227], v34 offset:512\n"
    "ds_read_b64_tr_b16 v[228:229], v35 offset:0\n"
    "ds_read_b64_tr_b16 v[230:231], v35 offset:512\n"
    "ds_read_b64_tr_b16 v[232:233], v36 offset:0\n"
    "ds_read_b64_tr_b16 v[234:235], v36 offset:512\n"
    "ds_read_b64_tr_b16 v[236:237], v37 offset:0\n"
    "ds_read_b64_tr_b16 v[238:239], v37 offset:512\n"
    "buffer_store_dwordx4 v[160:163], v1, s[36:39], s89 offen sc1\n"
    "s_mov_b64 exec, 0xffff\n"
    "buffer_store_dwordx4 v[168:171], v1, s[36:39], s89 offen offset:1024 sc1\n"
    "s_mov_b32 exec_lo, 0x01f001f0\n"
    "s_mov_b32 exec_hi, 0x01f001f0\n"
    "buffer_store_dwordx4 v[164:167], v61, s[36:39], s89 offen offset:1216 sc1\n"
    "s_mov_b64 exec, 0x1f0\n"
    "buffer_store_dwordx4 v[172:175], v61, s[36:39], s89 offen offset:1536 sc1\n"
    "s_mov_b64 exec, -1\n"
    "s_waitcnt lgkmcnt(0)\n"
    "s_waitcnt lgkmcnt(4)\n"
    "v_mfma_f32_16x16x32_bf16 v[192:195], v[224:227], a[0:3], 0\n"
    "ds_read_b64_tr_b16 v[144:145], v34 offset:4096\n"
    "ds_read_b64_tr_b16 v[146:147], v34 offset:4608\n"
    "v_mfma_f32_16x16x32_bf16 v[196:199], v[224:227], a[4:7], 0\n"
    "buffer_load_dwordx4 a[180:183], v1, s[36:39], s64 offen sc1\n"
    "v_mfma_f32_16x16x32_bf16 v[204:207], v[228:231], a[4:7], 0\n"
    "ds_read_b64_tr_b16 v[148:149], v35 offset:4096\n"
    "ds_read_b64_tr_b16 v[150:151], v35 offset:4608\n"
    "v_mfma_f32_16x16x32_bf16 v[200:203], v[228:231], a[0:3], 0\n"
    "buffer_load_dwordx4 a[184:187], v1, s[36:39], s64 offen offset:1024 sc1\n"
    "s_waitcnt lgkmcnt(4)\n"
    "v_mfma_f32_16x16x32_bf16 v[208:211], v[232:235], a[0:3], 0\n"
    "ds_read_b64_tr_b16 v[152:153], v36 offset:4096\n"
    "ds_read_b64_tr_b16 v[154:155], v36 offset:4608\n"
    "v_mfma_f32_16x16x32_bf16 v[212:215], v[232:235], a[4:7], 0\n"
    "buffer_load_dwordx4 a[188:191], v1, s[36:39], s64 offen offset:2048 sc1\n"
    "v_mfma_f32_16x16x32_bf16 v[220:223], v[236:239], a[4:7], 0\n"
    "ds_read_b64_tr_b16 v[156:157], v37 offset:4096\n"
    "ds_read_b64_tr_b16 v[158:159], v37 offset:4608\n"
    "v_mfma_f32_16x16x32_bf16 v[216:219], v[236:239], a[0:3], 0\n"
    "buffer_load_dwordx4 a[192:195], v1, s[36:39], s64 offen offset:3072 sc1\n"
    "s_waitcnt lgkmcnt(4)\n"
    "v_mfma_f32_16x16x32_bf16 v[192:195], v[144:147], a[8:11], v[192:195]\n"
    "ds_read_b64_tr_b16 v[224:225], v34 offset:8192\n"
    "ds_read_b64_tr_b16 v[226:227], v34 offset:8704\n"
    "v_mfma_f32_16x16x32_bf16 v[196:199], v[144:147], a[12:15], v[196:199]\n"
    "buffer_load_dwordx4 a[196:199], v247, s[36:39], s64 offen sc1\n"
    "v_mfma_f32_16x16x32_bf16 v[204:207], v[148:151], a[12:15], v[204:207]\n"
    "ds_read_b64_tr_b16 v[228:229], v35 offset:8192\n"
    "s_add_u32 s64, s64, 5120\n"
    "v_mfma_f32_16x16x32_bf16 v[200:203], v[148:151], a[8:11], v[200:203]\n"
    "ds_read_b64_tr_b16 v[230:231], v35 offset:8704\n"
    "s_waitcnt lgkmcnt(4)\n"
    "v_mfma_f32_16x16x32_bf16 v[208:211], v[152:155], a[8:11], v[208:211]\n"
    "ds_read_b64_tr_b16 v[232:233], v36 offset:8192\n"
    "s_cmp_eq_u32 s64, s72\n"
    "v_mfma_f32_16x16x32_bf16 v[212:215], v[152:155], a[12:15], v[212:215]\n"
    "ds_read_b64_tr_b16 v[234:235], v36 offset:8704\n"
    "v_mfma_f32_16x16x32_bf16 v[220:223], v[156:159], a[12:15], v[220:223]\n"
    "ds_read_b64_tr_b16 v[236:237], v37 offset:8192\n"
    "s_cselect_b32 s67, 20480, 0\n"
    "s_sub_u32 s64, s64, s67\n"
    "v_mfma_f32_16x16x32_bf16 v[216:219], v[156:159], a[8:11], v[216:219]\n"
    "ds_read_b64_tr_b16 v[238:239], v37 offset:8704\n"
    "s_waitcnt lgkmcnt(4)\n"
    "v_mfma_f32_16x16x32_bf16 v[192:195], v[224:227], a[16:19], v[192:195]\n"
    "ds_read_b64_tr_b16 v[144:145], v34 offset:12288\n"
    "v_mfma_f32_16x16x32_bf16 v[196:199], v[224:227], a[20:23], v[196:199]\n"
    "ds_read_b64_tr_b16 v[146:147], v34 offset:12800\n"
    "v_mfma_f32_16x16x32_bf16 v[204:207], v[228:231], a[20:23], v[204:207]\n"
    "ds_read_b64_tr_b16 v[148:149], v35 offset:12288\n"
    "ds_read_b64_tr_b16 v[150:151], v35 offset:12800\n"
    "v_mfma_f32_16x16x32_bf16 v[200:203], v[228:231], a[16:19], v[200:203]\n"
    "buffer_load_dwordx4 a[200:203], v1, s[36:39], s64 offen sc1\n"
    "s_waitcnt lgkmcnt(4)\n"
    "v_mfma_f32_16x16x32_bf16 v[208:211], v[232:235], a[16:19], v[208:211]\n"
    "ds_read_b64_tr_b16 v[152:153], v36 offset:12288\n"
    "ds_read_b64_tr_b16 v[154:155], v36 offset:12800\n"
    "v_mfma_f32_16x16x32_bf16 v[212:215], v[232:235], a[20:23], v[212:215]\n"
    "buffer_load_dwordx4 a[204:207], v1, s[36:39], s64 offen offset:1024 sc1\n"
    "v_mfma_f32_16x16x32_bf16 v[220:223], v[236:239], a[20:23], v[220:223]\n"
    "ds_read_b64_tr_b16 v[156:157], v37 offset:12288\n"
    "ds_read_b64_tr_b16 v[158:159], v37 offset:12800\n"
    "v_mfma_f32_16x16x32_bf16 v[216:219], v[236:239], a[16:19], v[216:219]\n"
    "buffer_load_dwordx4 a[208:211], v1, s[36:39], s64 offen offset:2048 sc1\n"
    "s_waitcnt lgkmcnt(4)\n"
    "v_mfma_f32_16x16x32_bf16 v[192:195], v[144:147], a[24:27], v[192:195]\n"
    "s_waitcnt vmcnt(17)\n"
    "s_barrier\n"
    "ds_read_b64_tr_b16 v[224:225], v34 offset:16384\n"
    "v_mfma_f32_16x16x32_bf16 v[196:199], v[144:147], a[28:31], v[196:199]\n"
    "ds_read_b64_tr_b16 v[226:227], v34 offset:16896\n"
    "v_mfma_f32_16x16x32_bf16 v[204:207], v[148:151], a[28:31], v[204:207]\n"
    "ds_read_b64_tr_b16 v[228:229], v35 offset:16384\n"
    "ds_read_b64_tr_b16 v[230:231], v35 offset:16896\n"
    "v_mfma_f32_16x16x32_bf16 v[200:203], v[148:151], a[24:27], v[200:203]\n"
    "buffer_load_dwordx4 a[212:215], v1, s[36:39], s64 offen offset:3072 sc1\n"
    "s_waitcnt lgkmcnt(4)\n"
    "v_mfma_f32_16x16x32_bf16 v[208:211], v[152:155], a[24:27], v[208:211]\n"
    "ds_read_b64_tr_b16 v[232:233], v36 offset:16384\n"
    "ds_read_b64_tr_b16 v[234:235], v36 offset:16896\n"
    "v_mfma_f32_16x16x32_bf16 v[212:215], v[152:155], a[28:31], v[212:215]\n"
    "buffer_load_dwordx4 a[216:219], v247, s[36:39], s64 offen sc1\n"
    "v_mfma_f32_16x16x32_bf16 v[220:223], v[156:159], a[28:31], v[220:223]\n"
    "ds_read_b64_tr_b16 v[236:237], v37 offset:16384\n"
    "s_add_u32 s64, s64, 5120\n"
    "v_mfma_f32_16x16x32_bf16 v[216:219], v[156:159], a[24:27], v[216:219]\n"
    "ds_read_b64_tr_b16 v[238:239], v37 offset:16896\n"
    "s_waitcnt lgkmcnt(4)\n"
    "v_mfma_f32_16x16x32_bf16 v[192:195], v[224:227], a[32:35], v[192:195]\n"
    "ds_read_b64_tr_b16 v[144:145], v34 offset:20480\n"
    "s_cmp_eq_u32 s64, s72\n"
    "v_mfma_f32_16x16x32_bf16 v[196:199], v[224:227], a[36:39], v[196:199]\n"
    "ds_read_b64_tr_b16 v[146:147], v34 offset:20992\n"
    "v_mfma_f32_16x16x32_bf16 v[204:207], v[228:231], a[36:39], v[204:207]\n"
    "ds_read_b64_tr_b16 v[148:149], v35 offset:20480\n"
    "s_cselect_b32 s67, 20480, 0\n"
    "v_mfma_f32_16x16x32_bf16 v[200:203], v[228:231], a[32:35], v[200:203]\n"
    "ds_read_b64_tr_b16 v[150:151], v35 offset:20992\n"
    "s_waitcnt lgkmcnt(4)\n"
    "v_mfma_f32_16x16x32_bf16 v[208:211], v[232:235], a[32:35], v[208:211]\n"
    "ds_read_b64_tr_b16 v[152:153], v36 offset:20480\n"
    "s_sub_u32 s64, s64, s67\n"
    "v_mfma_f32_16x16x32_bf16 v[212:215], v[232:235], a[36:39], v[212:215]\n"
    "ds_read_b64_tr_b16 v[154:155], v36 offset:20992\n"
    "v_mfma_f32_16x16x32_bf16 v[220:223], v[236:239], a[36:39], v[220:223]\n"
    "ds_read_b64_tr_b16 v[156:157], v37 offset:20480\n"
    "ds_read_b64_tr_b16 v[158:159], v37 offset:20992\n"
    "v_mfma_f32_16x16x32_bf16 v[216:219], v[236:239], a[32:35], v[216:219]\n"
    "buffer_load_dwordx4 a[220:223], v1, s[36:39], s64 offen sc1\n"
    "buffer_load_dwordx4 a[224:227], v1, s[36:39], s64 offen offset:1024 sc1\n"
    "s_waitcnt lgkmcnt(4)\n"
    "v_mfma_f32_16x16x32_bf16 v[192:195], v[144:147], a[40:43], v[192:195]\n"
    "ds_read_b64_tr_b16 v[224:225], v34 offset:24576\n"
    "v_mfma_f32_16x16x32_bf16 v[196:199], v[144:147], a[44:47], v[196:199]\n"
    "ds_read_b64_tr_b16 v[226:227], v34 offset:25088\n"
    "v_mfma_f32_16x16x32_bf16 v[204:207], v[148:151], a[44:47], v[204:207]\n"
    "ds_read_b64_tr_b16 v[228:229], v35 offset:24576\n"
    "ds_read_b64_tr_b16 v[230:231], v35 offset:25088\n"
    "v_mfma_f32_16x16x32_bf16 v[200:203], v[148:151], a[40:43], v[200:203]\n"
    "buffer_load_dwordx4 a[228:231], v1, s[36:39], s64 offen offset:2048 sc1\n"
    "s_waitcnt lgkmcnt(4)\n"
    "v_mfma_f32_16x16x32_bf16 v[208:211], v[152:155], a[40:43], v[208:211]\n"
    "ds_read_b64_tr_b16 v[232:233], v36 offset:24576\n"
    "ds_read_b64_tr_b16 v[234:235], v36 offset:25088\n"
    "v_mfma_f32_16x16x32_bf16 v[212:215], v[152:155], a[44:47], v[212:215]\n"
    "buffer_load_dwordx4 a[232:235], v1, s[36:39], s64 offen offset:3072 sc1\n"
    "v_mfma_f32_16x16x32_bf16 v[220:223], v[156:159], a[44:47], v[220:223]\n"
    "ds_read_b64_tr_b16 v[236:237], v37 offset:24576\n"
    "ds_read_b64_tr_b16 v[238:239], v37 offset:25088\n"
    "v_mfma_f32_16x16x32_bf16 v[216:219], v[156:159], a[40:43], v[216:219]\n"
    "buffer_load_dwordx4 a[236:239], v247, s[36:39], s64 offen sc1\n"
    "s_waitcnt lgkmcnt(4)\n"
    "v_mfma_f32_16x16x32_bf16 v[192:195], v[224:227], a[48:51], v[192:195]\n"
    "ds_read_b64_tr_b16 v[144:145], v34 offset:28672\n"
    "s_sub_u32 s72, s72, 20480\n"
    "v_mfma_f32_16x16x32_bf16 v[196:199], v[224:227], a[52:55], v[196:199]\n"
    "ds_read_b64_tr_b16 v[146:147], v34 offset:29184\n"
    "v_mfma_f32_16x16x32_bf16 v[204:207], v[228:231], a[52:55], v[204:207]\n"
    "ds_read_b64_tr_b16 v[148:149], v35 offset:28672\n"
    "s_mul_i32 s64, s47, 5120\n"
    "v_mfma_f32_16x16x32_bf16 v[200:203], v[228:231], a[48:51], v[200:203]\n"
    "ds_read_b64_tr_b16 v[150:151], v35 offset:29184\n"
    "s_waitcnt lgkmcnt(4)\n"
    "v_mfma_f32_16x16x32_bf16 v[208:211], v[232:235], a[48:51], v[208:211]\n"
    "ds_read_b64_tr_b16 v[152:153], v36 offset:28672\n"
    "s_add_u32 s64, s64, s72\n"
    "v_mfma_f32_16x16x32_bf16 v[212:215], v[232:235], a[52:55], v[212:215]\n"
    "ds_read_b64_tr_b16 v[154:155], v36 offset:29184\n"
    "v_mfma_f32_16x16x32_bf16 v[220:223], v[236:239], a[52:55], v[220:223]\n"
    "ds_read_b64_tr_b16 v[156:157], v37 offset:28672\n"
    "ds_read_b64_tr_b16 v[158:159], v37 offset:29184\n"
    "v_mfma_f32_16x16x32_bf16 v[216:219], v[236:239], a[48:51], v[216:219]\n"
    "s_sub_u32 s64, s64, 40960\n"
    "buffer_load_dwordx4 a[0:3], v1, s[36:39], s64 offen sc1\n"
    "s_waitcnt lgkmcnt(4)\n"
    "v_mfma_f32_16x16x32_bf16 v[192:195], v[144:147], a[56:59], v[192:195]\n"
    "ds_read_b64_tr_b16 v[224:225], v34 offset:32768\n"
    "v_mfma_f32_16x16x32_bf16 v[196:199], v[144:147], a[60:63], v[196:199]\n"
    "ds_read_b64_tr_b16 v[226:227], v34 offset:33280\n"
    "v_mfma_f32_16x16x32_bf16 v[204:207], v[148:151], a[60:63], v[204:207]\n"
    "ds_read_b64_tr_b16 v[228:229], v35 offset:32768\n"
    "ds_read_b64_tr_b16 v[230:231], v35 offset:33280\n"
    "v_mfma_f32_16x16x32_bf16 v[200:203], v[148:151], a[56:59], v[200:203]\n"
    "buffer_load_dwordx4 a[4:7], v1, s[36:39], s64 offen offset:1024 sc1\n"
    "s_waitcnt lgkmcnt(4)\n"
    "v_mfma_f32_16x16x32_bf16 v[208:211], v[152:155], a[56:59], v[208:211]\n"
    "ds_read_b64_tr_b16 v[232:233], v36 offset:32768\n"
    "ds_read_b64_tr_b16 v[234:235], v36 offset:33280\n"
    "v_mfma_f32_16x16x32_bf16 v[212:215], v[152:155], a[60:63], v[212:215]\n"
    "buffer_load_dwordx4 a[8:11], v1, s[36:39], s64 offen offset:2048 sc1\n"
    "v_mfma_f32_16x16x32_bf16 v[220:223], v[156:159], a[60:63], v[220:223]\n"
    "ds_read_b64_tr_b16 v[236:237], v37 offset:32768\n"
    "ds_read_b64_tr_b16 v[238:239], v37 offset:33280\n"
    "v_mfma_f32_16x16x32_bf16 v[216:219], v[156:159], a[56:59], v[216:219]\n"
    "buffer_load_dwordx4 a[12:15], v1, s[36:39], s64 offen offset:3072 sc1\n"
    "s_waitcnt lgkmcnt(4)\n"
    "v_mfma_f32_16x16x32_bf16 v[192:195], v[224:227], a[64:67], v[192:195]\n"
    "ds_read_b64_tr_b16 v[144:145], v34 offset:36864\n"
    "ds_read_b64_tr_b16 v[146:147], v34 offset:37376\n"
    "v_mfma_f32_16x16x32_bf16 v[196:199], v[224:227], a[68:71], v[196:199]\n"
    "buffer_load_dwordx4 a[16:19], v247, s[36:39], s64 offen sc1\n"
    "v_mfma_f32_16x16x32_bf16 v[204:207], v[228:231], a[68:71], v[204:207]\n"
    "ds_read_b64_tr_b16 v[148:149], v35 offset:36864\n"
    "s_add_u32 s64, s64, 5120\n"
    "v_mfma_f32_16x16x32_bf16 v[200:203], v[228:231], a[64:67], v[200:203]\n"
    "ds_read_b64_tr_b16 v[150:151], v35 offset:37376\n"
    "s_waitcnt lgkmcnt(4)\n"
    "v_mfma_f32_16x16x32_bf16 v[208:211], v[232:235], a[64:67], v[208:211]\n"
    "ds_read_b64_tr_b16 v[152:153], v36 offset:36864\n"
    "s_cmp_eq_u32 s64, s72\n"
    "v_mfma_f32_16x16x32_bf16 v[212:215], v[232:235], a[68:71], v[212:215]\n"
    "ds_read_b64_tr_b16 v[154:155], v36 offset:37376\n"
    "v_mfma_f32_16x16x32_bf16 v[220:223], v[236:239], a[68:71], v[220:223]\n"
    "ds_read_b64_tr_b16 v[156:157], v37 offset:36864\n"
    "s_cselect_b32 s67, 40960, 0\n"
    "s_sub_u32 s64, s64, s67\n"
    "v_mfma_f32_16x16x32_bf16 v[216:219], v[236:239], a[64:67], v[216:219]\n"
    "ds_read_b64_tr_b16 v[158:159], v37 offset:37376\n"
    "s_waitcnt lgkmcnt(4)\n"
    "v_mfma_f32_16x16x32_bf16 v[192:195], v[144:147], a[72:75], v[192:195]\n"
    "ds_read_b64_tr_b16 v[224:225], v34 offset:40960\n"
    "v_mfma_f32_16x16x32_bf16 v[196:199], v[144:147], a[76:79], v[196:199]\n"
    "ds_read_b64_tr_b16 v[226:227], v34 offset:41472\n"
    "v_mfma_f32_16x16x32_bf16 v[204:207], v[148:151], a[76:79], v[204:207]\n"
    "ds_read_b64_tr_b16 v[228:229], v35 offset:40960\n"
    "ds_read_b64_tr_b16 v[230:231], v35 offset:41472\n"
    "v_mfma_f32_16x16x32_bf16 v[200:203], v[148:151], a[72:75], v[200:203]\n"
    "buffer_load_dwordx4 a[20:23], v1, s[36:39], s64 offen sc1\n"
    "s_waitcnt lgkmcnt(4)\n"
    "v_mfma_f32_16x16x32_bf16 v[208:211], v[152:155], a[72:75], v[208:211]\n"
    "ds_read_b64_tr_b16 v[232:233], v36 offset:40960\n"
    "ds_read_b64_tr_b16 v[234:235], v36 offset:41472\n"
    "v_mfma_f32_16x16x32_bf16 v[212:215], v[152:155], a[76:79], v[212:215]\n"
    "buffer_load_dwordx4 a[24:27], v1, s[36:39], s64 offen offset:1024 sc1\n"
    "v_mfma_f32_16x16x32_bf16 v[220:223], v[156:159], a[76:79], v[220:223]\n"
    "ds_read_b64_tr_b16 v[236:237], v37 offset:40960\n"
    "ds_read_b64_tr_b16 v[238:239], v37 offset:41472\n"
    "v_mfma_f32_16x16x32_bf16 v[216:219], v[156:159], a[72:75], v[216:219]\n"
    "buffer_load_dwordx4 a[28:31], v1, s[36:39], s64 offen offset:2048 sc1\n"
    "s_waitcnt lgkmcnt(4)\n"
    "v_mfma_f32_16x16x32_bf16 v[192:195], v[224:227], a[80:83], v[192:195]\n"
    "ds_read_b64_tr_b16 v[144:145], v34 offset:45056\n"
    "v_mfma_f32_16x16x32_bf16 v[196:199], v[224:227], a[84:87], v[196:199]\n"
    "ds_read_b64_tr_b16 v[146:147], v34 offset:45568\n"
    "v_mfma_f32_16x16x32_bf16 v[204:207], v[228:231], a[84:87], v[204:207]\n"
    "ds_read_b64_tr_b16 v[148:149], v35 offset:45056\n"
    "ds_read_b64_tr_b16 v[150:151], v35 offset:45568\n"
    "v_mfma_f32_16x16x32_bf16 v[200:203], v[228:231], a[80:83], v[200:203]\n"
    "buffer_load_dwordx4 a[32:35], v1, s[36:39], s64 offen offset:3072 sc1\n"
    "s_waitcnt lgkmcnt(4)\n"
    "v_mfma_f32_16x16x32_bf16 v[208:211], v[232:235], a[80:83], v[208:211]\n"
    "ds_read_b64_tr_b16 v[152:153], v36 offset:45056\n"
    "ds_read_b64_tr_b16 v[154:155], v36 offset:45568\n"
    "v_mfma_f32_16x16x32_bf16 v[212:215], v[232:235], a[84:87], v[212:215]\n"
    "buffer_load_dwordx4 a[36:39], v247, s[36:39], s64 offen sc1\n"
    "v_mfma_f32_16x16x32_bf16 v[220:223], v[236:239], a[84:87], v[220:223]\n"
    "ds_read_b64_tr_b16 v[156:157], v37 offset:45056\n"
    "s_add_u32 s64, s64, 5120\n"
    "v_mfma_f32_16x16x32_bf16 v[216:219], v[236:239], a[80:83], v[216:219]\n"
    "ds_read_b64_tr_b16 v[158:159], v37 offset:45568\n"
    "s_waitcnt lgkmcnt(4)\n"
    "v_mfma_f32_16x16x32_bf16 v[192:195], v[144:147], a[88:91], v[192:195]\n"
    "buffer_load_dwordx4 v[76:79], v12, s[36:39], s19 offen sc1\n"
    "buffer_load_dwordx4 v[80:83], v12, s[36:39], s20 offen sc1\n"
    "ds_read_b64_tr_b16 v[224:225], v34 offset:49152\n"
    "s_cmp_eq_u32 s64, s72\n"
    "v_mfma_f32_16x16x32_bf16 v[196:199], v[144:147], a[92:95], v[196:199]\n"
    "ds_read_b64_tr_b16 v[226:227], v34 offset:49664\n"
    "v_mfma_f32_16x16x32_bf16 v[204:207], v[148:151], a[92:95], v[204:207]\n"
    "ds_read_b64_tr_b16 v[228:229], v35 offset:49152\n"
    "s_cselect_b32 s67, 40960, 0\n"
    "v_mfma_f32_16x16x32_bf16 v[200:203], v[148:151], a[88:91], v[200:203]\n"
    "ds_read_b64_tr_b16 v[230:231], v35 offset:49664\n"
    "s_waitcnt lgkmcnt(4)\n"
    "v_mfma_f32_16x16x32_bf16 v[208:211], v[152:155], a[88:91], v[208:211]\n"
    "ds_read_b64_tr_b16 v[232:233], v36 offset:49152\n"
    "s_sub_u32 s64, s64, s67\n"
    "v_mfma_f32_16x16x32_bf16 v[212:215], v[152:155], a[92:95], v[212:215]\n"
    "ds_read_b64_tr_b16 v[234:235], v36 offset:49664\n"
    "v_mfma_f32_16x16x32_bf16 v[220:223], v[156:159], a[92:95], v[220:223]\n"
    "ds_read_b64_tr_b16 v[236:237], v37 offset:49152\n"
    "ds_read_b64_tr_b16 v[238:239], v37 offset:49664\n"
    "v_mfma_f32_16x16x32_bf16 v[216:219], v[156:159], a[88:91], v[216:219]\n"
    "buffer_load_dwordx4 a[40:43], v1, s[36:39], s64 offen sc1\n"
    "buffer_load_dwordx4 a[44:47], v1, s[36:39], s64 offen offset:1024 sc1\n"
    "s_waitcnt lgkmcnt(4)\n"
    "v_mfma_f32_16x16x32_bf16 v[192:195], v[224:227], a[96:99], v[192:195]\n"
    "ds_read_b64_tr_b16 v[144:145], v34 offset:53248\n"
    "v_mfma_f32_16x16x32_bf16 v[196:199], v[224:227], a[100:103], v[196:199]\n"
    "ds_read_b64_tr_b16 v[146:147], v34 offset:53760\n"
    "v_mfma_f32_16x16x32_bf16 v[204:207], v[228:231], a[100:103], v[204:207]\n"
    "ds_read_b64_tr_b16 v[148:149], v35 offset:53248\n"
    "ds_read_b64_tr_b16 v[150:151], v35 offset:53760\n"
    "v_mfma_f32_16x16x32_bf16 v[200:203], v[228:231], a[96:99], v[200:203]\n"
    "buffer_load_dwordx4 a[48:51], v1, s[36:39], s64 offen offset:2048 sc1\n"
    "s_waitcnt lgkmcnt(4)\n"
    "v_mfma_f32_16x16x32_bf16 v[208:211], v[232:235], a[96:99], v[208:211]\n"
    "ds_read_b64_tr_b16 v[152:153], v36 offset:53248\n"
    "ds_read_b64_tr_b16 v[154:155], v36 offset:53760\n"
    "v_mfma_f32_16x16x32_bf16 v[212:215], v[232:235], a[100:103], v[212:215]\n"
    "buffer_load_dwordx4 a[52:55], v1, s[36:39], s64 offen offset:3072 sc1\n"
    "v_mfma_f32_16x16x32_bf16 v[220:223], v[236:239], a[100:103], v[220:223]\n"
    "ds_read_b64_tr_b16 v[156:157], v37 offset:53248\n"
    "ds_read_b64_tr_b16 v[158:159], v37 offset:53760\n"
    "v_mfma_f32_16x16x32_bf16 v[216:219], v[236:239], a[96:99], v[216:219]\n"
    "buffer_load_dwordx4 a[56:59], v247, s[36:39], s64 offen sc1\n"
    "s_waitcnt lgkmcnt(4)\n"
    "v_mfma_f32_16x16x32_bf16 v[192:195], v[144:147], a[104:107], v[192:195]\n"
    "ds_read_b64_tr_b16 v[224:225], v34 offset:57344\n"
    "s_add_u32 s64, s64, 5120\n"
    "v_mfma_f32_16x16x32_bf16 v[196:199], v[144:147], a[108:111], v[196:199]\n"
    "ds_read_b64_tr_b16 v[226:227], v34 offset:57856\n"
    "v_mfma_f32_16x16x32_bf16 v[204:207], v[148:151], a[108:111], v[204:207]\n"
    "ds_read_b64_tr_b16 v[228:229], v35 offset:57344\n"
    "s_cmp_eq_u32 s64, s72\n"
    "v_mfma_f32_16x16x32_bf16 v[200:203], v[148:151], a[104:107], v[200:203]\n"
    "ds_read_b64_tr_b16 v[230:231], v35 offset:57856\n"
    "s_waitcnt lgkmcnt(4)\n"
    "v_mfma_f32_16x16x32_bf16 v[208:211], v[152:155], a[104:107], v[208:211]\n"
    "ds_read_b64_tr_b16 v[232:233], v36 offset:57344\n"
    "s_cselect_b32 s67, 40960, 0\n"
    "v_mfma_f32_16x16x32_bf16 v[212:215], v[152:155], a[108:111], v[212:215]\n"
    "ds_read_b64_tr_b16 v[234:235], v36 offset:57856\n"
    "v_mfma_f32_16x16x32_bf16 v[220:223], v[156:159], a[108:111], v[220:223]\n"
    "ds_read_b64_tr_b16 v[236:237], v37 offset:57344\n"
    "ds_read_b64_tr_b16 v[238:239], v37 offset:57856\n"
    "v_mfma_f32_16x16x32_bf16 v[216:219], v[156:159], a[104:107], v[216:219]\n"
    "s_sub_u32 s64, s64, s67\n"
    "buffer_load_dwordx4 a[60:63], v1, s[36:39], s64 offen sc1\n"
    "s_waitcnt lgkmcnt(4)\n"
    "v_mfma_f32_16x16x32_bf16 v[192:195], v[224:227], a[112:115], v[192:195]\n"
    "ds_read_b64_tr_b16 v[144:145], v34 offset:61440\n"
    "v_mfma_f32_16x16x32_bf16 v[196:199], v[224:227], a[116:119], v[196:199]\n"
    "ds_read_b64_tr_b16 v[146:147], v34 offset:61952\n"
    "v_mfma_f32_16x16x32_bf16 v[204:207], v[228:231], a[116:119], v[204:207]\n"
    "ds_read_b64_tr_b16 v[148:149], v35 offset:61440\n"
    "ds_read_b64_tr_b16 v[150:151], v35 offset:61952\n"
    "v_mfma_f32_16x16x32_bf16 v[200:203], v[228:231], a[112:115], v[200:203]\n"
    "buffer_load_dwordx4 a[64:67], v1, s[36:39], s64 offen offset:1024 sc1\n"
    "s_waitcnt lgkmcnt(4)\n"
    "v_mfma_f32_16x16x32_bf16 v[208:211], v[232:235], a[112:115], v[208:211]\n"
    "ds_read_b64_tr_b16 v[152:153], v36 offset:61440\n"
    "ds_read_b64_tr_b16 v[154:155], v36 offset:61952\n"
    "v_mfma_f32_16x16x32_bf16 v[212:215], v[232:235], a[116:119], v[212:215]\n"
    "buffer_load_dwordx4 a[68:71], v1, s[36:39], s64 offen offset:2048 sc1\n"
    "v_mfma_f32_16x16x32_bf16 v[220:223], v[236:239], a[116:119], v[220:223]\n"
    "ds_read_b64_tr_b16 v[156:157], v37 offset:61440\n"
    "ds_read_b64_tr_b16 v[158:159], v37 offset:61952\n"
    "v_mfma_f32_16x16x32_bf16 v[216:219], v[236:239], a[112:115], v[216:219]\n"
    "buffer_load_dwordx4 a[72:75], v1, s[36:39], s64 offen offset:3072 sc1\n"
    "s_waitcnt lgkmcnt(4)\n"
    "v_mfma_f32_16x16x32_bf16 v[192:195], v[144:147], a[120:123], v[192:195]\n"
    "s_waitcnt vmcnt(9)\n"
    "s_nop 0\n"
    "v_max3_u32 v84, v76, v77, v78\n"
    "v_max3_u32 v85, v79, v80, v81\n"
    "v_max3_u32 v84, v84, v82, v83\n"
    "v_max_u32 v84, v84, v85\n"
    "v_cmp_eq_u32 vcc, -1, v84\n"
    "s_nop 1\n"
    "s_and_b64 vcc, vcc, s[22:23]\n"
    "s_cbranch_scc1 L_ka_f_%=\n"
    "v_add_f32 v76, v76, v80\n"
    "v_add_f32 v77, v77, v81\n"
    "v_add_f32 v78, v78, v82\n"
    "v_add_f32 v79, v79, v83\n"
    "s_nop 0\n"
    "v_permlane32_swap_b32 v76, v78\n"
    "v_permlane32_swap_b32 v77, v79\n"
    "v_add_f32 v76, v76, v78\n"
    "v_add_f32 v77, v77, v79\n"
    "v_mov_b32 v78, v76\n"
    "v_mov_b32 v79, v77\n"
    "s_nop 0\n"
    "v_permlane16_swap_b32 v76, v78\n"
    "v_permlane16_swap_b32 v77, v79\n"
    "v_add_f32 v76, v76, v78\n"
    "v_add_f32 v77, v77, v79\n"
    "s_nop 0\n"
    "v_add_f32_dpp v76, v76, v76 row_ror:8 row_mask:0xf bank_mask:0xf\n"
    "v_add_f32_dpp v77, v77, v77 row_ror:8 row_mask:0xf bank_mask:0xf\n"
    "v_cvt_pk_bf16_f32 v76, v76, v77\n"
    "v_min_u32_e64 v76, v76, -2\n"
    "buffer_store_dword v76, v13, s[36:39], s88 offen sc1\n"
    "s_mov_b64 s[22:23], 0\n"
    "L_ka_r0_%=:\n"
    "s_nop 0\n"
    "v_add_u32 v34, 4294918144, v34\n"
    "v_add_u32 v35, 4294918144, v35\n"
    "v_add_u32 v36, 4294918144, v36\n"
    "v_add_u32 v37, 4294918144, v37\n"
    "ds_read_b64_tr_b16 v[224:225], v34 offset:0\n"
    "ds_read_b64_tr_b16 v[226:227], v34 offset:512\n"
    "v_mfma_f32_16x16x32_bf16 v[196:199], v[144:147], a[124:127], v[196:199]\n"
    "buffer_load_dwordx4 a[76:79], v247, s[36:39], s64 offen sc1\n"
    "v_mfma_f32_16x16x32_bf16 v[204:207], v[148:151], a[124:127], v[204:207]\n"
    "ds_read_b64_tr_b16 v[228:229], v35 offset:0\n"
    "s_add_u32 s64, s64, 5120\n"
    "v_mfma_f32_16x16x32_bf16 v[200:203], v[148:151], a[120:123], v[200:203]\n"
    "ds_read_b64_tr_b16 v[230:231], v35 offset:512\n"
    "s_waitcnt lgkmcnt(4)\n"
    "v_mfma_f32_16x16x32_bf16 v[208:211], v[152:155], a[120:123], v[208:211]\n"
    "ds_read_b64_tr_b16 v[232:233], v36 offset:0\n"
    "s_cmp_eq_u32 s64, s72\n"
    "v_mfma_f32_16x16x32_bf16 v[212:215], v[152:155], a[124:127], v[212:215]\n"
    "ds_read_b64_tr_b16 v[234:235], v36 offset:512\n"
    "v_mfma_f32_16x16x32_bf16 v[220:223], v[156:159], a[124:127], v[220:223]\n"
    "ds_read_b64_tr_b16 v[236:237], v37 offset:0\n"
    "s_cselect_b32 s67, 40960, 0\n"
    "s_sub_u32 s64, s64, s67\n"
    "v_mfma_f32_16x16x32_bf16 v[216:219], v[156:159], a[120:123], v[216:219]\n"
    "ds_read_b64_tr_b16 v[238:239], v37 offset:512\n"
    "s_waitcnt lgkmcnt(4)\n"
    "v_mfma_f32_16x16x32_bf16 v[192:195], v[224:227], a[128:131], v[192:195]\n"
    "ds_read_b64_tr_b16 v[144:145], v34 offset:4096\n"
    "v_mfma_f32_16x16x32_bf16 v[196:199], v[224:227], a[132:135], v[196:199]\n"
    "ds_read_b64_tr_b16 v[146:147], v34 offset:4608\n"
    "v_mfma_f32_16x16x32_bf16 v[204:207], v[228:231], a[132:135], v[204:207]\n"
    "ds_read_b64_tr_b16 v[148:149], v35 offset:4096\n"
    "ds_read_b64_tr_b16 v[150:151], v35 offset:4608\n"
    "v_mfma_f32_16x16x32_bf16 v[200:203], v[228:231], a[128:131], v[200:203]\n"
    "buffer_load_dwordx4 a[80:83], v1, s[36:39], s64 offen sc1\n"
    "s_waitcnt lgkmcnt(4)\n"
    "v_mfma_f32_16x16x32_bf16 v[208:211], v[232:235], a[128:131], v[208:211]\n"
    "ds_read_b64_tr_b16 v[152:153], v36 offset:4096\n"
    "ds_read_b64_tr_b16 v[154:155], v36 offset:4608\n"
    "v_mfma_f32_16x16x32_bf16 v[212:215], v[232:235], a[132:135], v[212:215]\n"
    "buffer_load_dwordx4 a[84:87], v1, s[36:39], s64 offen offset:1024 sc1\n"
    "v_mfma_f32_16x16x32_bf16 v[220:223], v[236:239], a[132:135], v[220:223]\n"
    "ds_read_b64_tr_b16 v[156:157], v37 offset:4096\n"
    "ds_read_b64_tr_b16 v[158:159], v37 offset:4608\n"
    "v_mfma_f32_16x16x32_bf16 v[216:219], v[236:239], a[128:131], v[216:219]\n"
    "buffer_load_dwordx4 a[88:91], v1, s[36:39], s64 offen offset:2048 sc1\n"
    "s_waitcnt lgkmcnt(4)\n"
    "v_mfma_f32_16x16x32_bf16 v[192:195], v[144:147], a[136:139], v[192:195]\n"
    "ds_read_b64_tr_b16 v[224:225], v34 offset:8192\n"
    "v_mfma_f32_16x16x32_bf16 v[196:199], v[144:147], a[140:143], v[196:199]\n"
    "ds_read_b64_tr_b16 v[226:227], v34 offset:8704\n"
    "v_mfma_f32_16x16x32_bf16 v[204:207], v[148:151], a[140:143], v[204:207]\n"
    "ds_read_b64_tr_b16 v[228:229], v35 offset:8192\n"
    "ds_read_b64_tr_b16 v[230:231], v35 offset:8704\n"
    "v_mfma_f32_16x16x32_bf16 v[200:203], v[148:151], a[136:139], v[200:203]\n"
    "buffer_load_dwordx4 a[92:95], v1, s[36:39], s64 offen offset:3072 sc1\n"
    "s_waitcnt lgkmcnt(4)\n"
    "v_mfma_f32_16x16x32_bf16 v[208:211], v[152:155], a[136:139], v[208:211]\n"
    "ds_read_b64_tr_b16 v[232:233], v36 offset:8192\n"
    "ds_read_b64_tr_b16 v[234:235], v36 offset:8704\n"
    "v_mfma_f32_16x16x32_bf16 v[212:215], v[152:155], a[140:143], v[212:215]\n"
    "buffer_load_dwordx4 a[96:99], v247, s[36:39], s64 offen sc1\n"
    "v_mfma_f32_16x16x32_bf16 v[220:223], v[156:159], a[140:143], v[220:223]\n"
    "ds_read_b64_tr_b16 v[236:237], v37 offset:8192\n"
    "s_add_u32 s64, s64, 5120\n"
    "v_mfma_f32_16x16x32_bf16 v[216:219], v[156:159], a[136:139], v[216:219]\n"
    "ds_read_b64_tr_b16 v[238:239], v37 offset:8704\n"
    "s_waitcnt lgkmcnt(4)\n"
    "v_mfma_f32_16x16x32_bf16 v[192:195], v[224:227], a[144:147], v[192:195]\n"
    "ds_read_b64_tr_b16 v[144:145], v34 offset:12288\n"
    "v_mfma_f32_16x16x32_bf16 v[196:199], v[224:227], a[148:151], v[196:199]\n"
    "ds_read_b64_tr_b16 v[146:147], v34 offset:12800\n"
    "v_mfma_f32_16x16x32_bf16 v[204:207], v[228:231], a[148:151], v[204:207]\n"
    "ds_read_b64_tr_b16 v[148:149], v35 offset:12288\n"
    "s_cmp_eq_u32 s64, s72\n"
    "v_mfma_f32_16x16x32_bf16 v[200:203], v[228:231], a[144:147], v[200:203]\n"
    "ds_read_b64_tr_b16 v[150:151], v35 offset:12800\n"
    "s_waitcnt lgkmcnt(4)\n"
    "v_mfma_f32_16x16x32_bf16 v[208:211], v[232:235], a[144:147], v[208:211]\n"
    "ds_read_b64_tr_b16 v[152:153], v36 offset:12288\n"
    "s_cselect_b32 s67, 40960, 0\n"
    "v_mfma_f32_16x16x32_bf16 v[212:215], v[232:235], a[148:151], v[212:215]\n"
    "ds_read_b64_tr_b16 v[154:155], v36 offset:12800\n"
    "v_mfma_f32_16x16x32_bf16 v[220:223], v[236:239], a[148:151], v[220:223]\n"
    "ds_read_b64_tr_b16 v[156:157], v37 offset:12288\n"
    "s_sub_u32 s64, s64, s67\n"
    "v_mfma_f32_16x16x32_bf16 v[216:219], v[236:239], a[144:147], v[216:219]\n"
    "ds_read_b64_tr_b16 v[158:159], v37 offset:12800\n"
    "s_waitcnt lgkmcnt(4)\n"
    "v_mfma_f32_16x16x32_bf16 v[192:195], v[144:147], a[152:155], v[192:195]\n"
    "s_cmp_eq_u64 s[22:23], 0\n"
    "s_cbranch_scc0 L_ka_c0_%=\n"
    "L_ka_r1_%=:\n"
    "v_mfma_f32_16x16x32_bf16 v[196:199], v[144:147], a[156:159], v[196:199]\n"
    "v_mfma_f32_16x16x32_bf16 v[204:207], v[148:151], a[156:159], v[204:207]\n"
    "v_mfma_f32_16x16x32_bf16 v[200:203], v[148:151], a[152:155], v[200:203]\n"
    "s_waitcnt lgkmcnt(0)\n"
    "v_mfma_f32_16x16x32_bf16 v[208:211], v[152:155], a[152:155], v[208:211]\n"
    "v_mfma_f32_16x16x32_bf16 v[212:215], v[152:155], a[156:159], v[212:215]\n"
    "v_mfma_f32_16x16x32_bf16 v[220:223], v[156:159], a[156:159], v[220:223]\n"
    "v_mfma_f32_16x16x32_bf16 v[216:219], v[156:159], a[152:155], v[216:219]\n"
    "s_cmp_eq_u64 s[22:23], 0\n"
    "s_cbranch_scc0 L_ka_slow_%=\n"
    "L_ka_done_%=:\n"
    "s_barrier\n"
    "s_waitcnt vmcnt(63)\n"
    "v_xor_b32 v44, s63, v65\n"
    "v_xor_b32 v43, s63, v67\n"
    "v_or_b32 v44, v44, v43\n"
    "v_cmp_ne_u32 vcc, 0, v44\n"
    "s_nop 1\n"
    "s_cmp_eq_u64 vcc, 0\n"
    "s_cbranch_scc0 L_gkv_slow_%=\n"
    "L_gkv_ok_%=:\n"
    "v_sub_f32 v72, 1.0, v64\n"
    "v_sub_f32 v73, 1.0, v66\n"
    "v_pk_mul_f32 v[192:193], v[192:193], v[72:73] op_sel_hi:[1,0]\n"
    "v_pk_mul_f32 v[194:195], v[194:195], v[72:73] op_sel_hi:[1,0]\n"
    "v_pk_mul_f32 v[200:201], v[200:201], v[72:73] op_sel_hi:[1,0]\n"
    "v_pk_mul_f32 v[202:203], v[202:203], v[72:73] op_sel_hi:[1,0]\n"
    "v_cvt_pk_bf16_f32 v96, v192, v193\n"
    "v_cvt_pk_bf16_f32 v97, v194, v195\n"
    "v_cvt_pk_bf16_f32 v98, v200, v201\n"
    "v_cvt_pk_bf16_f32 v99, v202, v203\n"
    "v_pk_max_i16 v96, v96, 0\n"
    "v_pk_max_i16 v97, v97, 0\n"
    "v_pk_max_i16 v98, v98, 0\n"
    "v_pk_max_i16 v99, v99, 0\n"
    "buffer_store_dwordx4 v[96:99], v1, s[36:39], s17 offen sc1\n"
    "v_pk_mul_f32 v[196:197], v[196:197], v[72:73] op_sel:[0,1] op_sel_hi:[1,1]\n"
    "v_pk_mul_f32 v[198:199], v[198:199], v[72:73] op_sel:[0,1] op_sel_hi:[1,1]\n"
    "v_pk_mul_f32 v[204:205], v[204:205], v[72:73] op_sel:[0,1] op_sel_hi:[1,1]\n"
    "v_pk_mul_f32 v[206:207], v[206:207], v[72:73] op_sel:[0,1] op_sel_hi:[1,1]\n"
    "v_cvt_pk_bf16_f32 v100, v196, v197\n"
    "v_cvt_pk_bf16_f32 v101, v198, v199\n"
    "v_cvt_pk_bf16_f32 v102, v204, v205\n"
    "v_cvt_pk_bf16_f32 v103, v206, v207\n"
    "v_pk_max_i16 v100, v100, 0\n"
    "v_pk_max_i16 v101, v101, 0\n"
    "v_pk_max_i16 v102, v102, 0\n"
    "v_pk_max_i16 v103, v103, 0\n"
    "buffer_store_dwordx4 v[100:103], v1, s[36:39], s17 offen offset:1024 sc1\n"
    "v_pk_mul_f32 v[208:209], v[208:209], v[72:73] op_sel_hi:[1,0]\n"
    "v_pk_mul_f32 v[210:211], v[210:211], v[72:73] op_sel_hi:[1,0]\n"
    "v_pk_mul_f32 v[216:217], v[216:217], v[72:73] op_sel_hi:[1,0]\n"
    "v_pk_mul_f32 v[218:219], v[218:219], v[72:73] op_sel_hi:[1,0]\n"
    "v_cvt_pk_bf16_f32 v104, v208, v209\n"
    "v_cvt_pk_bf16_f32 v105, v210, v211\n"
    "v_cvt_pk_bf16_f32 v106, v216, v217\n"
    "v_cvt_pk_bf16_f32 v107, v218, v219\n"
    "v_pk_max_i16 v104, v104, 0\n"
    "v_pk_max_i16 v105, v105, 0\n"
    "v_pk_max_i16 v106, v106, 0\n"
    "v_pk_max_i16 v107, v107, 0\n"
    "buffer_store_dwordx4 v[104:107], v247, s[36:39], s17 offen sc1\n"
    "v_pk_mul_f32 v[212:213], v[212:213], v[72:73] op_sel:[0,1] op_sel_hi:[1,1]\n"
    "v_pk_mul_f32 v[214:215], v[214:215], v[72:73] op_sel:[0,1] op_sel_hi:[1,1]\n"
    "v_pk_mul_f32 v[220:221], v[220:221], v[72:73] op_sel:[0,1] op_sel_hi:[1,1]\n"
    "v_pk_mul_f32 v[222:223], v[222:223], v[72:73] op_sel:[0,1] op_sel_hi:[1,1]\n"
    "v_cvt_pk_bf16_f32 v108, v212, v213\n"
    "v_cvt_pk_bf16_f32 v109, v214, v215\n"
    "v_cvt_pk_bf16_f32 v110, v220, v221\n"
    "v_cvt_pk_bf16_f32 v111, v222, v223\n"
    "v_pk_max_i16 v108, v108, 0\n"
    "v_pk_max_i16 v109, v109, 0\n"
    "v_pk_max_i16 v110, v110, 0\n"
    "v_pk_max_i16 v111, v111, 0\n"
    "buffer_store_dwordx4 v[108:111], v247, s[36:39], s17 offen offset:1024 sc1\n"
    "buffer_load_dword v14, v6, s[36:39], s16 offen offset:512 sc1\n"
    "s_lshl2_add_u32 s46, s54, s3\n"
    "s_mul_i32 s91, s46, 49152\n"
    "buffer_load_dwordx4 a[100:103], v1, s[36:39], s64 offen sc1\n"
    "s_lshl_b32 s90, s47, 12\n"
    "s_add_u32 s90, s90, s91\n"
    "s_nop 0\n"
    "s_add_u32 s90, s90, 7110656\n"
    "s_add_u32 s91, s91, 7143424\n"
    "s_mul_i32 s46, s46, 20480\n"
    "s_mul_i32 s6, s55, 160\n"
    "buffer_load_dwordx4 a[104:107], v1, s[36:39], s64 offen offset:1024 sc1\n"
    "s_add_u32 s46, s46, s6\n"
    "s_mul_i32 s7, s54, 81920\n"
    "s_mul_i32 s6, s3, 5120\n"
    "s_add_u32 s7, s7, s6\n"
    "s_add_u32 s7, s7, 4489216\n"
    "s_add_u32 s6, s3, 0\n"
    "s_and_b32 s6, s6, 3\n"
    "buffer_load_dwordx4 a[108:111], v1, s[36:39], s64 offen offset:2048 sc1\n"
    "v_lshl_add_u32 v2, s6, 10, v1\n"
    "s_mul_i32 s8, s6, 20480\n"
    "s_add_u32 s8, s8, s7\n"
    "s_add_u32 s6, s3, 1\n"
    "s_and_b32 s6, s6, 3\n"
    "s_nop 0\n"
    "buffer_load_dwordx4 a[112:115], v1, s[36:39], s64 offen offset:3072 sc1\n"
    "v_lshl_add_u32 v3, s6, 10, v1\n"
    "s_mul_i32 s9, s6, 20480\n"
    "s_add_u32 s9, s9, s7\n"
    "s_sub_u32 s6, s3, 1\n"
    "s_and_b32 s6, s6, 3\n"
    "buffer_load_dwordx4 a[116:119], v247, s[36:39], s64 offen sc1\n"
    "s_mul_i32 s6, s6, 25600\n"
    "s_add_u32 s6, s6, 0\n"
    "v_add_u32_e64 v8, s6, v1\n"
    "s_add_u32 s6, s3, 2\n"
    "s_and_b32 s6, s6, 3\n"
    "v_lshl_add_u32 v4, s6, 10, v1\n"
    "s_mul_i32 s10, s6, 20480\n"
    "s_add_u32 s64, s64, 5120\n"
    "s_add_u32 s10, s10, s7\n"
    "s_sub_u32 s6, s3, 2\n"
    "s_and_b32 s6, s6, 3\n"
    "s_mul_i32 s6, s6, 25600\n"
    "s_add_u32 s6, s6, 5120\n"
    "s_cmp_eq_u32 s64, s72\n"
    "s_cselect_b32 s67, 40960, 0\n"
    "s_sub_u32 s64, s64, s67\n"
    "v_add_u32 v9, s6, v1\n"
    "s_add_u32 s6, s3, 3\n"
    "s_and_b32 s6, s6, 3\n"
    "v_lshl_add_u32 v5, s6, 10, v1\n"
    "s_mul_i32 s11, s6, 20480\n"
    "s_add_u32 s11, s11, s7\n"
    "buffer_load_dwordx4 a[120:123], v1, s[36:39], s64 offen sc1\n"
    "s_sub_u32 s6, s3, 3\n"
    "s_and_b32 s6, s6, 3\n"
    "s_mul_i32 s6, s6, 25600\n"
    "s_add_u32 s6, s6, 10240\n"
    "v_add_u32 v10, s6, v1\n"
    "buffer_load_dwordx4 a[124:127], v1, s[36:39], s64 offen offset:1024 sc1\n"
    "v_add_u32 v7, s73, v1\n"
    "v_lshlrev_b32 v11, 3, v0\n"
    "v_add_u32 v11, 4096, v11\n"
    "v_and_b32 v38, 15, v0\n"
    "v_lshrrev_b32 v39, 4, v0\n"
    "s_add_u32 s6, s73, 15360\n"
    "v_mul_u32_u24 v38, 0xb0, v38\n"
    "buffer_load_dwordx4 a[128:131], v1, s[36:39], s64 offen offset:2048 sc1\n"
    "v_lshl_add_u32 v39, v39, 3, s6\n"
    "v_add_u32 v240, v38, v39\n"
    "v_add_u32 v38, 0, v0\n"
    "v_mul_u32_u24 v39, 0xcd, v38\n"
    "v_lshrrev_b32 v39, 11, v39\n"
    "v_lshlrev_b32 v38, 4, v38\n"
    "buffer_load_dwordx4 a[132:135], v1, s[36:39], s64 offen offset:3072 sc1\n"
    "v_lshl_add_u32 v40, v39, 4, v38\n"
    "v_mul_u32_u24 v39, 0x460, v39\n"
    "v_add_u32 v241, s6, v40\n"
    "v_add_u32 v244, v39, v38\n"
    "buffer_load_dwordx4 a[136:139], v247, s[36:39], s64 offen sc1\n"
    "v_add_u32_e64 v38, 64, v0\n"
    "v_mul_u32_u24 v39, 0xcd, v38\n"
    "v_lshrrev_b32 v39, 11, v39\n"
    "v_lshlrev_b32 v38, 4, v38\n"
    "v_lshl_add_u32 v40, v39, 4, v38\n"
    "v_mul_u32_u24 v39, 0x460, v39\n"
    "v_add_u32_e64 v242, s6, v40\n"
    "s_add_u32 s64, s64, 5120\n"
    "v_add_u32_e64 v245, v39, v38\n"
    "v_add_u32 v38, 128, v0\n"
    "v_mul_u32_u24 v39, 0xcd, v38\n"
    "v_lshrrev_b32_e64 v39, 11, v39\n"
    "v_lshlrev_b32 v38, 4, v38\n"
    "s_cmp_eq_u32 s64, s72\n"
    "s_cselect_b32 s67, 40960, 0\n"
    "s_sub_u32 s64, s64, s67\n"
    "v_lshl_add_u32 v40, v39, 4, v38\n"
    "v_mul_u32_u24 v39, 0x460, v39\n"
    "v_add_u32 v243, s6, v40\n"
    "v_cmp_gt_u32_e64 vcc, 32, v0\n"
    "v_mov_b32 v40, 2147483648\n"
    "v_add_u32_e64 v246, v39, v38\n"
    "v_cndmask_b32 v246, v40, v246, vcc\n"
    "s_lshl_b32 s6, s66, 7\n"
    "s_add_u32 s6, s6, 131072\n"
    "s_waitcnt vmcnt(10)\n"
    "buffer_store_dword v248, v249, s[36:39], s6 offen sc1\n"
    "s_or_b32 s6, s2, s3\n"
    "s_cbranch_scc1 L_noep_%=\n"
    "buffer_store_dword v248, v249, s[36:39], 0 offen sc1\n"
    "L_noep_%=:\n"
    "v_cmp_ne_u32 vcc, s63, v14\n"
    "s_nop 1\n"
    "s_cmp_eq_u64 vcc, 0\n"
    "s_cbranch_scc0 L_hfk_slow_%=\n"
    "L_hfk_ok_%=:\n"
    "s_nop 0\n"
    "buffer_load_dwordx4 a[140:143], v1, s[36:39], s64 offen sc1\n"
    "buffer_load_dwordx4 a[144:147], v1, s[36:39], s64 offen offset:1024 sc1\n"
    "buffer_load_dwordx4 a[148:151], v1, s[36:39], s64 offen offset:2048 sc1\n"
    "buffer_load_dwordx4 a[152:155], v1, s[36:39], s64 offen offset:3072 sc1\n"
    "buffer_load_dwordx4 a[156:159], v247, s[36:39], s64 offen sc1\n"
    "s_add_u32 s64, s72, 20480\n"
    "s_add_u32 m0, s73, 0\n"
    "s_nop 0\n"
    "buffer_load_dwordx4 v1, s[36:39], s64 offen sc1 lds\n"
    "buffer_load_dwordx4 v1, s[36:39], s64 offen offset:1024 sc1 lds\n"
    "buffer_load_dwordx4 v1, s[36:39], s64 offen offset:2048 sc1 lds\n"
    "buffer_load_dwordx4 v1, s[36:39], s64 offen offset:3072 sc1 lds\n"
    "s_add_u32 m0, s73, 4096\n"
    "s_nop 0\n"
    "s_nop 0\n"
    "buffer_load_dwordx4 v247, s[36:39], s64 offen sc1 lds\n"
    "s_add_u32 s64, s64, 5120\n"
    "s_add_u32 m0, s73, 5120\n"
    "s_nop 0\n"
    "s_nop 0\n"
    "buffer_load_dwordx4 v1, s[36:39], s64 offen sc1 lds\n"
    "buffer_load_dwordx4 v1, s[36:39], s64 offen offset:1024 sc1 lds\n"
    "buffer_load_dwordx4 v1, s[36:39], s64 offen offset:2048 sc1 lds\n"
    "buffer_load_dwordx4 v1, s[36:39], s64 offen offset:3072 sc1 lds\n"
    "s_add_u32 m0, s73, 9216\n"
    "s_nop 0\n"
    "s_nop 0\n"
    "buffer_load_dwordx4 v247, s[36:39], s64 offen sc1 lds\n"
    "s_add_u32 s64, s64, 5120\n"
    "s_add_u32 m0, s73, 10240\n"
    "s_nop 0\n"
    "s_nop 0\n"
    "buffer_load_dwordx4 v1, s[36:39], s64 offen sc1 lds\n"
    "buffer_load_dwordx4 v1, s[36:39], s64 offen offset:1024 sc1 lds\n"
    "buffer_load_dwordx4 v1, s[36:39], s64 offen offset:2048 sc1 lds\n"
    "buffer_load_dwordx4 v1, s[36:39], s64 offen offset:3072 sc1 lds\n"
    "s_add_u32 m0, s73, 14336\n"
    "s_nop 0\n"
    "s_nop 0\n"
    "buffer_load_dwordx4 v247, s[36:39], s64 offen sc1 lds\n"
    "s_add_u32 s64, s64, 5120\n"
    "s_add_u32 m0, s73, 15360\n"
    "s_nop 0\n"
    "s_nop 0\n"
    "buffer_load_dwordx4 v1, s[36:39], s64 offen sc1 lds\n"
    "buffer_load_dwordx4 v1, s[36:39], s64 offen offset:1024 sc1 lds\n"
    "buffer_load_dwordx4 v1, s[36:39], s64 offen offset:2048 sc1 lds\n"
    "buffer_load_dwordx4 v1, s[36:39], s64 offen offset:3072 sc1 lds\n"
    "s_add_u32 m0, s73, 19456\n"
    "s_nop 0\n"
    "s_nop 0\n"
    "buffer_load_dwordx4 v247, s[36:39], s64 offen sc1 lds\n"
    "s_add_u32 s64, s64, 5120\n"
    "s_add_u32 m0, s73, 20480\n"
    "s_nop 0\n"
    "s_nop 0\n"
    "buffer_load_dwordx4 v1, s[36:39], s64 offen sc1 lds\n"
    "buffer_load_dwordx4 v1, s[36:39], s64 offen offset:1024 sc1 lds\n"
    "buffer_load_dwordx4 v1, s[36:39], s64 offen offset:2048 sc1 lds\n"
    "buffer_load_dwordx4 v1, s[36:39], s64 offen offset:3072 sc1 lds\n"
    "s_add_u32 m0, s73, 24576\n"
    "s_nop 0\n"
    "s_nop 0\n"
    "buffer_load_dwordx4 v247, s[36:39], s64 offen sc1 lds\n"
    "s_add_u32 s64, s64, 5120\n"
    "buffer_load_dwordx4 v[16:19], v2, s[36:39], s90 offen sc1\n"
    "buffer_load_dwordx4 v[20:23], v3, s[36:39], s90 offen sc1\n"
    "buffer_load_dwordx4 v[24:27], v4, s[36:39], s90 offen sc1\n"
    "buffer_load_dwordx4 v[28:31], v5, s[36:39], s90 offen sc1\n"
    "s_add_u32 s90, s90, 4096\n"
    "s_cmp_eq_u32 s90, s91\n"
    "s_cselect_b32 s67, 32768, 0\n"
    "s_sub_u32 s90, s90, s67\n"
    "buffer_load_dwordx4 v[32:35], v2, s[36:39], s90 offen sc1\n"
    "buffer_load_dwordx4 v[36:39], v3, s[36:39], s90 offen sc1\n"
    "buffer_load_dwordx4 v[40:43], v4, s[36:39], s90 offen sc1\n"
    "buffer_load_dwordx4 v[44:47], v5, s[36:39], s90 offen sc1\n"
    "s_add_u32 s90, s90, 4096\n"
    "s_cmp_eq_u32 s90, s91\n"
    "s_cselect_b32 s67, 32768, 0\n"
    "s_sub_u32 s90, s90, s67\n"
    "buffer_load_dwordx4 v[48:51], v2, s[36:39], s90 offen sc1\n"
    "buffer_load_dwordx4 v[52:55], v3, s[36:39], s90 offen sc1\n"
    "buffer_load_dwordx4 v[56:59], v4, s[36:39], s90 offen sc1\n"
    "buffer_load_dwordx4 v[60:63], v5, s[36:39], s90 offen sc1\n"
    "s_add_u32 s90, s90, 4096\n"
    "s_cmp_eq_u32 s90, s91\n"
    "s_cselect_b32 s67, 32768, 0\n"
    "s_sub_u32 s90, s90, s67\n"
    "s_branch L_hloop_%=\n"
    "L_q_%=:\n"
    "s_lshl3_add_u32 s6, s4, s5\n"
    "s_lshl1_add_u32 s6, s6, 1\n"
    "s_lshl2_add_u32 s66, s6, s3\n"
    "s_mul_i32 s7, s66, 2560\n"
    "v_and_b32 v46, 31, v0\n"
    "v_lshlrev_b32 v46, 3, v46\n"
    "v_lshrrev_b32 v47, 5, v0\n"
    "s_movk_i32 s6, 1280\n"
    "v_mad_u32_u24 v47, v47, s6, v46\n"
    "buffer_load_dwordx2 v[76:77], v47, s[24:27], s7 offen offset:0\n"
    "buffer_load_dwordx2 v[78:79], v47, s[24:27], s7 offen offset:256\n"
    "buffer_load_dwordx2 v[80:81], v47, s[24:27], s7 offen offset:512\n"
    "buffer_load_dwordx2 v[82:83], v47, s[24:27], s7 offen offset:768\n"
    "buffer_load_dwordx2 v[84:85], v47, s[24:27], s7 offen offset:1024\n"
    "s_waitcnt lgkmcnt(0)\n"
    "s_nop 0\n"
    "buffer_load_dwordx2 v[86:87], v46, s[44:47], 0 offen offset:0\n"
    "buffer_load_dwordx2 v[88:89], v46, s[44:47], 0 offen offset:256\n"
    "buffer_load_dwordx2 v[90:91], v46, s[44:47], 0 offen offset:512\n"
    "buffer_load_dwordx2 v[92:93], v46, s[44:47], 0 offen offset:768\n"
    "buffer_load_dwordx2 v[94:95], v46, s[44:47], 0 offen offset:1024\n"
    "s_load_dword s63, s[36:37], 0x0\n"
    "v_and_b32 v38, 15, v0\n"
    "v_lshrrev_b32 v39, 4, v0\n"
    "v_lshrrev_b32 v40, 1, v38\n"
    "v_xor_b32 v40, v40, v39\n"
    "v_lshlrev_b32_e64 v40, 4, v40\n"
    "v_lshl_add_u32 v40, v38, 7, v40\n"
    "v_add_u32 v21, s58, v40\n"
    "v_xor_b32 v22, 64, v21\n"
    "v_xor_b32 v23, 0x1000, v21\n"
    "v_xor_b32 v24, 0x1040, v21\n"
    "v_bfe_u32 v40, v0, 2, 2\n"
    "v_bfe_u32 v41, v0, 3, 2\n"
    "v_and_b32 v38, 3, v0\n"
    "v_lshlrev_b32 v38, 3, v38\n"
    "v_lshl_add_u32 v38, v41, 5, v38\n"
    "v_lshl_add_u32 v38, v40, 7, v38\n"
    "s_movk_i32 s6, 10240\n"
    "v_mad_u32_u24 v38, v39, s6, v38\n"
    "s_lshl_b32 s6, s57, 10\n"
    "s_add_u32 s6, s6, 49152\n"
    "v_add_u32 v25, s6, v38\n"
    "v_xor_b32 v26, 32, v25\n"
    "v_xor_b32_e64 v27, 64, v25\n"
    "v_xor_b32 v28, 0x60, v25\n"
    "v_mov_b32 v38, 0\n"
    "v_mov_b32 v39, 0\n"
    "v_mov_b32 v40, 0\n"
    "v_mov_b32 v41, 0\n"
    "s_mul_i32 s6, s3, 5120\n"
    "s_add_u32 s6, s6, 143360\n"
    "v_add_u32_e64 v42, s6, v1\n"
    "ds_write_b128 v42, v[38:41] offset:0\n"
    "ds_write_b128 v42, v[38:41] offset:1024\n"
    "ds_write_b128 v42, v[38:41] offset:2048\n"
    "ds_write_b128 v42, v[38:41] offset:3072\n"
    "ds_write_b128 v42, v[38:41] offset:4096\n"
    "s_add_u32 s53, s53, 1\n"
    "s_cmp_eq_u32 s53, 10\n"
    "s_cselect_b32 s53, 0, s53\n"
    "s_lshl_b32 s60, s53, 7\n"
    "s_add_u32 m0, s58, 16384\n"
    "s_nop 0\n"
    "s_nop 0\n"
    "buffer_load_dwordx4 v16, s[24:27], s60 offen lds\n"
    "buffer_load_dwordx4 v17, s[24:27], s60 offen offset:1024 lds\n"
    "s_add_u32 m0, s59, 4096\n"
    "s_mul_i32 s61, s53, 196608\n"
    "buffer_load_dwordx4 v18, s[32:35], s61 offen lds\n"
    "buffer_load_dwordx4 v19, s[32:35], s61 offen offset:1024 lds\n"
    "s_waitcnt vmcnt(16)\n"
    "s_barrier\n"
    "ds_read_b128 a[0:3], v21 offset:0\n"
    "ds_read_b64_tr_b16 v[104:105], v25 offset:0\n"
    "ds_read_b64_tr_b16 v[106:107], v25 offset:512\n"
    "ds_read_b128 a[4:7], v21 offset:2048\n"
    "ds_read_b128 v[96:99], v23 offset:0\n"
    "ds_read_b128 v[100:103], v23 offset:2048\n"
    "ds_read_b64_tr_b16 v[108:109], v26 offset:0\n"
    "ds_read_b64_tr_b16 v[110:111], v26 offset:512\n"
    "ds_read_b64_tr_b16 v[112:113], v27 offset:0\n"
    "ds_read_b64_tr_b16 v[114:115], v27 offset:512\n"
    "ds_read_b64_tr_b16 v[116:117], v28 offset:0\n"
    "ds_read_b64_tr_b16 v[118:119], v28 offset:512\n"
    "s_add_u32 s60, s60, 20480\n"
    "s_add_u32 m0, s58, 18432\n"
    "s_nop 0\n"
    "s_nop 0\n"
    "buffer_load_dwordx4 v16, s[24:27], s60 offen lds\n"
    "buffer_load_dwordx4 v17, s[24:27], s60 offen offset:1024 lds\n"
    "s_add_u32 m0, s59, 6144\n"
    "s_add_u32 s61, s61, 98304\n"
    "buffer_load_dwordx4 v18, s[32:35], s61 offen lds\n"
    "buffer_load_dwordx4 v19, s[32:35], s61 offen offset:1024 lds\n"
    "s_waitcnt lgkmcnt(0)\n"
    "v_mfma_f32_16x16x32_bf16 v[160:163], v[104:107], a[0:3], 0\n"
    "s_waitcnt vmcnt(18)\n"
    "s_barrier\n"
    "v_mfma_f32_16x16x32_bf16 v[164:167], v[104:107], a[4:7], 0\n"
    "ds_read_b128 a[8:11], v22 offset:0\n"
    "s_add_u32 s53, s53, 1\n"
    "v_mfma_f32_16x16x32_bf16 v[168:171], v[104:107], v[96:99], 0\n"
    "s_cmp_eq_u32 s53, 10\n"
    "s_cselect_b32 s53, 0, s53\n"
    "s_lshl_b32 s60, s53, 7\n"
    "v_mfma_f32_16x16x32_bf16 v[172:175], v[104:107], v[100:103], 0\n"
    "ds_read_b64_tr_b16 v[132:133], v25 offset:2048\n"
    "s_add_u32 m0, s58, 32768\n"
    "v_mfma_f32_16x16x32_bf16 v[188:191], v[108:111], v[100:103], 0\n"
    "ds_read_b64_tr_b16 v[134:135], v25 offset:2560\n"
    "buffer_load_dwordx4 v16, s[24:27], s60 offen lds\n"
    "v_mfma_f32_16x16x32_bf16 v[184:187], v[108:111], v[96:99], 0\n"
    "ds_read_b128 a[12:15], v22 offset:2048\n"
    "buffer_load_dwordx4 v17, s[24:27], s60 offen offset:1024 lds\n"
    "v_mfma_f32_16x16x32_bf16 v[180:183], v[108:111], a[4:7], 0\n"
    "ds_read_b128 v[124:127], v24 offset:0\n"
    "s_add_u32 m0, s59, 8192\n"
    "v_mfma_f32_16x16x32_bf16 v[176:179], v[108:111], a[0:3], 0\n"
    "ds_read_b128 v[128:131], v24 offset:2048\n"
    "s_mul_i32 s61, s53, 196608\n"
    "buffer_load_dwordx4 v18, s[32:35], s61 offen lds\n"
    "v_mfma_f32_16x16x32_bf16 v[192:195], v[112:115], a[0:3], 0\n"
    "ds_read_b64_tr_b16 v[136:137], v26 offset:2048\n"
    "v_mfma_f32_16x16x32_bf16 v[196:199], v[112:115], a[4:7], 0\n"
    "ds_read_b64_tr_b16 v[138:139], v26 offset:2560\n"
    "buffer_load_dwordx4 v19, s[32:35], s61 offen offset:1024 lds\n"
    "v_mfma_f32_16x16x32_bf16 v[200:203], v[112:115], v[96:99], 0\n"
    "ds_read_b64_tr_b16 v[140:141], v27 offset:2048\n"
    "s_add_u32 s60, s60, 20480\n"
    "s_add_u32 m0, s58, 34816\n"
    "v_mfma_f32_16x16x32_bf16 v[204:207], v[112:115], v[100:103], 0\n"
    "ds_read_b64_tr_b16 v[142:143], v27 offset:2560\n"
    "buffer_load_dwordx4 v16, s[24:27], s60 offen lds\n"
    "v_mfma_f32_16x16x32_bf16 v[220:223], v[116:119], v[100:103], 0\n"
    "ds_read_b64_tr_b16 v[144:145], v28 offset:2048\n"
    "buffer_load_dwordx4 v17, s[24:27], s60 offen offset:1024 lds\n"
    "v_mfma_f32_16x16x32_bf16 v[216:219], v[116:119], v[96:99], 0\n"
    "ds_read_b64_tr_b16 v[146:147], v28 offset:2560\n"
    "s_add_u32 m0, s59, 0\n"
    "v_mfma_f32_16x16x32_bf16 v[212:215], v[116:119], a[4:7], 0\n"
    "s_add_u32 s61, s61, 98304\n"
    "buffer_load_dwordx4 v18, s[32:35], s61 offen lds\n"
    "v_mfma_f32_16x16x32_bf16 v[208:211], v[116:119], a[0:3], 0\n"
    "buffer_load_dwordx4 v19, s[32:35], s61 offen offset:1024 lds\n"
    "v_lshrrev_b32 v50, 3, v0\n"
    "v_and_b32 v51, 7, v0\n"
    "v_and_b32 v52, 6, v50\n"
    "v_xor_b32 v51, v51, v52\n"
    "v_lshlrev_b32 v51, 4, v51\n"
    "s_movk_i32 s6, 1280\n"
    "v_mad_u32_u24 v50, v50, s6, v51\n"
    "v_lshrrev_b32 v51, 2, v0\n"
    "v_and_b32 v52, 3, v0\n"
    "v_lshlrev_b32_e64 v52, 3, v52\n"
    "v_lshl_add_u32 v52, v51, 7, v52\n"
    "v_bfe_u32 v51, v0, 3, 2\n"
    "v_lshl_add_u32 v52, v51, 5, v52\n"
    "s_lshl_b32 s74, s3, 12\n"
    "s_add_u32 s74, s74, 90112\n"
    "v_add_u32 v51, s74, v52\n"
    "v_xor_b32 v52, 32, v51\n"
    "v_xor_b32 v53, 64, v51\n"
    "v_xor_b32 v54, 0x60, v51\n"
    "s_lshl3_add_u32 s84, s4, s5\n"
    "s_lshl_b32 s85, s3, 7\n"
    "s_add_u32 s84, s84, s85\n"
    "s_mul_i32 s85, s84, 6554\n"
    "s_lshr_b32 s85, s85, 16\n"
    "s_mul_i32 s86, s85, 10\n"
    "s_sub_u32 s86, s84, s86\n"
    "s_mul_i32 s6, s85, 5\n"
    "s_lshl2_add_u32 s6, s86, s6\n"
    "s_add_u32 s6, s6, 1664\n"
    "s_lshl2_add_u32 s76, s86, 0\n"
    "s_mul_i32 s76, s76, 52\n"
    "s_lshr_b32 s76, s76, 8\n"
    "s_mul_i32 s76, s76, 335\n"
    "s_add_u32 s76, s76, s6\n"
    "s_add_u32 s76, s76, 0\n"
    "s_lshl_b32 s76, s76, 10\n"
    "s_lshl2_add_u32 s77, s86, 1\n"
    "s_mul_i32 s77, s77, 52\n"
    "s_lshr_b32 s77, s77, 8\n"
    "s_mul_i32 s77, s77, 335\n"
    "s_add_u32 s77, s77, s6\n"
    "s_add_u32 s77, s77, 1\n"
    "s_lshl_b32 s77, s77, 10\n"
    "s_lshl2_add_u32 s78, s86, 2\n"
    "s_mul_i32 s78, s78, 52\n"
    "s_lshr_b32 s78, s78, 8\n"
    "s_mul_i32 s78, s78, 335\n"
    "s_add_u32 s78, s78, s6\n"
    "s_add_u32 s78, s78, 2\n"
    "s_lshl_b32 s78, s78, 10\n"
    "s_lshl2_add_u32 s79, s86, 3\n"
    "s_mul_i32 s79, s79, 52\n"
    "s_lshr_b32 s79, s79, 8\n"
    "s_mul_i32 s79, s79, 335\n"
    "s_add_u32 s79, s79, s6\n"
    "s_add_u32 s79, s79, 3\n"
    "s_lshl_b32 s79, s79, 10\n"
    "s_mul_i32 s6, s85, 241\n"
    "s_lshr_b32 s6, s6, 12\n"
    "s_mul_i32 s7, s6, 17\n"
    "s_sub_u32 s84, s85, s7\n"
    "s_mul_i32 s7, s6, 5\n"
    "s_sub_u32 s85, s85, s7\n"
    "s_sub_u32 s87, s84, 12\n"
    "s_mul_i32 s6, s6, 160\n"
    "s_lshr_b32 s7, s87, 1\n"
    "s_lshl1_add_u32 s7, s87, s7\n"
    "s_lshl_b32 s7, s7, 4\n"
    "s_cmp_eq_u32 s87, 4\n"
    "s_cselect_b32 s7, 64, s7\n"
    "s_cselect_b32 s87, 80, 16\n"
    "s_add_u32 s6, s6, s7\n"
    "s_lshl_b32 s7, s85, 5\n"
    "s_cmp_lt_u32 s84, 12\n"
    "s_cselect_b32 s6, s7, s6\n"
    "s_cselect_b32 s87, 16, s87\n"
    "s_cselect_b64 s[48:49], s[14:15], s[12:13]\n"
    "s_nop 0\n"
    "s_mov_b32 s50, 819200\n"
    "s_cselect_b32 s50, 1966080, s50\n"
    "s_mov_b32 s51, 0x20000\n"
    "s_mul_i32 s6, s6, 1280\n"
    "s_lshl_b32 s86, s86, 7\n"
    "s_add_u32 s19, s6, s86\n"
    "s_mul_i32 s87, s87, 1280\n"
    "s_add_u32 s20, s19, s87\n"
    "s_lshl3_add_u32 s84, s4, s5\n"
    "s_lshl_b32 s85, s3, 7\n"
    "s_add_u32 s84, s84, s85\n"
    "s_add_u32 s84, s84, 512\n"
    "s_cmp_lt_u32 s84, 680\n"
    "s_cselect_b32 s75, 1, 0\n"
    "s_cbranch_scc0 L_rp_no2_%=\n"
    "s_mul_i32 s85, s84, 6554\n"
    "s_lshr_b32 s85, s85, 16\n"
    "s_mul_i32 s86, s85, 10\n"
    "s_sub_u32 s86, s84, s86\n"
    "s_mul_i32 s6, s85, 5\n"
    "s_lshl2_add_u32 s6, s86, s6\n"
    "s_add_u32 s6, s6, 1664\n"
    "s_lshl2_add_u32 s80, s86, 0\n"
    "s_mul_i32 s80, s80, 52\n"
    "s_lshr_b32 s80, s80, 8\n"
    "s_mul_i32 s80, s80, 335\n"
    "s_add_u32 s80, s80, s6\n"
    "s_add_u32 s80, s80, 0\n"
    "s_lshl_b32 s80, s80, 10\n"
    "s_lshl2_add_u32 s81, s86, 1\n"
    "s_mul_i32 s81, s81, 52\n"
    "s_lshr_b32 s81, s81, 8\n"
    "s_mul_i32 s81, s81, 335\n"
    "s_add_u32 s81, s81, s6\n"
    "s_add_u32 s81, s81, 1\n"
    "s_lshl_b32 s81, s81, 10\n"
    "s_lshl2_add_u32 s82, s86, 2\n"
    "s_mul_i32 s82, s82, 52\n"
    "s_lshr_b32 s82, s82, 8\n"
    "s_mul_i32 s82, s82, 335\n"
    "s_add_u32 s82, s82, s6\n"
    "s_add_u32 s82, s82, 2\n"
    "s_lshl_b32 s82, s82, 10\n"
    "s_lshl2_add_u32 s83, s86, 3\n"
    "s_mul_i32 s83, s83, 52\n"
    "s_lshr_b32 s83, s83, 8\n"
    "s_mul_i32 s83, s83, 335\n"
    "s_add_u32 s83, s83, s6\n"
    "s_add_u32 s83, s83, 3\n"
    "s_lshl_b32 s83, s83, 10\n"
    "s_mul_i32 s6, s85, 241\n"
    "s_lshr_b32 s6, s6, 12\n"
    "s_mul_i32 s7, s6, 17\n"
    "s_sub_u32 s84, s85, s7\n"
    "s_mul_i32 s7, s6, 5\n"
    "s_sub_u32 s85, s85, s7\n"
    "s_sub_u32 s87, s84, 12\n"
    "s_mul_i32 s6, s6, 160\n"
    "s_lshr_b32 s7, s87, 1\n"
    "s_lshl1_add_u32 s7, s87, s7\n"
    "s_lshl_b32 s7, s7, 4\n"
    "s_cmp_eq_u32 s87, 4\n"
    "s_cselect_b32 s7, 64, s7\n"
    "s_cselect_b32 s87, 80, 16\n"
    "s_add_u32 s6, s6, s7\n"
    "s_lshl_b32 s7, s85, 5\n"
    "s_cmp_lt_u32 s84, 12\n"
    "s_cselect_b32 s6, s7, s6\n"
    "s_cselect_b32 s87, 16, s87\n"
    "s_cselect_b64 s[92:93], s[14:15], s[12:13]\n"
    "s_mov_b32 s94, 819200\n"
    "s_cselect_b32 s94, 1966080, s94\n"
    "s_mov_b32 s95, 0x20000\n"
    "s_mul_i32 s6, s6, 1280\n"
    "s_lshl_b32 s86, s86, 7\n"
    "s_add_u32 s22, s6, s86\n"
    "s_mul_i32 s87, s87, 1280\n"
    "s_add_u32 s23, s22, s87\n"
    "L_rp_no2_%=:\n"
    "s_add_u32 m0, s74, 0\n"
    "s_nop 0\n"
    "s_nop 0\n"
    "buffer_load_dwordx4 v50, s[48:51], s19 offen lds\n"
    "s_add_u32 m0, s74, 1024\n"
    "s_add_u32 s19, s19, 10240\n"
    "buffer_load_dwordx4 v50, s[48:51], s19 offen lds\n"
    "s_add_u32 m0, s74, 2048\n"
    "s_nop 0\n"
    "s_nop 0\n"
    "buffer_load_dwordx4 v50, s[48:51], s20 offen lds\n"
    "s_add_u32 m0, s74, 3072\n"
    "s_add_u32 s20, s20, 10240\n"
    "buffer_load_dwordx4 v50, s[48:51], s20 offen lds\n"
    "s_cmp_eq_u32 s75, 0\n"
    "s_cbranch_scc1 L_rp_noi_%=\n"
    "s_add_u32 m0, s74, 16384\n"
    "s_nop 0\n"
    "s_nop 0\n"
    "buffer_load_dwordx4 v50, s[92:95], s22 offen lds\n"
    "s_add_u32 m0, s74, 17408\n"
    "s_add_u32 s22, s22, 10240\n"
    "buffer_load_dwordx4 v50, s[92:95], s22 offen lds\n"
    "s_add_u32 m0, s74, 18432\n"
    "s_nop 0\n"
    "s_nop 0\n"
    "buffer_load_dwordx4 v50, s[92:95], s23 offen lds\n"
    "s_add_u32 m0, s74, 19456\n"
    "s_add_u32 s23, s23, 10240\n"
    "buffer_load_dwordx4 v50, s[92:95], s23 offen lds\n"
    "L_rp_noi_%=:\n"
    "s_waitcnt lgkmcnt(0)\n"
    "v_mfma_f32_16x16x32_bf16 v[160:163], v[132:135], a[8:11], v[160:163]\n"
    "s_waitcnt vmcnt(14)\n"
    "s_barrier\n"
    "v_mfma_f32_16x16x32_bf16 v[164:167], v[132:135], a[12:15], v[164:167]\n"
    "ds_read_b128 a[16:19], v21 offset:16384\n"
    "s_add_u32 s53, s53, 1\n"
    "v_mfma_f32_16x16x32_bf16 v[168:171], v[132:135], v[124:127], v[168:171]\n"
    "s_cmp_eq_u32 s53, 10\n"
    "s_cselect_b32 s53, 0, s53\n"
    "s_lshl_b32 s60, s53, 7\n"
    "v_mfma_f32_16x16x32_bf16 v[172:175], v[132:135], v[128:131], v[172:175]\n"
    "ds_read_b64_tr_b16 v[104:105], v25 offset:4096\n"
    "s_add_u32 m0, s58, 0\n"
    "v_mfma_f32_16x16x32_bf16 v[188:191], v[136:139], v[128:131], v[188:191]\n"
    "ds_read_b64_tr_b16 v[106:107], v25 offset:4608\n"
    "v_mfma_f32_16x16x32_bf16 v[184:187], v[136:139], v[124:127], v[184:187]\n"
    "ds_read_b128 a[20:23], v21 offset:18432\n"
    "v_mfma_f32_16x16x32_bf16 v[180:183], v[136:139], a[12:15], v[180:183]\n"
    "ds_read_b128 v[96:99], v23 offset:16384\n"
    "v_mfma_f32_16x16x32_bf16 v[176:179], v[136:139], a[8:11], v[176:179]\n"
    "ds_read_b128 v[100:103], v23 offset:18432\n"
    "buffer_load_dwordx4 v16, s[24:27], s60 offen lds\n"
    "v_mfma_f32_16x16x32_bf16 v[192:195], v[140:143], a[8:11], v[192:195]\n"
    "ds_read_b64_tr_b16 v[108:109], v26 offset:4096\n"
    "v_mfma_f32_16x16x32_bf16 v[196:199], v[140:143], a[12:15], v[196:199]\n"
    "ds_read_b64_tr_b16 v[110:111], v26 offset:4608\n"
    "buffer_load_dwordx4 v17, s[24:27], s60 offen offset:1024 lds\n"
    "v_mfma_f32_16x16x32_bf16 v[200:203], v[140:143], v[124:127], v[200:203]\n"
    "ds_read_b64_tr_b16 v[112:113], v27 offset:4096\n"
    "s_add_u32 m0, s59, 2048\n"
    "v_mfma_f32_16x16x32_bf16 v[204:207], v[140:143], v[128:131], v[204:207]\n"
    "ds_read_b64_tr_b16 v[114:115], v27 offset:4608\n"
    "s_mul_i32 s61, s53, 196608\n"
    "v_mfma_f32_16x16x32_bf16 v[220:223], v[144:147], v[128:131], v[220:223]\n"
    "ds_read_b64_tr_b16 v[116:117], v28 offset:4096\n"
    "v_mfma_f32_16x16x32_bf16 v[216:219], v[144:147], v[124:127], v[216:219]\n"
    "ds_read_b64_tr_b16 v[118:119], v28 offset:4608\n"
    "buffer_load_dwordx4 v18, s[32:35], s61 offen lds\n"
    "v_mfma_f32_16x16x32_bf16 v[212:215], v[144:147], a[12:15], v[212:215]\n"
    "buffer_load_dwordx4 v19, s[32:35], s61 offen offset:1024 lds\n"
    "v_mfma_f32_16x16x32_bf16 v[208:211], v[144:147], a[8:11], v[208:211]\n"
    "s_waitcnt lgkmcnt(0)\n"
    "v_mfma_f32_16x16x32_bf16 v[160:163], v[104:107], a[16:19], v[160:163]\n"
    "s_waitcnt vmcnt(16)\n"
    "s_barrier\n"
    "v_mfma_f32_16x16x32_bf16 v[164:167], v[104:107], a[20:23], v[164:167]\n"
    "ds_read_b128 a[24:27], v22 offset:16384\n"
    "s_waitcnt vmcnt(24)\n"
    "v_mfma_f32_16x16x32_bf16 v[168:171], v[104:107], v[96:99], v[168:171]\n"
    "v_dot2_f32_bf16 v44, v76, v86, 0\n"
    "v_dot2_f32_bf16 v44, v77, v87, v44\n"
    "s_add_u32 s60, s60, 20480\n"
    "v_mfma_f32_16x16x32_bf16 v[172:175], v[104:107], v[100:103], v[172:175]\n"
    "ds_read_b64_tr_b16 v[132:133], v25 offset:6144\n"
    "v_dot2_f32_bf16 v44, v78, v88, v44\n"
    "v_dot2_f32_bf16 v44, v79, v89, v44\n"
    "v_dot2_f32_bf16 v44, v80, v90, v44\n"
    "v_mfma_f32_16x16x32_bf16 v[188:191], v[108:111], v[100:103], v[188:191]\n"
    "ds_read_b64_tr_b16 v[134:135], v25 offset:6656\n"
    "s_add_u32 m0, s58, 2048\n"
    "v_dot2_f32_bf16 v44, v81, v91, v44\n"
    "v_dot2_f32_bf16 v44, v82, v92, v44\n"
    "v_mfma_f32_16x16x32_bf16 v[184:187], v[108:111], v[96:99], v[184:187]\n"
    "ds_read_b128 a[28:31], v22 offset:18432\n"
    "v_dot2_f32_bf16 v44, v83, v93, v44\n"
    "v_dot2_f32_bf16 v44, v84, v94, v44\n"
    "buffer_load_dwordx4 v16, s[24:27], s60 offen lds\n"
    "v_mfma_f32_16x16x32_bf16 v[180:183], v[108:111], a[20:23], v[180:183]\n"
    "ds_read_b128 v[124:127], v24 offset:16384\n"
    "v_dot2_f32_bf16 v44, v85, v95, v44\n"
    "s_add_u32 s63, s63, 1\n"
    "v_mfma_f32_16x16x32_bf16 v[176:179], v[108:111], a[16:19], v[176:179]\n"
    "ds_read_b128 v[128:131], v24 offset:18432\n"
    "v_and_b32 v46, 31, v0\n"
    "v_lshrrev_b32 v47, 5, v0\n"
    "v_mov_b32 v248, s63\n"
    "v_mfma_f32_16x16x32_bf16 v[192:195], v[112:115], a[16:19], v[192:195]\n"
    "ds_read_b64_tr_b16 v[136:137], v26 offset:6144\n"
    "buffer_load_dwordx4 v17, s[24:27], s60 offen offset:1024 lds\n"
    "v_mfma_f32_16x16x32_bf16 v[196:199], v[112:115], a[20:23], v[196:199]\n"
    "ds_read_b64_tr_b16 v[138:139], v26 offset:6656\n"
    "v_add_f32_dpp v44, v44, v44 row_shr:1 row_mask:0xf bank_mask:0xf\n"
    "s_nop 1\n"
    "v_add_f32_dpp v44, v44, v44 row_shr:2 row_mask:0xf bank_mask:0xf\n"
    "s_nop 1\n"
    "v_add_f32_dpp v44, v44, v44 row_shr:4 row_mask:0xf bank_mask:0xf\n"
    "v_mfma_f32_16x16x32_bf16 v[200:203], v[112:115], v[96:99], v[200:203]\n"
    "ds_read_b64_tr_b16 v[140:141], v27 offset:6144\n"
    "s_add_u32 m0, s59, 4096\n"
    "s_add_u32 s61, s61, 98304\n"
    "v_add_f32_dpp v44, v44, v44 row_shr:8 row_mask:0xf bank_mask:0xf\n"
    "v_mfma_f32_16x16x32_bf16 v[204:207], v[112:115], v[100:103], v[204:207]\n"
    "ds_read_b64_tr_b16 v[142:143], v27 offset:6656\n"
    "v_add_f32_dpp v44, v44, v44 row_bcast:15 row_mask:0xa bank_mask:0xf\n"
    "v_mul_f32 v44, 0xbfb8aa3b, v44\n"
    "v_exp_f32_e64 v44, v44\n"
    "v_mfma_f32_16x16x32_bf16 v[220:223], v[116:119], v[100:103], v[220:223]\n"
    "ds_read_b64_tr_b16 v[144:145], v28 offset:6144\n"
    "buffer_load_dwordx4 v18, s[32:35], s61 offen lds\n"
    "v_mfma_f32_16x16x32_bf16 v[216:219], v[116:119], v[96:99], v[216:219]\n"
    "ds_read_b64_tr_b16 v[146:147], v28 offset:6656\n"
    "v_cmp_eq_u32 vcc, 31, v46\n"
    "v_lshlrev_b32 v47, 3, v47\n"
    "v_mov_b32 v46, 2147483648\n"
    "v_mfma_f32_16x16x32_bf16 v[212:215], v[116:119], a[20:23], v[212:215]\n"
    "v_cndmask_b32 v47, v46, v47, vcc\n"
    "v_add_f32 v44, 1.0, v44\n"
    "v_rcp_f32_e64 v44, v44\n"
    "v_mfma_f32_16x16x32_bf16 v[208:211], v[116:119], a[16:19], v[208:211]\n"
    "buffer_load_dwordx4 v19, s[32:35], s61 offen offset:1024 lds\n"
    "v_mov_b32_e64 v45, s63\n"
    "s_lshl4_add_u32 s65, s66, 16384\n"
    "s_waitcnt lgkmcnt(0)\n"
    "v_mfma_f32_16x16x32_bf16 v[160:163], v[132:135], a[24:27], v[160:163]\n"
    "s_waitcnt vmcnt(14)\n"
    "s_barrier\n"
    "v_mfma_f32_16x16x32_bf16 v[164:167], v[132:135], a[28:31], v[164:167]\n"
    "ds_read_b128 a[32:35], v21 offset:32768\n"
    "s_lshl4_add_u32 s89, s5, s4\n"
    "s_lshl2_add_u32 s89, s89, s3\n"
    "s_add_u32 s53, s53, 1\n"
    "v_mfma_f32_16x16x32_bf16 v[168:171], v[132:135], v[124:127], v[168:171]\n"
    "s_cmp_eq_u32 s53, 10\n"
    "s_cselect_b32 s53, 0, s53\n"
    "v_mov_b32 v76, -1\n"
    "v_mov_b32 v77, -1\n"
    "v_mov_b32_e64 v78, -1\n"
    "v_mfma_f32_16x16x32_bf16 v[172:175], v[132:135], v[128:131], v[172:175]\n"
    "ds_read_b64_tr_b16 v[104:105], v25 offset:8192\n"
    "v_mov_b32 v79, -1\n"
    "s_lshl_b32 s89, s89, 6\n"
    "s_add_u32 s89, s89, 32768\n"
    "s_lshl_b32 s60, s53, 7\n"
    "s_add_u32 m0, s58, 16384\n"
    "v_mfma_f32_16x16x32_bf16 v[188:191], v[136:139], v[128:131], v[188:191]\n"
    "ds_read_b64_tr_b16 v[106:107], v25 offset:8704\n"
    "v_cmp_gt_u32 vcc, 4, v0\n"
    "v_mov_b32 v43, 2147483648\n"
    "v_mov_b32_e64 v55, v1\n"
    "v_cndmask_b32 v55, v43, v55, vcc\n"
    "s_waitcnt vmcnt(8)\n"
    "ds_read_b64_tr_b16 a[224:225], v51 offset:0\n"
    "v_mfma_f32_16x16x32_bf16 v[184:187], v[136:139], v[124:127], v[184:187]\n"
    "ds_read_b128 a[36:39], v21 offset:34816\n"
    "ds_read_b64_tr_b16 a[226:227], v51 offset:2048\n"
    "buffer_load_dwordx4 v16, s[24:27], s60 offen lds\n"
    "v_mfma_f32_16x16x32_bf16 v[180:183], v[136:139], a[28:31], v[180:183]\n"
    "ds_read_b128 v[96:99], v23 offset:32768\n"
    "ds_read_b64_tr_b16 a[228:229], v52 offset:0\n"
    "ds_read_b64_tr_b16 a[230:231], v52 offset:2048\n"
    "v_mfma_f32_16x16x32_bf16 v[176:179], v[136:139], a[24:27], v[176:179]\n"
    "ds_read_b128 v[100:103], v23 offset:34816\n"
    "ds_read_b64_tr_b16 a[232:233], v53 offset:0\n"
    "buffer_load_dwordx4 v17, s[24:27], s60 offen offset:1024 lds\n"
    "ds_read_b64_tr_b16 a[234:235], v53 offset:2048\n"
    "v_mfma_f32_16x16x32_bf16 v[192:195], v[140:143], a[24:27], v[192:195]\n"
    "ds_read_b64_tr_b16 v[108:109], v26 offset:8192\n"
    "ds_read_b64_tr_b16 a[236:237], v54 offset:0\n"
    "ds_read_b64_tr_b16 a[238:239], v54 offset:2048\n"
    "v_mfma_f32_16x16x32_bf16 v[196:199], v[140:143], a[28:31], v[196:199]\n"
    "ds_read_b64_tr_b16 v[110:111], v26 offset:8704\n"
    "s_add_u32 m0, s59, 6144\n"
    "s_mul_i32 s61, s53, 196608\n"
    "s_cmp_eq_u32 s75, 0\n"
    "s_cbranch_scc1 L_rp_nord_%=\n"
    "ds_read_b64_tr_b16 a[240:241], v51 offset:16384\n"
    "ds_read_b64_tr_b16 a[242:243], v51 offset:18432\n"
    "ds_read_b64_tr_b16 a[244:245], v52 offset:16384\n"
    "ds_read_b64_tr_b16 a[246:247], v52 offset:18432\n"
    "ds_read_b64_tr_b16 a[248:249], v53 offset:16384\n"
    "ds_read_b64_tr_b16 a[250:251], v53 offset:18432\n"
    "ds_read_b64_tr_b16 a[252:253], v54 offset:16384\n"
    "ds_read_b64_tr_b16 a[254:255], v54 offset:18432\n"
    "L_rp_nord_%=:\n"
    "v_mfma_f32_16x16x32_bf16 v[200:203], v[140:143], v[124:127], v[200:203]\n"
    "ds_read_b64_tr_b16 v[112:113], v27 offset:8192\n"
    "v_mfma_f32_16x16x32_bf16 v[204:207], v[140:143], v[128:131], v[204:207]\n"
    "ds_read_b64_tr_b16 v[114:115], v27 offset:8704\n"
    "v_mfma_f32_16x16x32_bf16 v[220:223], v[144:147], v[128:131], v[220:223]\n"
    "ds_read_b64_tr_b16 v[116:117], v28 offset:8192\n"
    "s_sub_u32 s6, s62, 20\n"
    "v_mfma_f32_16x16x32_bf16 v[216:219], v[144:147], v[124:127], v[216:219]\n"
    "ds_read_b64_tr_b16 v[118:119], v28 offset:8704\n"
    "s_cselect_b32 s62, s62, s6\n"
    "s_mul_i32 s71, s3, 5120\n"
    "buffer_load_dwordx4 v18, s[32:35], s61 offen lds\n"
    "v_mfma_f32_16x16x32_bf16 v[212:215], v[144:147], a[28:31], v[212:215]\n"
    "s_mul_i32 s6, s5, 160\n"
    "v_add_u32_e64 v38, 0, v0\n"
    "v_mul_u32_u24 v39, 0x667, v38\n"
    "v_lshrrev_b32_e64 v39, 14, v39\n"
    "v_mfma_f32_16x16x32_bf16 v[208:211], v[144:147], a[24:27], v[208:211]\n"
    "v_mul_u32_u24_e64 v40, 10, v39\n"
    "buffer_load_dwordx4 v19, s[32:35], s61 offen offset:1024 lds\n"
    "v_sub_u32 v38, v38, v40\n"
    "v_lshrrev_b32 v40, 1, v39\n"
    "v_and_b32_e64 v40, 4, v40\n"
    "v_xor_b32 v39, v39, v40\n"
    "s_waitcnt lgkmcnt(0)\n"
    "buffer_store_dwordx2 v[44:45], v47, s[36:39], s65 offen sc1\n"
    "buffer_store_dwordx4 v[76:79], v55, s[36:39], s89 offen sc1\n"
    "buffer_store_dwordx4 a[224:227], v1, s[36:39], s76 offen sc1\n"
    "buffer_store_dwordx4 a[228:231], v1, s[36:39], s77 offen sc1\n"
    "buffer_store_dwordx4 a[232:235], v1, s[36:39], s78 offen sc1\n"
    "buffer_store_dwordx4 a[236:239], v1, s[36:39], s79 offen sc1\n"
    "s_cmp_eq_u32 s75, 0\n"
    "s_cbranch_scc1 L_rp_nost_%=\n"
    "buffer_store_dwordx4 a[240:243], v1, s[36:39], s80 offen sc1\n"
    "buffer_store_dwordx4 a[244:247], v1, s[36:39], s81 offen sc1\n"
    "buffer_store_dwordx4 a[248:251], v1, s[36:39], s82 offen sc1\n"
    "buffer_store_dwordx4 a[252:255], v1, s[36:39], s83 offen sc1\n"
    "L_rp_nost_%=:\n"
    "s_waitcnt lgkmcnt(0)\n"
    "v_mfma_f32_16x16x32_bf16 v[160:163], v[104:107], a[32:35], v[160:163]\n"
    "s_waitcnt vmcnt(22)\n"
    "s_barrier\n"
    "v_mfma_f32_16x16x32_bf16 v[164:167], v[104:107], a[36:39], v[164:167]\n"
    "ds_read_b128 a[40:43], v22 offset:32768\n"
    "s_add_u32 s60, s60, 20480\n"
    "v_mfma_f32_16x16x32_bf16 v[168:171], v[104:107], v[96:99], v[168:171]\n"
    "v_mul_u32_u24 v39, 0xf00, v39\n"
    "v_lshl_add_u32 v38, v38, 4, s6\n"
    "v_mfma_f32_16x16x32_bf16 v[172:175], v[104:107], v[100:103], v[172:175]\n"
    "ds_read_b64_tr_b16 v[132:133], v25 offset:0\n"
    "s_add_u32 m0, s58, 18432\n"
    "v_mfma_f32_16x16x32_bf16 v[188:191], v[108:111], v[100:103], v[188:191]\n"
    "ds_read_b64_tr_b16 v[134:135], v25 offset:512\n"
    "v_add3_u32 v29, v38, v39, 0\n"
    "v_mfma_f32_16x16x32_bf16 v[184:187], v[108:111], v[96:99], v[184:187]\n"
    "ds_read_b128 a[44:47], v22 offset:34816\n"
    "buffer_load_dwordx4 v16, s[24:27], s60 offen lds\n"
    "v_mfma_f32_16x16x32_bf16 v[180:183], v[108:111], a[36:39], v[180:183]\n"
    "ds_read_b128 v[124:127], v24 offset:32768\n"
    "v_add_u32 v38, 64, v0\n"
    "v_mfma_f32_16x16x32_bf16 v[176:179], v[108:111], a[32:35], v[176:179]\n"
    "ds_read_b128 v[128:131], v24 offset:34816\n"
    "v_mul_u32_u24 v39, 0x667, v38\n"
    "buffer_load_dwordx4 v17, s[24:27], s60 offen offset:1024 lds\n"
    "v_mfma_f32_16x16x32_bf16 v[192:195], v[112:115], a[32:35], v[192:195]\n"
    "ds_read_b64_tr_b16 v[136:137], v26 offset:0\n"
    "v_lshrrev_b32_e64 v39, 14, v39\n"
    "v_mfma_f32_16x16x32_bf16 v[196:199], v[112:115], a[36:39], v[196:199]\n"
    "ds_read_b64_tr_b16 v[138:139], v26 offset:512\n"
    "v_mul_u32_u24_e64 v40, 10, v39\n"
    "v_mfma_f32_16x16x32_bf16 v[200:203], v[112:115], v[96:99], v[200:203]\n"
    "ds_read_b64_tr_b16 v[140:141], v27 offset:0\n"
    "s_add_u32 m0, s59, 8192\n"
    "s_add_u32 s61, s61, 98304\n"
    "v_mfma_f32_16x16x32_bf16 v[204:207], v[112:115], v[100:103], v[204:207]\n"
    "ds_read_b64_tr_b16 v[142:143], v27 offset:512\n"
    "v_sub_u32 v38, v38, v40\n"
    "v_lshrrev_b32 v40, 1, v39\n"
    "v_mfma_f32_16x16x32_bf16 v[220:223], v[116:119], v[100:103], v[220:223]\n"
    "ds_read_b64_tr_b16 v[144:145], v28 offset:0\n"
    "buffer_load_dwordx4 v18, s[32:35], s61 offen lds\n"
    "v_mfma_f32_16x16x32_bf16 v[216:219], v[116:119], v[96:99], v[216:219]\n"
    "ds_read_b64_tr_b16 v[146:147], v28 offset:512\n"
    "v_and_b32_e64 v40, 4, v40\n"
    "v_mfma_f32_16x16x32_bf16 v[212:215], v[116:119], a[36:39], v[212:215]\n"
    "v_xor_b32_e64 v39, v39, v40\n"
    "buffer_load_dwordx4 v19, s[32:35], s61 offen offset:1024 lds\n"
    "v_mfma_f32_16x16x32_bf16 v[208:211], v[116:119], a[32:35], v[208:211]\n"
    "v_mul_u32_u24 v39, 0xf00, v39\n"
    "v_lshl_add_u32 v38, v38, 4, s6\n"
    "s_waitcnt lgkmcnt(0)\n"
    "v_mfma_f32_16x16x32_bf16 v[160:163], v[132:135], a[40:43], v[160:163]\n"
    "s_waitcnt vmcnt(16)\n"
    "s_barrier\n"
    "v_mfma_f32_16x16x32_bf16 v[164:167], v[132:135], a[44:47], v[164:167]\n"
    "ds_read_b128 a[48:51], v21 offset:0\n"
    "s_add_u32 s53, s53, 1\n"
    "v_mfma_f32_16x16x32_bf16 v[168:171], v[132:135], v[124:127], v[168:171]\n"
    "s_cmp_eq_u32 s53, 10\n"
    "s_cselect_b32 s53, 0, s53\n"
    "v_add3_u32 v30, v38, v39, 0\n"
    "v_mfma_f32_16x16x32_bf16 v[172:175], v[132:135], v[128:131], v[172:175]\n"
    "ds_read_b64_tr_b16 v[104:105], v25 offset:2048\n"
    "v_subrev_u32 v30, 1024, v30\n"
    "v_mfma_f32_16x16x32_bf16 v[188:191], v[136:139], v[128:131], v[188:191]\n"
    "ds_read_b64_tr_b16 v[106:107], v25 offset:2560\n"
    "s_lshl_b32 s60, s53, 7\n"
    "v_mfma_f32_16x16x32_bf16 v[184:187], v[136:139], v[124:127], v[184:187]\n"
    "ds_read_b128 a[52:55], v21 offset:2048\n"
    "s_add_u32 m0, s58, 32768\n"
    "v_mfma_f32_16x16x32_bf16 v[180:183], v[136:139], a[44:47], v[180:183]\n"
    "ds_read_b128 v[96:99], v23 offset:0\n"
    "v_add_u32 v38, 128, v0\n"
    "v_mul_u32_u24 v39, 0x667, v38\n"
    "v_mfma_f32_16x16x32_bf16 v[176:179], v[136:139], a[40:43], v[176:179]\n"
    "ds_read_b128 v[100:103], v23 offset:2048\n"
    "buffer_load_dwordx4 v16, s[24:27], s60 offen lds\n"
    "v_mfma_f32_16x16x32_bf16 v[192:195], v[140:143], a[40:43], v[192:195]\n"
    "ds_read_b64_tr_b16 v[108:109], v26 offset:2048\n"
    "buffer_load_dwordx4 v17, s[24:27], s60 offen offset:1024 lds\n"
    "v_mfma_f32_16x16x32_bf16 v[196:199], v[140:143], a[44:47], v[196:199]\n"
    "ds_read_b64_tr_b16 v[110:111], v26 offset:2560\n"
    "v_lshrrev_b32 v39, 14, v39\n"
    "v_mfma_f32_16x16x32_bf16 v[200:203], v[140:143], v[124:127], v[200:203]\n"
    "ds_read_b64_tr_b16 v[112:113], v27 offset:2048\n"
    "v_mul_u32_u24_e64 v40, 10, v39\n"
    "v_mfma_f32_16x16x32_bf16 v[204:207], v[140:143], v[128:131], v[204:207]\n"
    "ds_read_b64_tr_b16 v[114:115], v27 offset:2560\n"
    "s_add_u32 m0, s59, 0\n"
    "s_mul_i32 s61, s53, 196608\n"
    "v_mfma_f32_16x16x32_bf16 v[220:223], v[144:147], v[128:131], v[220:223]\n"
    "ds_read_b64_tr_b16 v[116:117], v28 offset:2048\n"
    "v_sub_u32 v38, v38, v40\n"
    "v_lshrrev_b32_e64 v40, 1, v39\n"
    "v_mfma_f32_16x16x32_bf16 v[216:219], v[144:147], v[124:127], v[216:219]\n"
    "ds_read_b64_tr_b16 v[118:119], v28 offset:2560\n"
    "buffer_load_dwordx4 v18, s[32:35], s61 offen lds\n"
    "v_mfma_f32_16x16x32_bf16 v[212:215], v[144:147], a[44:47], v[212:215]\n"
    "v_and_b32 v40, 4, v40\n"
    "v_xor_b32 v39, v39, v40\n"
    "v_mfma_f32_16x16x32_bf16 v[208:211], v[144:147], a[40:43], v[208:211]\n"
    "buffer_load_dwordx4 v19, s[32:35], s61 offen offset:1024 lds\n"
    "s_waitcnt lgkmcnt(0)\n"
    "v_mfma_f32_16x16x32_bf16 v[160:163], v[104:107], a[48:51], v[160:163]\n"
    "s_waitcnt vmcnt(18)\n"
    "s_barrier\n"
    "v_mfma_f32_16x16x32_bf16 v[164:167], v[104:107], a[52:55], v[164:167]\n"
    "ds_read_b128 a[56:59], v22 offset:0\n"
    "s_add_u32 s60, s60, 20480\n"
    "v_mfma_f32_16x16x32_bf16 v[168:171], v[104:107], v[96:99], v[168:171]\n"
    "v_mul_u32_u24 v39, 0xf00, v39\n"
    "v_lshl_add_u32 v38, v38, 4, s6\n"
    "v_mfma_f32_16x16x32_bf16 v[172:175], v[104:107], v[100:103], v[172:175]\n"
    "ds_read_b64_tr_b16 v[132:133], v25 offset:4096\n"
    "s_add_u32 m0, s58, 34816\n"
    "v_mfma_f32_16x16x32_bf16 v[188:191], v[108:111], v[100:103], v[188:191]\n"
    "ds_read_b64_tr_b16 v[134:135], v25 offset:4608\n"
    "v_add3_u32 v31, v38, v39, 0\n"
    "v_mfma_f32_16x16x32_bf16 v[184:187], v[108:111], v[96:99], v[184:187]\n"
    "ds_read_b128 a[60:63], v22 offset:2048\n"
    "v_subrev_u32 v31, 2048, v31\n"
    "buffer_load_dwordx4 v16, s[24:27], s60 offen lds\n"
    "v_mfma_f32_16x16x32_bf16 v[180:183], v[108:111], a[52:55], v[180:183]\n"
    "ds_read_b128 v[124:127], v24 offset:0\n"
    "v_add_u32 v38, 192, v0\n"
    "v_mfma_f32_16x16x32_bf16 v[176:179], v[108:111], a[48:51], v[176:179]\n"
    "ds_read_b128 v[128:131], v24 offset:2048\n"
    "v_mul_u32_u24 v39, 0x667, v38\n"
    "v_mfma_f32_16x16x32_bf16 v[192:195], v[112:115], a[48:51], v[192:195]\n"
    "ds_read_b64_tr_b16 v[136:137], v26 offset:4096\n"
    "buffer_load_dwordx4 v17, s[24:27], s60 offen offset:1024 lds\n"
    "v_mfma_f32_16x16x32_bf16 v[196:199], v[112:115], a[52:55], v[196:199]\n"
    "ds_read_b64_tr_b16 v[138:139], v26 offset:4608\n"
    "v_lshrrev_b32 v39, 14, v39\n"
    "v_mfma_f32_16x16x32_bf16 v[200:203], v[112:115], v[96:99], v[200:203]\n"
    "ds_read_b64_tr_b16 v[140:141], v27 offset:4096\n"
    "v_mul_u32_u24_e64 v40, 10, v39\n"
    "v_mfma_f32_16x16x32_bf16 v[204:207], v[112:115], v[100:103], v[204:207]\n"
    "ds_read_b64_tr_b16 v[142:143], v27 offset:4608\n"
    "s_add_u32 m0, s59, 2048\n"
    "s_add_u32 s61, s61, 98304\n"
    "v_mfma_f32_16x16x32_bf16 v[220:223], v[116:119], v[100:103], v[220:223]\n"
    "ds_read_b64_tr_b16 v[144:145], v28 offset:4096\n"
    "v_sub_u32 v38, v38, v40\n"
    "v_lshrrev_b32 v40, 1, v39\n"
    "v_mfma_f32_16x16x32_bf16 v[216:219], v[116:119], v[96:99], v[216:219]\n"
    "ds_read_b64_tr_b16 v[146:147], v28 offset:4608\n"
    "buffer_load_dwordx4 v18, s[32:35], s61 offen lds\n"
    "v_mfma_f32_16x16x32_bf16 v[212:215], v[116:119], a[52:55], v[212:215]\n"
    "v_and_b32 v40, 4, v40\n"
    "v_xor_b32 v39, v39, v40\n"
    "v_mfma_f32_16x16x32_bf16 v[208:211], v[116:119], a[48:51], v[208:211]\n"
    "buffer_load_dwordx4 v19, s[32:35], s61 offen offset:1024 lds\n"
    "s_waitcnt lgkmcnt(0)\n"
    "v_mfma_f32_16x16x32_bf16 v[160:163], v[132:135], a[56:59], v[160:163]\n"
    "s_waitcnt vmcnt(10)\n"
    "s_barrier\n"
    "v_mfma_f32_16x16x32_bf16 v[164:167], v[132:135], a[60:63], v[164:167]\n"
    "ds_read_b128 a[64:67], v21 offset:16384\n"
    "s_add_u32 s53, s53, 1\n"
    "v_mfma_f32_16x16x32_bf16 v[168:171], v[132:135], v[124:127], v[168:171]\n"
    "s_cmp_eq_u32 s53, 10\n"
    "s_cselect_b32 s53, 0, s53\n"
    "v_mul_u32_u24 v39, 0xf00, v39\n"
    "v_mfma_f32_16x16x32_bf16 v[172:175], v[132:135], v[128:131], v[172:175]\n"
    "ds_read_b64_tr_b16 v[104:105], v25 offset:6144\n"
    "v_lshl_add_u32 v38, v38, 4, s6\n"
    "v_mfma_f32_16x16x32_bf16 v[188:191], v[136:139], v[128:131], v[188:191]\n"
    "ds_read_b64_tr_b16 v[106:107], v25 offset:6656\n"
    "s_lshl_b32 s60, s53, 7\n"
    "v_mfma_f32_16x16x32_bf16 v[184:187], v[136:139], v[124:127], v[184:187]\n"
    "ds_read_b128 a[68:71], v21 offset:18432\n"
    "s_add_u32 m0, s58, 0\n"
    "v_add3_u32 v32, v38, v39, 0\n"
    "v_mfma_f32_16x16x32_bf16 v[180:183], v[136:139], a[60:63], v[180:183]\n"
    "ds_read_b128 v[96:99], v23 offset:16384\n"
    "v_subrev_u32 v32, 3072, v32\n"
    "buffer_load_dwordx4 v16, s[24:27], s60 offen lds\n"
    "v_mfma_f32_16x16x32_bf16 v[176:179], v[136:139], a[56:59], v[176:179]\n"
    "ds_read_b128 v[100:103], v23 offset:18432\n"
    "v_add_u32 v38, 256, v0\n"
    "v_mfma_f32_16x16x32_bf16 v[192:195], v[140:143], a[56:59], v[192:195]\n"
    "ds_read_b64_tr_b16 v[108:109], v26 offset:6144\n"
    "v_mul_u32_u24 v39, 0x667, v38\n"
    "v_mfma_f32_16x16x32_bf16 v[196:199], v[140:143], a[60:63], v[196:199]\n"
    "ds_read_b64_tr_b16 v[110:111], v26 offset:6656\n"
    "buffer_load_dwordx4 v17, s[24:27], s60 offen offset:1024 lds\n"
    "v_mfma_f32_16x16x32_bf16 v[200:203], v[140:143], v[124:127], v[200:203]\n"
    "ds_read_b64_tr_b16 v[112:113], v27 offset:6144\n"
    "v_lshrrev_b32_e64 v39, 14, v39\n"
    "v_mfma_f32_16x16x32_bf16 v[204:207], v[140:143], v[128:131], v[204:207]\n"
    "ds_read_b64_tr_b16 v[114:115], v27 offset:6656\n"
    "v_mul_u32_u24_e64 v40, 10, v39\n"
    "s_add_u32 m0, s59, 4096\n"
    "v_mfma_f32_16x16x32_bf16 v[220:223], v[144:147], v[128:131], v[220:223]\n"
    "ds_read_b64_tr_b16 v[116:117], v28 offset:6144\n"
    "s_mul_i32 s61, s53, 196608\n"
    "v_sub_u32_e64 v38, v38, v40\n"
    "v_mfma_f32_16x16x32_bf16 v[216:219], v[144:147], v[124:127], v[216:219]\n"
    "ds_read_b64_tr_b16 v[118:119], v28 offset:6656\n"
    "v_lshrrev_b32_e64 v40, 1, v39\n"
    "buffer_load_dwordx4 v18, s[32:35], s61 offen lds\n"
    "v_mfma_f32_16x16x32_bf16 v[212:215], v[144:147], a[60:63], v[212:215]\n"
    "v_and_b32 v40, 4, v40\n"
    "v_xor_b32 v39, v39, v40\n"
    "v_mfma_f32_16x16x32_bf16 v[208:211], v[144:147], a[56:59], v[208:211]\n"
    "buffer_load_dwordx4 v19, s[32:35], s61 offen offset:1024 lds\n"
    "s_waitcnt lgkmcnt(0)\n"
    "v_mfma_f32_16x16x32_bf16 v[160:163], v[104:107], a[64:67], v[160:163]\n"
    "s_waitcnt vmcnt(12)\n"
    "s_barrier\n"
    "v_mfma_f32_16x16x32_bf16 v[164:167], v[104:107], a[68:71], v[164:167]\n"
    "ds_read_b128 a[72:75], v22 offset:16384\n"
    "s_mul_i32 s67, s2, 16\n"
    "v_mfma_f32_16x16x32_bf16 v[168:171], v[104:107], v[96:99], v[168:171]\n"
    "s_add_u32 s67, s67, 65536\n"
    "s_add_u32 s60, s60, 20480\n"
    "v_cmp_gt_u32_e64 vcc, 4, v0\n"
    "v_mfma_f32_16x16x32_bf16 v[172:175], v[104:107], v[100:103], v[172:175]\n"
    "ds_read_b64_tr_b16 v[132:133], v25 offset:8192\n"
    "v_mov_b32 v47, 2147483648\n"
    "v_mfma_f32_16x16x32_bf16 v[188:191], v[108:111], v[100:103], v[188:191]\n"
    "ds_read_b64_tr_b16 v[134:135], v25 offset:8704\n"
    "v_lshlrev_b32 v46, 2, v0\n"
    "v_cndmask_b32 v46, v47, v46, vcc\n"
    "s_add_u32 m0, s58, 2048\n"
    "v_mfma_f32_16x16x32_bf16 v[184:187], v[108:111], v[96:99], v[184:187]\n"
    "ds_read_b128 a[76:79], v22 offset:18432\n"
    "s_cmp_eq_u32 s3, 0\n"
    "s_cbranch_scc0 L_f1s_q_%=\n"
    "buffer_store_dword v248, v46, s[36:39], s67 offen sc1\n"
    "L_f1s_q_%=:\n"
    "v_mfma_f32_16x16x32_bf16 v[180:183], v[108:111], a[68:71], v[180:183]\n"
    "ds_read_b128 v[124:127], v24 offset:16384\n"
    "v_mfma_f32_16x16x32_bf16 v[176:179], v[108:111], a[64:67], v[176:179]\n"
    "ds_read_b128 v[128:131], v24 offset:18432\n"
    "buffer_load_dwordx4 v16, s[24:27], s60 offen lds\n"
    "v_mfma_f32_16x16x32_bf16 v[192:195], v[112:115], a[64:67], v[192:195]\n"
    "ds_read_b64_tr_b16 v[136:137], v26 offset:8192\n"
    "v_mul_u32_u24 v39, 0xf00, v39\n"
    "v_mfma_f32_16x16x32_bf16 v[196:199], v[112:115], a[68:71], v[196:199]\n"
    "ds_read_b64_tr_b16 v[138:139], v26 offset:8704\n"
    "v_lshl_add_u32 v38, v38, 4, s6\n"
    "buffer_load_dwordx4 v17, s[24:27], s60 offen offset:1024 lds\n"
    "v_mfma_f32_16x16x32_bf16 v[200:203], v[112:115], v[96:99], v[200:203]\n"
    "ds_read_b64_tr_b16 v[140:141], v27 offset:8192\n"
    "v_add3_u32 v33, v38, v39, 0\n"
    "v_mfma_f32_16x16x32_bf16 v[204:207], v[112:115], v[100:103], v[204:207]\n"
    "ds_read_b64_tr_b16 v[142:143], v27 offset:8704\n"
    "v_lshrrev_b32_e64 v38, 4, v0\n"
    "v_bfe_u32 v39, v0, 2, 2\n"
    "s_add_u32 m0, s59, 6144\n"
    "v_mfma_f32_16x16x32_bf16 v[220:223], v[116:119], v[100:103], v[220:223]\n"
    "ds_read_b64_tr_b16 v[144:145], v28 offset:8192\n"
    "s_add_u32 s61, s61, 98304\n"
    "v_lshl_add_u32 v39, v38, 3, v39\n"
    "v_mfma_f32_16x16x32_bf16 v[216:219], v[116:119], v[96:99], v[216:219]\n"
    "ds_read_b64_tr_b16 v[146:147], v28 offset:8704\n"
    "v_mul_u32_u24 v39, 0xa0, v39\n"
    "buffer_load_dwordx4 v18, s[32:35], s61 offen lds\n"
    "v_mfma_f32_16x16x32_bf16 v[212:215], v[116:119], a[68:71], v[212:215]\n"
    "v_and_b32_e64 v40, 3, v0\n"
    "v_lshl_add_u32 v39, v40, 3, v39\n"
    "v_mfma_f32_16x16x32_bf16 v[208:211], v[116:119], a[64:67], v[208:211]\n"
    "buffer_load_dwordx4 v19, s[32:35], s61 offen offset:1024 lds\n"
    "v_and_b32_e64 v38, 1, v38\n"
    "v_mul_u32_u24 v38, 0x280, v38\n"
    "s_waitcnt lgkmcnt(0)\n"
    "v_mfma_f32_16x16x32_bf16 v[160:163], v[132:135], a[72:75], v[160:163]\n"
    "s_waitcnt vmcnt(10)\n"
    "s_barrier\n"
    "v_mfma_f32_16x16x32_bf16 v[164:167], v[132:135], a[76:79], v[164:167]\n"
    "ds_read_b128 a[80:83], v21 offset:32768\n"
    "v_add_u32 v39, 90112, v39\n"
    "v_mfma_f32_16x16x32_bf16 v[168:171], v[132:135], v[124:127], v[168:171]\n"
    "s_add_u32 s53, s53, 1\n"
    "s_cmp_eq_u32 s53, 10\n"
    "s_cselect_b32 s53, 0, s53\n"
    "v_mfma_f32_16x16x32_bf16 v[172:175], v[132:135], v[128:131], v[172:175]\n"
    "ds_read_b64_tr_b16 v[104:105], v25 offset:0\n"
    "v_add_u32_e64 v34, v39, v38\n"
    "v_mfma_f32_16x16x32_bf16 v[188:191], v[136:139], v[128:131], v[188:191]\n"
    "ds_read_b64_tr_b16 v[106:107], v25 offset:512\n"
    "v_add_u32 v39, 0x280, v39\n"
    "v_mfma_f32_16x16x32_bf16 v[184:187], v[136:139], v[124:127], v[184:187]\n"
    "ds_read_b128 a[84:87], v21 offset:34816\n"
    "s_lshl_b32 s60, s53, 7\n"
    "v_mfma_f32_16x16x32_bf16 v[180:183], v[136:139], a[76:79], v[180:183]\n"
    "ds_read_b128 v[96:99], v23 offset:32768\n"
    "s_add_u32 m0, s58, 16384\n"
    "v_sub_u32 v35, v39, v38\n"
    "v_mfma_f32_16x16x32_bf16 v[176:179], v[136:139], a[72:75], v[176:179]\n"
    "ds_read_b128 v[100:103], v23 offset:34816\n"
    "buffer_load_dwordx4 v16, s[24:27], s60 offen lds\n"
    "v_mfma_f32_16x16x32_bf16 v[192:195], v[140:143], a[72:75], v[192:195]\n"
    "ds_read_b64_tr_b16 v[108:109], v26 offset:0\n"
    "s_mov_b32 s70, 0x358637bd\n"
    "v_mfma_f32_16x16x32_bf16 v[196:199], v[140:143], a[76:79], v[196:199]\n"
    "ds_read_b64_tr_b16 v[110:111], v26 offset:512\n"
    "buffer_load_dwordx4 v17, s[24:27], s60 offen offset:1024 lds\n"
    "v_mfma_f32_16x16x32_bf16 v[200:203], v[140:143], v[124:127], v[200:203]\n"
    "ds_read_b64_tr_b16 v[112:113], v27 offset:0\n"
    "v_mov_b32_e64 v224, s70\n"
    "v_mfma_f32_16x16x32_bf16 v[204:207], v[140:143], v[128:131], v[204:207]\n"
    "ds_read_b64_tr_b16 v[114:115], v27 offset:512\n"
    "v_mov_b32_e64 v225, s70\n"
    "s_add_u32 m0, s59, 8192\n"
    "v_mfma_f32_16x16x32_bf16 v[220:223], v[144:147], v[128:131], v[220:223]\n"
    "ds_read_b64_tr_b16 v[116:117], v28 offset:0\n"
    "s_mul_i32 s61, s53, 196608\n"
    "v_mov_b32_e64 v226, s70\n"
    "v_mfma_f32_16x16x32_bf16 v[216:219], v[144:147], v[124:127], v[216:219]\n"
    "ds_read_b64_tr_b16 v[118:119], v28 offset:512\n"
    "buffer_load_dwordx4 v18, s[32:35], s61 offen lds\n"
    "v_mfma_f32_16x16x32_bf16 v[212:215], v[144:147], a[76:79], v[212:215]\n"
    "v_mov_b32_e64 v227, s70\n"
    "v_mfma_f32_16x16x32_bf16 v[208:211], v[144:147], a[72:75], v[208:211]\n"
    "buffer_load_dwordx4 v19, s[32:35], s61 offen offset:1024 lds\n"
    "s_lshl1_add_u32 s6, s4, s56\n"
    "s_mul_i32 s6, s6, 24\n"
    "s_waitcnt lgkmcnt(0)\n"
    "v_mfma_f32_16x16x32_bf16 v[160:163], v[104:107], a[80:83], v[160:163]\n"
    "s_waitcnt vmcnt(12)\n"
    "s_barrier\n"
    "v_mfma_f32_16x16x32_bf16 v[164:167], v[104:107], a[84:87], v[164:167]\n"
    "ds_read_b128 a[88:91], v22 offset:32768\n"
    "s_add_u32 s60, s60, 20480\n"
    "v_mfma_f32_16x16x32_bf16 v[168:171], v[104:107], v[96:99], v[168:171]\n"
    "s_add_u32 s17, s17, s6\n"
    "s_add_u32 m0, s58, 18432\n"
    "s_add_u32 s17, s17, s57\n"
    "v_mfma_f32_16x16x32_bf16 v[172:175], v[104:107], v[100:103], v[172:175]\n"
    "ds_read_b64_tr_b16 v[132:133], v25 offset:2048\n"
    "v_mfma_f32_16x16x32_bf16 v[188:191], v[108:111], v[100:103], v[188:191]\n"
    "ds_read_b64_tr_b16 v[134:135], v25 offset:2560\n"
    "buffer_load_dwordx4 v16, s[24:27], s60 offen lds\n"
    "v_mfma_f32_16x16x32_bf16 v[184:187], v[108:111], v[96:99], v[184:187]\n"
    "ds_read_b128 a[92:95], v22 offset:34816\n"
    "s_lshl2_add_u32 s17, s17, s57\n"
    "v_mfma_f32_16x16x32_bf16 v[180:183], v[108:111], a[84:87], v[180:183]\n"
    "ds_read_b128 v[124:127], v24 offset:32768\n"
    "s_lshl_b32 s17, s17, 11\n"
    "v_mfma_f32_16x16x32_bf16 v[176:179], v[108:111], a[80:83], v[176:179]\n"
    "ds_read_b128 v[128:131], v24 offset:34816\n"
    "buffer_load_dwordx4 v17, s[24:27], s60 offen offset:1024 lds\n"
    "v_mfma_f32_16x16x32_bf16 v[192:195], v[112:115], a[80:83], v[192:195]\n"
    "ds_read_b64_tr_b16 v[136:137], v26 offset:2048\n"
    "s_xor_b32 s45, s17, 0x800\n"
    "v_mfma_f32_16x16x32_bf16 v[196:199], v[112:115], a[84:87], v[196:199]\n"
    "ds_read_b64_tr_b16 v[138:139], v26 offset:2560\n"
    "s_add_u32 s17, s17, 7110656\n"
    "v_mfma_f32_16x16x32_bf16 v[200:203], v[112:115], v[96:99], v[200:203]\n"
    "ds_read_b64_tr_b16 v[140:141], v27 offset:2048\n"
    "s_add_u32 m0, s59, 0\n"
    "s_add_u32 s61, s61, 98304\n"
    "v_mfma_f32_16x16x32_bf16 v[204:207], v[112:115], v[100:103], v[204:207]\n"
    "ds_read_b64_tr_b16 v[142:143], v27 offset:2560\n"
    "s_add_u32 s45, s45, 7110656\n"
    "buffer_load_dwordx4 v18, s[32:35], s61 offen lds\n"
    "v_mfma_f32_16x16x32_bf16 v[220:223], v[116:119], v[100:103], v[220:223]\n"
    "ds_read_b64_tr_b16 v[144:145], v28 offset:2048\n"
    "v_add_u32 v247, 4096, v1\n"
    "v_mfma_f32_16x16x32_bf16 v[216:219], v[116:119], v[96:99], v[216:219]\n"
    "ds_read_b64_tr_b16 v[146:147], v28 offset:2560\n"
    "buffer_load_dwordx4 v19, s[32:35], s61 offen offset:1024 lds\n"
    "v_mfma_f32_16x16x32_bf16 v[212:215], v[116:119], a[84:87], v[212:215]\n"
    "v_cmp_gt_u32_e64 vcc, 32, v0\n"
    "v_mov_b32 v38, 2147483648\n"
    "v_mfma_f32_16x16x32_bf16 v[208:211], v[116:119], a[80:83], v[208:211]\n"
    "v_lshlrev_b32_e64 v249, 2, v0\n"
    "v_cndmask_b32 v249, v38, v249, vcc\n"
    "s_waitcnt lgkmcnt(0)\n"
    "v_mfma_f32_16x16x32_bf16 v[160:163], v[132:135], a[88:91], v[160:163]\n"
    "s_waitcnt vmcnt(10)\n"
    "s_barrier\n"
    "v_mfma_f32_16x16x32_bf16 v[164:167], v[132:135], a[92:95], v[164:167]\n"
    "ds_read_b128 a[96:99], v21 offset:0\n"
    "s_add_u32 s53, s53, 1\n"
    "v_mfma_f32_16x16x32_bf16 v[168:171], v[132:135], v[124:127], v[168:171]\n"
    "s_cmp_eq_u32 s53, 10\n"
    "s_cselect_b32 s53, 0, s53\n"
    "s_bfe_u32 s6, s2, 0x20001\n"
    "v_mfma_f32_16x16x32_bf16 v[172:175], v[132:135], v[128:131], v[172:175]\n"
    "ds_read_b64_tr_b16 v[104:105], v25 offset:4096\n"
    "s_lshr_b32 s54, s52, 3\n"
    "v_mfma_f32_16x16x32_bf16 v[188:191], v[136:139], v[128:131], v[188:191]\n"
    "ds_read_b64_tr_b16 v[106:107], v25 offset:4608\n"
    "s_lshl_b32 s60, s53, 7\n"
    "v_mfma_f32_16x16x32_bf16 v[184:187], v[136:139], v[124:127], v[184:187]\n"
    "ds_read_b128 a[100:103], v21 offset:2048\n"
    "s_add_u32 m0, s58, 32768\n"
    "s_lshl2_add_u32 s54, s6, s54\n"
    "v_mfma_f32_16x16x32_bf16 v[180:183], v[136:139], a[92:95], v[180:183]\n"
    "ds_read_b128 v[96:99], v23 offset:0\n"
    "s_bfe_u32 s6, s52, 0x10001\n"
    "buffer_load_dwordx4 v16, s[24:27], s60 offen lds\n"
    "v_mfma_f32_16x16x32_bf16 v[176:179], v[136:139], a[88:91], v[176:179]\n"
    "ds_read_b128 v[100:103], v23 offset:2048\n"
    "s_and_b32 s55, s52, 1\n"
    "v_mfma_f32_16x16x32_bf16 v[192:195], v[140:143], a[88:91], v[192:195]\n"
    "ds_read_b64_tr_b16 v[108:109], v26 offset:4096\n"
    "s_xor_b32 s6, s6, s55\n"
    "v_mfma_f32_16x16x32_bf16 v[196:199], v[140:143], a[92:95], v[196:199]\n"
    "ds_read_b64_tr_b16 v[110:111], v26 offset:4608\n"
    "buffer_load_dwordx4 v17, s[24:27], s60 offen offset:1024 lds\n"
    "v_mfma_f32_16x16x32_bf16 v[200:203], v[140:143], v[124:127], v[200:203]\n"
    "ds_read_b64_tr_b16 v[112:113], v27 offset:4096\n"
    "s_lshl1_add_u32 s54, s54, s6\n"
    "v_mfma_f32_16x16x32_bf16 v[204:207], v[140:143], v[128:131], v[204:207]\n"
    "ds_read_b64_tr_b16 v[114:115], v27 offset:4608\n"
    "s_bfe_u32 s6, s52, 0x10002\n"
    "s_add_u32 m0, s59, 2048\n"
    "v_mfma_f32_16x16x32_bf16 v[220:223], v[144:147], v[128:131], v[220:223]\n"
    "ds_read_b64_tr_b16 v[116:117], v28 offset:4096\n"
    "s_mul_i32 s61, s53, 196608\n"
    "s_lshl1_add_u32 s55, s6, s55\n"
    "v_mfma_f32_16x16x32_bf16 v[216:219], v[144:147], v[124:127], v[216:219]\n"
    "ds_read_b64_tr_b16 v[118:119], v28 offset:4608\n"
    "s_and_b32 s6, s2, 1\n"
    "buffer_load_dwordx4 v18, s[32:35], s61 offen lds\n"
    "v_mfma_f32_16x16x32_bf16 v[212:215], v[144:147], a[92:95], v[212:215]\n"
    "s_lshl2_add_u32 s55, s6, s55\n"
    "s_and_b32 s47, s54, 7\n"
    "v_mfma_f32_16x16x32_bf16 v[208:211], v[144:147], a[88:91], v[208:211]\n"
    "buffer_load_dwordx4 v19, s[32:35], s61 offen offset:1024 lds\n"
    "s_waitcnt lgkmcnt(0)\n"
    "v_mfma_f32_16x16x32_bf16 v[160:163], v[104:107], a[96:99], v[160:163]\n"
    "s_waitcnt vmcnt(12)\n"
    "s_barrier\n"
    "v_mfma_f32_16x16x32_bf16 v[164:167], v[104:107], a[100:103], v[164:167]\n"
    "ds_read_b128 a[104:107], v22 offset:0\n"
    "s_add_u32 s60, s60, 20480\n"
    "v_mfma_f32_16x16x32_bf16 v[168:171], v[104:107], v[96:99], v[168:171]\n"
    "s_lshl2_add_u32 s64, s55, s3\n"
    "s_mul_i32 s64, s64, 87040\n"
    "s_add_u32 m0, s58, 34816\n"
    "v_mfma_f32_16x16x32_bf16 v[172:175], v[104:107], v[100:103], v[172:175]\n"
    "ds_read_b64_tr_b16 v[132:133], v25 offset:6144\n"
    "s_add_u32 s64, s64, 1703936\n"
    "v_mfma_f32_16x16x32_bf16 v[188:191], v[108:111], v[100:103], v[188:191]\n"
    "ds_read_b64_tr_b16 v[134:135], v25 offset:6656\n"
    "s_add_u32 s72, s64, 61440\n"
    "v_mfma_f32_16x16x32_bf16 v[184:187], v[108:111], v[96:99], v[184:187]\n"
    "ds_read_b128 a[108:111], v22 offset:2048\n"
    "v_mfma_f32_16x16x32_bf16 v[180:183], v[108:111], a[100:103], v[180:183]\n"
    "ds_read_b128 v[124:127], v24 offset:0\n"
    "buffer_load_dwordx4 v16, s[24:27], s60 offen lds\n"
    "v_mfma_f32_16x16x32_bf16 v[176:179], v[108:111], a[96:99], v[176:179]\n"
    "ds_read_b128 v[128:131], v24 offset:2048\n"
    "s_mov_b32 s68, 0x3fb8aa3b\n"
    "v_mfma_f32_16x16x32_bf16 v[192:195], v[112:115], a[96:99], v[192:195]\n"
    "ds_read_b64_tr_b16 v[136:137], v26 offset:6144\n"
    "s_mov_b32 s69, 0xb5c1a2a0\n"
    "buffer_load_dwordx4 v17, s[24:27], s60 offen offset:1024 lds\n"
    "v_mfma_f32_16x16x32_bf16 v[196:199], v[112:115], a[100:103], v[196:199]\n"
    "ds_read_b64_tr_b16 v[138:139], v26 offset:6656\n"
    "s_lshr_b32 s6, s47, 1\n"
    "v_mfma_f32_16x16x32_bf16 v[200:203], v[112:115], v[96:99], v[200:203]\n"
    "ds_read_b64_tr_b16 v[140:141], v27 offset:6144\n"
    "s_add_u32 m0, s59, 4096\n"
    "v_mfma_f32_16x16x32_bf16 v[204:207], v[112:115], v[100:103], v[204:207]\n"
    "ds_read_b64_tr_b16 v[142:143], v27 offset:6656\n"
    "s_add_u32 s61, s61, 98304\n"
    "v_mfma_f32_16x16x32_bf16 v[220:223], v[116:119], v[100:103], v[220:223]\n"
    "ds_read_b64_tr_b16 v[144:145], v28 offset:6144\n"
    "s_mul_i32 s6, s6, 5120\n"
    "s_add_u32 s64, s64, s6\n"
    "v_mfma_f32_16x16x32_bf16 v[216:219], v[116:119], v[96:99], v[216:219]\n"
    "ds_read_b64_tr_b16 v[146:147], v28 offset:6656\n"
    "buffer_load_dwordx4 v18, s[32:35], s61 offen lds\n"
    "v_mfma_f32_16x16x32_bf16 v[212:215], v[116:119], a[100:103], v[212:215]\n"
    "s_add_u32 s64, s64, 40960\n"
    "s_mul_i32 s73, s3, 25600\n"
    "v_mfma_f32_16x16x32_bf16 v[208:211], v[116:119], a[96:99], v[208:211]\n"
    "buffer_load_dwordx4 v19, s[32:35], s61 offen offset:1024 lds\n"
    "s_waitcnt lgkmcnt(0)\n"
    "v_mfma_f32_16x16x32_bf16 v[160:163], v[132:135], a[104:107], v[160:163]\n"
    "s_waitcnt vmcnt(10)\n"
    "s_barrier\n"
    "v_mfma_f32_16x16x32_bf16 v[164:167], v[132:135], a[108:111], v[164:167]\n"
    "ds_read_b128 a[112:115], v21 offset:16384\n"
    "s_add_u32 s53, s53, 1\n"
    "v_mfma_f32_16x16x32_bf16 v[168:171], v[132:135], v[124:127], v[168:171]\n"
    "s_cmp_eq_u32 s53, 10\n"
    "s_cselect_b32 s53, 0, s53\n"
    "s_lshr_b32 s6, s54, 1\n"
    "v_mfma_f32_16x16x32_bf16 v[172:175], v[132:135], v[128:131], v[172:175]\n"
    "ds_read_b64_tr_b16 v[104:105], v25 offset:8192\n"
    "s_lshl2_add_u32 s16, s6, s3\n"
    "v_mfma_f32_16x16x32_bf16 v[188:191], v[136:139], v[128:131], v[188:191]\n"
    "ds_read_b64_tr_b16 v[106:107], v25 offset:8704\n"
    "s_lshl_b32 s60, s53, 7\n"
    "v_mfma_f32_16x16x32_bf16 v[184:187], v[136:139], v[124:127], v[184:187]\n"
    "ds_read_b128 a[116:119], v21 offset:18432\n"
    "s_add_u32 m0, s58, 0\n"
    "v_mfma_f32_16x16x32_bf16 v[180:183], v[136:139], a[108:111], v[180:183]\n"
    "ds_read_b128 v[96:99], v23 offset:16384\n"
    "s_and_b32 s6, s54, 1\n"
    "s_lshl3_add_u32 s16, s16, s6\n"
    "v_mfma_f32_16x16x32_bf16 v[176:179], v[136:139], a[104:107], v[176:179]\n"
    "ds_read_b128 v[100:103], v23 offset:18432\n"
    "buffer_load_dwordx4 v16, s[24:27], s60 offen lds\n"
    "v_mfma_f32_16x16x32_bf16 v[192:195], v[140:143], a[104:107], v[192:195]\n"
    "ds_read_b64_tr_b16 v[108:109], v26 offset:8192\n"
    "s_lshl_b32 s16, s16, 8\n"
    "v_mfma_f32_16x16x32_bf16 v[196:199], v[140:143], a[108:111], v[196:199]\n"
    "ds_read_b64_tr_b16 v[110:111], v26 offset:8704\n"
    "s_add_u32 s16, s16, 131072\n"
    "buffer_load_dwordx4 v17, s[24:27], s60 offen offset:1024 lds\n"
    "v_mfma_f32_16x16x32_bf16 v[200:203], v[140:143], v[124:127], v[200:203]\n"
    "ds_read_b64_tr_b16 v[112:113], v27 offset:8192\n"
    "v_min_u32_e64 v6, 3, v0\n"
    "v_mfma_f32_16x16x32_bf16 v[204:207], v[140:143], v[128:131], v[204:207]\n"
    "ds_read_b64_tr_b16 v[114:115], v27 offset:8704\n"
    "v_lshrrev_b32_e64 v38, 1, v6\n"
    "v_mfma_f32_16x16x32_bf16 v[220:223], v[144:147], v[128:131], v[220:223]\n"
    "ds_read_b64_tr_b16 v[116:117], v28 offset:8192\n"
    "s_add_u32 m0, s59, 6144\n"
    "s_mul_i32 s61, s53, 196608\n"
    "v_mfma_f32_16x16x32_bf16 v[216:219], v[144:147], v[124:127], v[216:219]\n"
    "ds_read_b64_tr_b16 v[118:119], v28 offset:8704\n"
    "v_and_b32_e64 v6, 1, v6\n"
    "v_lshl_add_u32 v6, v38, 3, v6\n"
    "v_mfma_f32_16x16x32_bf16 v[212:215], v[144:147], a[108:111], v[212:215]\n"
    "buffer_load_dwordx4 v18, s[32:35], s61 offen lds\n"
    "v_mfma_f32_16x16x32_bf16 v[208:211], v[144:147], a[104:107], v[208:211]\n"
    "v_lshlrev_b32_e64 v6, 7, v6\n"
    "buffer_load_dwordx4 v19, s[32:35], s61 offen offset:1024 lds\n"
    "s_waitcnt lgkmcnt(0)\n"
    "v_mfma_f32_16x16x32_bf16 v[160:163], v[104:107], a[112:115], v[160:163]\n"
    "s_waitcnt vmcnt(12)\n"
    "s_barrier\n"
    "v_mfma_f32_16x16x32_bf16 v[164:167], v[104:107], a[116:119], v[164:167]\n"
    "ds_read_b128 a[120:123], v22 offset:16384\n"
    "s_add_u32 s60, s60, 20480\n"
    "v_mfma_f32_16x16x32_bf16 v[168:171], v[104:107], v[96:99], v[168:171]\n"
    "s_lshl1_add_u32 s65, s4, s56\n"
    "s_lshl_b32 s65, s65, 9\n"
    "s_add_u32 m0, s58, 2048\n"
    "v_mfma_f32_16x16x32_bf16 v[172:175], v[104:107], v[100:103], v[172:175]\n"
    "ds_read_b64_tr_b16 v[132:133], v25 offset:0\n"
    "s_add_u32 s65, s65, 16384\n"
    "v_mfma_f32_16x16x32_bf16 v[188:191], v[108:111], v[100:103], v[188:191]\n"
    "ds_read_b64_tr_b16 v[134:135], v25 offset:512\n"
    "v_and_b32 v45, 15, v0\n"
    "v_mfma_f32_16x16x32_bf16 v[184:187], v[108:111], v[96:99], v[184:187]\n"
    "ds_read_b128 a[124:127], v22 offset:18432\n"
    "v_mfma_f32_16x16x32_bf16 v[180:183], v[108:111], a[116:119], v[180:183]\n"
    "ds_read_b128 v[124:127], v24 offset:16384\n"
    "buffer_load_dwordx4 v16, s[24:27], s60 offen lds\n"
    "v_mfma_f32_16x16x32_bf16 v[176:179], v[108:111], a[112:115], v[176:179]\n"
    "ds_read_b128 v[128:131], v24 offset:18432\n"
    "v_lshlrev_b32_e64 v45, 3, v45\n"
    "v_mfma_f32_16x16x32_bf16 v[192:195], v[112:115], a[112:115], v[192:195]\n"
    "ds_read_b64_tr_b16 v[136:137], v26 offset:0\n"
    "buffer_load_dwordx4 v17, s[24:27], s60 offen offset:1024 lds\n"
    "v_mfma_f32_16x16x32_bf16 v[196:199], v[112:115], a[116:119], v[196:199]\n"
    "ds_read_b64_tr_b16 v[138:139], v26 offset:512\n"
    "v_lshl_add_u32 v45, s57, 8, v45\n"
    "v_mfma_f32_16x16x32_bf16 v[200:203], v[112:115], v[96:99], v[200:203]\n"
    "ds_read_b64_tr_b16 v[140:141], v27 offset:0\n"
    "s_add_u32 m0, s59, 8192\n"
    "v_mfma_f32_16x16x32_bf16 v[204:207], v[112:115], v[100:103], v[204:207]\n"
    "ds_read_b64_tr_b16 v[142:143], v27 offset:512\n"
    "s_add_u32 s61, s61, 98304\n"
    "v_xor_b32 v46, 0x100, v45\n"
    "v_mfma_f32_16x16x32_bf16 v[220:223], v[116:119], v[100:103], v[220:223]\n"
    "ds_read_b64_tr_b16 v[144:145], v28 offset:0\n"
    "buffer_load_dwordx4 v18, s[32:35], s61 offen lds\n"
    "v_mfma_f32_16x16x32_bf16 v[216:219], v[116:119], v[96:99], v[216:219]\n"
    "ds_read_b64_tr_b16 v[146:147], v28 offset:512\n"
    "s_mul_i32 s44, s3, 1024\n"
    "v_mfma_f32_16x16x32_bf16 v[212:215], v[116:119], a[116:119], v[212:215]\n"
    "v_mul_u32_u24_e64 v47, 16, v0\n"
    "v_mfma_f32_16x16x32_bf16 v[208:211], v[116:119], a[112:115], v[208:211]\n"
    "buffer_load_dwordx4 v19, s[32:35], s61 offen offset:1024 lds\n"
    "s_add_u32 s44, s44, 65536\n"
    "buffer_load_dwordx2 v[64:65], v45, s[36:39], s65 offen offset:0 sc1\n"
    "buffer_load_dwordx2 v[66:67], v45, s[36:39], s65 offen offset:128 sc1\n"
    "buffer_load_dwordx2 v[68:69], v46, s[36:39], s65 offen offset:0 sc1\n"
    "buffer_load_dwordx2 v[70:71], v46, s[36:39], s65 offen offset:128 sc1\n"
    "s_waitcnt lgkmcnt(0)\n"
    "v_mfma_f32_16x16x32_bf16 v[160:163], v[132:135], a[120:123], v[160:163]\n"
    "s_waitcnt vmcnt(14)\n"
    "s_barrier\n"
    "v_mfma_f32_16x16x32_bf16 v[164:167], v[132:135], a[124:127], v[164:167]\n"
    "ds_read_b128 a[128:131], v21 offset:32768\n"
    "s_mul_i32 s21, s62, 122880\n"
    "v_mfma_f32_16x16x32_bf16 v[168:171], v[132:135], v[124:127], v[168:171]\n"
    "s_add_u32 m0, s71, 90112\n"
    "v_mfma_f32_16x16x32_bf16 v[172:175], v[132:135], v[128:131], v[172:175]\n"
    "ds_read_b64_tr_b16 v[104:105], v25 offset:2048\n"
    "v_mfma_f32_16x16x32_bf16 v[188:191], v[136:139], v[128:131], v[188:191]\n"
    "ds_read_b64_tr_b16 v[106:107], v25 offset:2560\n"
    "v_mfma_f32_16x16x32_bf16 v[184:187], v[136:139], v[124:127], v[184:187]\n"
    "ds_read_b128 a[132:135], v21 offset:34816\n"
    "v_mfma_f32_16x16x32_bf16 v[180:183], v[136:139], a[124:127], v[180:183]\n"
    "ds_read_b128 v[96:99], v23 offset:32768\n"
    "v_mfma_f32_16x16x32_bf16 v[176:179], v[136:139], a[120:123], v[176:179]\n"
    "ds_read_b128 v[100:103], v23 offset:34816\n"
    "buffer_load_dwordx4 v29, s[28:31], s21 offen lds\n"
    "v_mfma_f32_16x16x32_bf16 v[192:195], v[140:143], a[120:123], v[192:195]\n"
    "ds_read_b64_tr_b16 v[108:109], v26 offset:2048\n"
    "v_mfma_f32_16x16x32_bf16 v[196:199], v[140:143], a[124:127], v[196:199]\n"
    "ds_read_b64_tr_b16 v[110:111], v26 offset:2560\n"
    "v_mfma_f32_16x16x32_bf16 v[200:203], v[140:143], v[124:127], v[200:203]\n"
    "ds_read_b64_tr_b16 v[112:113], v27 offset:2048\n"
    "v_mfma_f32_16x16x32_bf16 v[204:207], v[140:143], v[128:131], v[204:207]\n"
    "ds_read_b64_tr_b16 v[114:115], v27 offset:2560\n"
    "buffer_load_dwordx4 v30, s[28:31], s21 offen offset:1024 lds\n"
    "v_mfma_f32_16x16x32_bf16 v[220:223], v[144:147], v[128:131], v[220:223]\n"
    "ds_read_b64_tr_b16 v[116:117], v28 offset:2048\n"
    "v_mfma_f32_16x16x32_bf16 v[216:219], v[144:147], v[124:127], v[216:219]\n"
    "ds_read_b64_tr_b16 v[118:119], v28 offset:2560\n"
    "v_mfma_f32_16x16x32_bf16 v[212:215], v[144:147], a[124:127], v[212:215]\n"
    "buffer_load_dwordx4 v31, s[28:31], s21 offen offset:2048 lds\n"
    "v_mfma_f32_16x16x32_bf16 v[208:211], v[144:147], a[120:123], v[208:211]\n"
    "s_waitcnt lgkmcnt(0)\n"
    "v_mfma_f32_16x16x32_bf16 v[160:163], v[104:107], a[128:131], v[160:163]\n"
    "s_waitcnt vmcnt(15)\n"
    "s_barrier\n"
    "v_mfma_f32_16x16x32_bf16 v[164:167], v[104:107], a[132:135], v[164:167]\n"
    "buffer_load_dwordx4 v32, s[28:31], s21 offen offset:3072 lds\n"
    "v_mfma_f32_16x16x32_bf16 v[168:171], v[104:107], v[96:99], v[168:171]\n"
    "ds_read_b128 a[136:139], v22 offset:32768\n"
    "v_mfma_f32_16x16x32_bf16 v[172:175], v[104:107], v[100:103], v[172:175]\n"
    "ds_read_b64_tr_b16 v[132:133], v25 offset:4096\n"
    "v_mfma_f32_16x16x32_bf16 v[188:191], v[108:111], v[100:103], v[188:191]\n"
    "ds_read_b64_tr_b16 v[134:135], v25 offset:4608\n"
    "s_add_u32 m0, s71, 94208\n"
    "v_mfma_f32_16x16x32_bf16 v[184:187], v[108:111], v[96:99], v[184:187]\n"
    "ds_read_b128 a[140:143], v22 offset:34816\n"
    "v_mfma_f32_16x16x32_bf16 v[180:183], v[108:111], a[132:135], v[180:183]\n"
    "ds_read_b128 v[124:127], v24 offset:32768\n"
    "v_mfma_f32_16x16x32_bf16 v[176:179], v[108:111], a[128:131], v[176:179]\n"
    "ds_read_b128 v[128:131], v24 offset:34816\n"
    "v_mfma_f32_16x16x32_bf16 v[192:195], v[112:115], a[128:131], v[192:195]\n"
    "ds_read_b64_tr_b16 v[136:137], v26 offset:4096\n"
    "v_mfma_f32_16x16x32_bf16 v[196:199], v[112:115], a[132:135], v[196:199]\n"
    "ds_read_b64_tr_b16 v[138:139], v26 offset:4608\n"
    "buffer_load_dwordx4 v33, s[28:31], s21 offen lds\n"
    "v_mfma_f32_16x16x32_bf16 v[200:203], v[112:115], v[96:99], v[200:203]\n"
    "ds_read_b64_tr_b16 v[140:141], v27 offset:4096\n"
    "s_add_u32 s62, s62, 4\n"
    "v_mfma_f32_16x16x32_bf16 v[204:207], v[112:115], v[100:103], v[204:207]\n"
    "ds_read_b64_tr_b16 v[142:143], v27 offset:4608\n"
    "s_sub_u32 s67, s62, 20\n"
    "v_mfma_f32_16x16x32_bf16 v[220:223], v[116:119], v[100:103], v[220:223]\n"
    "ds_read_b64_tr_b16 v[144:145], v28 offset:4096\n"
    "s_cselect_b32 s62, s62, s67\n"
    "v_mfma_f32_16x16x32_bf16 v[216:219], v[116:119], v[96:99], v[216:219]\n"
    "ds_read_b64_tr_b16 v[146:147], v28 offset:4608\n"
    "s_mul_i32 s21, s62, 122880\n"
    "v_mfma_f32_16x16x32_bf16 v[212:215], v[116:119], a[132:135], v[212:215]\n"
    "s_add_u32 m0, s71, 110592\n"
    "v_mfma_f32_16x16x32_bf16 v[208:211], v[116:119], a[128:131], v[208:211]\n"
    "s_waitcnt lgkmcnt(0)\n"
    "v_mfma_f32_16x16x32_bf16 v[160:163], v[132:135], a[136:139], v[160:163]\n"
    "s_waitcnt vmcnt(11)\n"
    "s_barrier\n"
    "v_mfma_f32_16x16x32_bf16 v[164:167], v[132:135], a[140:143], v[164:167]\n"
    "buffer_load_dwordx4 v29, s[28:31], s21 offen lds\n"
    "v_mfma_f32_16x16x32_bf16 v[168:171], v[132:135], v[124:127], v[168:171]\n"
    "ds_read_b128 a[144:147], v21 offset:0\n"
    "v_mfma_f32_16x16x32_bf16 v[172:175], v[132:135], v[128:131], v[172:175]\n"
    "ds_read_b64_tr_b16 v[104:105], v25 offset:6144\n"
    "v_mfma_f32_16x16x32_bf16 v[188:191], v[136:139], v[128:131], v[188:191]\n"
    "ds_read_b64_tr_b16 v[106:107], v25 offset:6656\n"
    "v_mfma_f32_16x16x32_bf16 v[184:187], v[136:139], v[124:127], v[184:187]\n"
    "ds_read_b128 a[148:151], v21 offset:2048\n"
    "v_mfma_f32_16x16x32_bf16 v[180:183], v[136:139], a[140:143], v[180:183]\n"
    "ds_read_b128 v[96:99], v23 offset:0\n"
    "v_mfma_f32_16x16x32_bf16 v[176:179], v[136:139], a[136:139], v[176:179]\n"
    "ds_read_b128 v[100:103], v23 offset:2048\n"
    "v_mfma_f32_16x16x32_bf16 v[192:195], v[140:143], a[136:139], v[192:195]\n"
    "ds_read_b64_tr_b16 v[108:109], v26 offset:6144\n"
    "buffer_load_dwordx4 v30, s[28:31], s21 offen offset:1024 lds\n"
    "v_mfma_f32_16x16x32_bf16 v[196:199], v[140:143], a[140:143], v[196:199]\n"
    "ds_read_b64_tr_b16 v[110:111], v26 offset:6656\n"
    "v_mfma_f32_16x16x32_bf16 v[200:203], v[140:143], v[124:127], v[200:203]\n"
    "ds_read_b64_tr_b16 v[112:113], v27 offset:6144\n"
    "v_mfma_f32_16x16x32_bf16 v[204:207], v[140:143], v[128:131], v[204:207]\n"
    "ds_read_b64_tr_b16 v[114:115], v27 offset:6656\n"
    "v_mfma_f32_16x16x32_bf16 v[220:223], v[144:147], v[128:131], v[220:223]\n"
    "ds_read_b64_tr_b16 v[116:117], v28 offset:6144\n"
    "buffer_load_dwordx4 v31, s[28:31], s21 offen offset:2048 lds\n"
    "v_mfma_f32_16x16x32_bf16 v[216:219], v[144:147], v[124:127], v[216:219]\n"
    "ds_read_b64_tr_b16 v[118:119], v28 offset:6656\n"
    "v_mfma_f32_16x16x32_bf16 v[212:215], v[144:147], a[140:143], v[212:215]\n"
    "buffer_load_dwordx4 v32, s[28:31], s21 offen offset:3072 lds\n"
    "v_mfma_f32_16x16x32_bf16 v[208:211], v[144:147], a[136:139], v[208:211]\n"
    "s_waitcnt lgkmcnt(0)\n"
    "v_mfma_f32_16x16x32_bf16 v[160:163], v[104:107], a[144:147], v[160:163]\n"
    "s_waitcnt vmcnt(13)\n"
    "s_barrier\n"
    "v_mfma_f32_16x16x32_bf16 v[164:167], v[104:107], a[148:151], v[164:167]\n"
    "ds_read_b128 a[152:155], v22 offset:0\n"
    "s_add_u32 m0, s71, 114688\n"
    "v_mfma_f32_16x16x32_bf16 v[168:171], v[104:107], v[96:99], v[168:171]\n"
    "buffer_load_dwordx4 v33, s[28:31], s21 offen lds\n"
    "v_mfma_f32_16x16x32_bf16 v[172:175], v[104:107], v[100:103], v[172:175]\n"
    "ds_read_b64_tr_b16 v[132:133], v25 offset:8192\n"
    "v_mfma_f32_16x16x32_bf16 v[188:191], v[108:111], v[100:103], v[188:191]\n"
    "ds_read_b64_tr_b16 v[134:135], v25 offset:8704\n"
    "v_mfma_f32_16x16x32_bf16 v[184:187], v[108:111], v[96:99], v[184:187]\n"
    "ds_read_b128 a[156:159], v22 offset:2048\n"
    "v_mfma_f32_16x16x32_bf16 v[180:183], v[108:111], a[148:151], v[180:183]\n"
    "ds_read_b128 v[124:127], v24 offset:0\n"
    "v_mfma_f32_16x16x32_bf16 v[176:179], v[108:111], a[144:147], v[176:179]\n"
    "ds_read_b128 v[128:131], v24 offset:2048\n"
    "v_mfma_f32_16x16x32_bf16 v[192:195], v[112:115], a[144:147], v[192:195]\n"
    "ds_read_b64_tr_b16 v[136:137], v26 offset:8192\n"
    "v_mfma_f32_16x16x32_bf16 v[196:199], v[112:115], a[148:151], v[196:199]\n"
    "ds_read_b64_tr_b16 v[138:139], v26 offset:8704\n"
    "s_add_u32 s62, s62, 4\n"
    "v_mfma_f32_16x16x32_bf16 v[200:203], v[112:115], v[96:99], v[200:203]\n"
    "ds_read_b64_tr_b16 v[140:141], v27 offset:8192\n"
    "v_mfma_f32_16x16x32_bf16 v[204:207], v[112:115], v[100:103], v[204:207]\n"
    "ds_read_b64_tr_b16 v[142:143], v27 offset:8704\n"
    "s_sub_u32 s67, s62, 20\n"
    "v_mfma_f32_16x16x32_bf16 v[220:223], v[116:119], v[100:103], v[220:223]\n"
    "ds_read_b64_tr_b16 v[144:145], v28 offset:8192\n"
    "v_mfma_f32_16x16x32_bf16 v[216:219], v[116:119], v[96:99], v[216:219]\n"
    "ds_read_b64_tr_b16 v[146:147], v28 offset:8704\n"
    "s_cselect_b32 s62, s62, s67\n"
    "v_mfma_f32_16x16x32_bf16 v[212:215], v[116:119], a[148:151], v[212:215]\n"
    "v_mfma_f32_16x16x32_bf16 v[208:211], v[116:119], a[144:147], v[208:211]\n"
    "s_waitcnt lgkmcnt(0)\n"
    "v_mfma_f32_16x16x32_bf16 v[160:163], v[132:135], a[152:155], v[160:163]\n"
    "s_waitcnt vmcnt(5)\n"
    "s_waitcnt vmcnt(10)\n"
    "v_xor_b32 v44, s63, v65\n"
    "v_xor_b32 v43, s63, v67\n"
    "v_or_b32 v44, v44, v43\n"
    "v_xor_b32 v43, s63, v69\n"
    "v_or_b32 v44, v44, v43\n"
    "v_xor_b32 v43, s63, v71\n"
    "v_or_b32 v44, v44, v43\n"
    "v_cmp_ne_u32_e64 vcc, 0, v44\n"
    "s_nop 1\n"
    "s_cmp_eq_u64 vcc, 0\n"
    "s_cbranch_scc0 L_gq_slow_%=\n"
    "L_gq_ok_%=:\n"
    "s_barrier\n"
    "v_mfma_f32_16x16x32_bf16 v[164:167], v[132:135], a[156:159], v[164:167]\n"
    "ds_read_b64_tr_b16 v[104:105], v34 offset:0\n"
    "s_mul_i32 s21, s62, 122880\n"
    "v_mfma_f32_16x16x32_bf16 v[168:171], v[132:135], v[124:127], v[168:171]\n"
    "s_add_u32 m0, s71, 0\n"
    "v_sub_f32 v72, 1.0, v64\n"
    "v_mfma_f32_16x16x32_bf16 v[172:175], v[132:135], v[128:131], v[172:175]\n"
    "buffer_load_dwordx4 v29, s[28:31], s21 offen lds\n"
    "v_mfma_f32_16x16x32_bf16 v[188:191], v[136:139], v[128:131], v[188:191]\n"
    "ds_read_b64_tr_b16 v[106:107], v35 offset:0\n"
    "v_sub_f32_e64 v73, 1.0, v66\n"
    "v_mfma_f32_16x16x32_bf16 v[184:187], v[136:139], v[124:127], v[184:187]\n"
    "buffer_load_dwordx4 v30, s[28:31], s21 offen offset:1024 lds\n"
    "v_mfma_f32_16x16x32_bf16 v[180:183], v[136:139], a[156:159], v[180:183]\n"
    "ds_read_b64_tr_b16 v[108:109], v34 offset:32\n"
    "v_mfma_f32_16x16x32_bf16 v[176:179], v[136:139], a[152:155], v[176:179]\n"
    "ds_read_b64_tr_b16 v[110:111], v35 offset:32\n"
    "v_mfma_f32_16x16x32_bf16 v[192:195], v[140:143], a[152:155], v[192:195]\n"
    "ds_read_b64_tr_b16 v[112:113], v34 offset:64\n"
    "buffer_load_dwordx4 v31, s[28:31], s21 offen offset:2048 lds\n"
    "v_mfma_f32_16x16x32_bf16 v[196:199], v[140:143], a[156:159], v[196:199]\n"
    "ds_read_b64_tr_b16 v[114:115], v35 offset:64\n"
    "v_sub_f32_e64 v74, 1.0, v68\n"
    "v_mfma_f32_16x16x32_bf16 v[200:203], v[140:143], v[124:127], v[200:203]\n"
    "ds_read_b64_tr_b16 v[116:117], v34 offset:96\n"
    "buffer_load_dwordx4 v32, s[28:31], s21 offen offset:3072 lds\n"
    "v_mfma_f32_16x16x32_bf16 v[204:207], v[140:143], v[128:131], v[204:207]\n"
    "ds_read_b64_tr_b16 v[118:119], v35 offset:96\n"
    "v_sub_f32_e64 v75, 1.0, v70\n"
    "v_mfma_f32_16x16x32_bf16 v[220:223], v[144:147], v[128:131], v[220:223]\n"
    "ds_read_b64_tr_b16 v[120:121], v34 offset:128\n"
    "s_add_u32 m0, s71, 4096\n"
    "v_mfma_f32_16x16x32_bf16 v[216:219], v[144:147], v[124:127], v[216:219]\n"
    "ds_read_b64_tr_b16 v[122:123], v35 offset:128\n"
    "buffer_load_dwordx4 v33, s[28:31], s21 offen lds\n"
    "v_mfma_f32_16x16x32_bf16 v[212:215], v[144:147], a[156:159], v[212:215]\n"
    "v_mov_b32 v158, v64\n"
    "v_mov_b32 v159, v66\n"
    "v_mfma_f32_16x16x32_bf16 v[208:211], v[144:147], a[152:155], v[208:211]\n"
    "s_add_u32 s62, s62, 4\n"
    "s_sub_u32 s67, s62, 20\n"
    "s_cselect_b32 s62, s62, s67\n"
    "s_waitcnt lgkmcnt(0)\n"
    "s_waitcnt lgkmcnt(6)\n"
    "v_mfma_f32_16x16x32_bf16 v[76:79], v[104:107], a[0:3], v[224:227]\n"
    "ds_read_b64_tr_b16 v[124:125], v34 offset:5120\n"
    "v_mfma_f32_16x16x32_bf16 v[80:83], v[104:107], a[4:7], v[224:227]\n"
    "ds_read_b64_tr_b16 v[126:127], v35 offset:5120\n"
    "v_mfma_f32_16x16x32_bf16 v[88:91], v[108:111], a[4:7], v[224:227]\n"
    "ds_read_b64_tr_b16 v[128:129], v34 offset:5152\n"
    "v_mfma_f32_16x16x32_bf16 v[84:87], v[108:111], a[0:3], v[224:227]\n"
    "ds_read_b64_tr_b16 v[130:131], v35 offset:5152\n"
    "s_waitcnt lgkmcnt(6)\n"
    "v_mfma_f32_16x16x32_bf16 v[92:95], v[112:115], a[0:3], v[224:227]\n"
    "ds_read_b64_tr_b16 v[132:133], v34 offset:5184\n"
    "v_mfma_f32_16x16x32_bf16 v[96:99], v[112:115], a[4:7], v[224:227]\n"
    "ds_read_b64_tr_b16 v[134:135], v35 offset:5184\n"
    "v_mfma_f32_16x16x32_bf16 v[144:147], v[116:119], a[4:7], v[224:227]\n"
    "ds_read_b64_tr_b16 v[136:137], v34 offset:5216\n"
    "v_mfma_f32_16x16x32_bf16 v[100:103], v[116:119], a[0:3], v[224:227]\n"
    "ds_read_b64_tr_b16 v[138:139], v35 offset:5216\n"
    "s_waitcnt lgkmcnt(8)\n"
    "v_mfma_f32_16x16x32_bf16 v[148:151], v[120:123], a[0:3], v[224:227]\n"
    "ds_read_b64_tr_b16 v[140:141], v34 offset:5248\n"
    "v_mfma_f32_16x16x32_bf16 v[152:155], v[120:123], a[4:7], v[224:227]\n"
    "ds_read_b64_tr_b16 v[142:143], v35 offset:5248\n"
    "s_waitcnt lgkmcnt(6)\n"
    "v_mfma_f32_16x16x32_bf16 v[76:79], v[124:127], a[8:11], v[76:79]\n"
    "v_pk_mul_f32 v[160:161], v[160:161], v[72:73] op_sel_hi:[1,0]\n"
    "v_pk_mul_f32 v[162:163], v[162:163], v[72:73] op_sel_hi:[1,0]\n"
    "v_pk_mul_f32 v[176:177], v[176:177], v[72:73] op_sel_hi:[1,0]\n"
    "v_pk_mul_f32 v[178:179], v[178:179], v[72:73] op_sel_hi:[1,0]\n"
    "v_cvt_pk_bf16_f32 v228, v160, v161\n"
    "v_cvt_pk_bf16_f32 v229, v162, v163\n"
    "v_cvt_pk_bf16_f32 v230, v176, v177\n"
    "v_cvt_pk_bf16_f32 v231, v178, v179\n"
    "v_pk_max_i16 v228, v228, 0\n"
    "v_pk_max_i16 v229, v229, 0\n"
    "v_pk_max_i16 v230, v230, 0\n"
    "v_pk_max_i16 v231, v231, 0\n"
    "buffer_store_dwordx4 v[228:231], v1, s[36:39], s17 offen sc1\n"
    "v_pk_mul_f32 v[164:165], v[164:165], v[72:73] op_sel:[0,1] op_sel_hi:[1,1]\n"
    "v_pk_mul_f32 v[166:167], v[166:167], v[72:73] op_sel:[0,1] op_sel_hi:[1,1]\n"
    "v_pk_mul_f32 v[180:181], v[180:181], v[72:73] op_sel:[0,1] op_sel_hi:[1,1]\n"
    "v_pk_mul_f32 v[182:183], v[182:183], v[72:73] op_sel:[0,1] op_sel_hi:[1,1]\n"
    "v_cvt_pk_bf16_f32 v232, v164, v165\n"
    "v_cvt_pk_bf16_f32 v233, v166, v167\n"
    "v_cvt_pk_bf16_f32 v234, v180, v181\n"
    "v_cvt_pk_bf16_f32 v235, v182, v183\n"
    "v_pk_max_i16 v232, v232, 0\n"
    "v_pk_max_i16 v233, v233, 0\n"
    "v_pk_max_i16 v234, v234, 0\n"
    "v_pk_max_i16 v235, v235, 0\n"
    "buffer_store_dwordx4 v[232:235], v1, s[36:39], s17 offen offset:1024 sc1\n"
    "v_pk_mul_f32 v[168:169], v[168:169], v[74:75] op_sel_hi:[1,0]\n"
    "v_pk_mul_f32 v[170:171], v[170:171], v[74:75] op_sel_hi:[1,0]\n"
    "v_pk_mul_f32 v[184:185], v[184:185], v[74:75] op_sel_hi:[1,0]\n"
    "v_pk_mul_f32 v[186:187], v[186:187], v[74:75] op_sel_hi:[1,0]\n"
    "v_cvt_pk_bf16_f32 v236, v168, v169\n"
    "v_cvt_pk_bf16_f32 v237, v170, v171\n"
    "v_cvt_pk_bf16_f32 v238, v184, v185\n"
    "v_cvt_pk_bf16_f32 v239, v186, v187\n"
    "v_pk_max_i16 v236, v236, 0\n"
    "v_pk_max_i16 v237, v237, 0\n"
    "v_pk_max_i16 v238, v238, 0\n"
    "v_pk_max_i16 v239, v239, 0\n"
    "buffer_store_dwordx4 v[236:239], v1, s[36:39], s45 offen sc1\n"
    "v_pk_mul_f32 v[172:173], v[172:173], v[74:75] op_sel:[0,1] op_sel_hi:[1,1]\n"
    "v_pk_mul_f32 v[174:175], v[174:175], v[74:75] op_sel:[0,1] op_sel_hi:[1,1]\n"
    "v_pk_mul_f32 v[188:189], v[188:189], v[74:75] op_sel:[0,1] op_sel_hi:[1,1]\n"
    "v_pk_mul_f32 v[190:191], v[190:191], v[74:75] op_sel:[0,1] op_sel_hi:[1,1]\n"
    "v_cvt_pk_bf16_f32 v228, v172, v173\n"
    "v_cvt_pk_bf16_f32 v229, v174, v175\n"
    "v_cvt_pk_bf16_f32 v230, v188, v189\n"
    "v_cvt_pk_bf16_f32 v231, v190, v191\n"
    "v_pk_max_i16 v228, v228, 0\n"
    "v_pk_max_i16 v229, v229, 0\n"
    "v_pk_max_i16 v230, v230, 0\n"
    "v_pk_max_i16 v231, v231, 0\n"
    "buffer_store_dwordx4 v[228:231], v1, s[36:39], s45 offen offset:1024 sc1\n"
    "v_pk_mul_f32 v[192:193], v[192:193], v[72:73] op_sel_hi:[1,0]\n"
    "v_pk_mul_f32 v[194:195], v[194:195], v[72:73] op_sel_hi:[1,0]\n"
    "v_pk_mul_f32 v[208:209], v[208:209], v[72:73] op_sel_hi:[1,0]\n"
    "v_pk_mul_f32 v[210:211], v[210:211], v[72:73] op_sel_hi:[1,0]\n"
    "v_cvt_pk_bf16_f32 v232, v192, v193\n"
    "v_cvt_pk_bf16_f32 v233, v194, v195\n"
    "v_cvt_pk_bf16_f32 v234, v208, v209\n"
    "v_cvt_pk_bf16_f32 v235, v210, v211\n"
    "v_pk_max_i16 v232, v232, 0\n"
    "v_pk_max_i16 v233, v233, 0\n"
    "v_pk_max_i16 v234, v234, 0\n"
    "v_pk_max_i16 v235, v235, 0\n"
    "buffer_store_dwordx4 v[232:235], v247, s[36:39], s17 offen sc1\n"
    "v_pk_mul_f32 v[196:197], v[196:197], v[72:73] op_sel:[0,1] op_sel_hi:[1,1]\n"
    "v_pk_mul_f32 v[198:199], v[198:199], v[72:73] op_sel:[0,1] op_sel_hi:[1,1]\n"
    "v_pk_mul_f32 v[212:213], v[212:213], v[72:73] op_sel:[0,1] op_sel_hi:[1,1]\n"
    "v_pk_mul_f32 v[214:215], v[214:215], v[72:73] op_sel:[0,1] op_sel_hi:[1,1]\n"
    "v_cvt_pk_bf16_f32 v236, v196, v197\n"
    "v_cvt_pk_bf16_f32 v237, v198, v199\n"
    "v_cvt_pk_bf16_f32 v238, v212, v213\n"
    "v_cvt_pk_bf16_f32 v239, v214, v215\n"
    "v_pk_max_i16 v236, v236, 0\n"
    "v_pk_max_i16 v237, v237, 0\n"
    "v_pk_max_i16 v238, v238, 0\n"
    "v_pk_max_i16 v239, v239, 0\n"
    "buffer_store_dwordx4 v[236:239], v247, s[36:39], s17 offen offset:1024 sc1\n"
    "v_pk_mul_f32 v[200:201], v[200:201], v[74:75] op_sel_hi:[1,0]\n"
    "v_pk_mul_f32 v[202:203], v[202:203], v[74:75] op_sel_hi:[1,0]\n"
    "v_pk_mul_f32 v[216:217], v[216:217], v[74:75] op_sel_hi:[1,0]\n"
    "v_pk_mul_f32 v[218:219], v[218:219], v[74:75] op_sel_hi:[1,0]\n"
    "v_cvt_pk_bf16_f32 v228, v200, v201\n"
    "v_cvt_pk_bf16_f32 v229, v202, v203\n"
    "v_cvt_pk_bf16_f32 v230, v216, v217\n"
    "v_cvt_pk_bf16_f32 v231, v218, v219\n"
    "v_pk_max_i16 v228, v228, 0\n"
    "v_pk_max_i16 v229, v229, 0\n"
    "v_pk_max_i16 v230, v230, 0\n"
    "v_pk_max_i16 v231, v231, 0\n"
    "buffer_store_dwordx4 v[228:231], v247, s[36:39], s45 offen sc1\n"
    "v_pk_mul_f32 v[204:205], v[204:205], v[74:75] op_sel:[0,1] op_sel_hi:[1,1]\n"
    "v_pk_mul_f32 v[206:207], v[206:207], v[74:75] op_sel:[0,1] op_sel_hi:[1,1]\n"
    "v_pk_mul_f32 v[220:221], v[220:221], v[74:75] op_sel:[0,1] op_sel_hi:[1,1]\n"
    "v_pk_mul_f32 v[222:223], v[222:223], v[74:75] op_sel:[0,1] op_sel_hi:[1,1]\n"
    "v_cvt_pk_bf16_f32 v232, v204, v205\n"
    "v_cvt_pk_bf16_f32 v233, v206, v207\n"
    "v_cvt_pk_bf16_f32 v234, v220, v221\n"
    "v_cvt_pk_bf16_f32 v235, v222, v223\n"
    "v_pk_max_i16 v232, v232, 0\n"
    "v_pk_max_i16 v233, v233, 0\n"
    "v_pk_max_i16 v234, v234, 0\n"
    "v_pk_max_i16 v235, v235, 0\n"
    "buffer_store_dwordx4 v[232:235], v247, s[36:39], s45 offen offset:1024 sc1\n"
    "buffer_load_dword v63, v47, s[36:39], s44 offen sc1\n"
    "ds_read_b64_tr_b16 v[104:105], v34 offset:10240\n"
    "v_mfma_f32_16x16x32_bf16 v[80:83], v[124:127], a[12:15], v[80:83]\n"
    "ds_read_b64_tr_b16 v[106:107], v35 offset:10240\n"
    "v_mfma_f32_16x16x32_bf16 v[88:91], v[128:131], a[12:15], v[88:91]\n"
    "ds_read_b64_tr_b16 v[108:109], v34 offset:10272\n"
    "v_mfma_f32_16x16x32_bf16 v[84:87], v[128:131], a[8:11], v[84:87]\n"
    "ds_read_b64_tr_b16 v[110:111], v35 offset:10272\n"
    "s_waitcnt lgkmcnt(6)\n"
    "v_mfma_f32_16x16x32_bf16 v[92:95], v[132:135], a[8:11], v[92:95]\n"
    "ds_read_b64_tr_b16 v[112:113], v34 offset:10304\n"
    "v_mfma_f32_16x16x32_bf16 v[96:99], v[132:135], a[12:15], v[96:99]\n"
    "ds_read_b64_tr_b16 v[114:115], v35 offset:10304\n"
    "v_mfma_f32_16x16x32_bf16 v[144:147], v[136:139], a[12:15], v[144:147]\n"
    "ds_read_b64_tr_b16 v[116:117], v34 offset:10336\n"
    "v_mfma_f32_16x16x32_bf16 v[100:103], v[136:139], a[8:11], v[100:103]\n"
    "ds_read_b64_tr_b16 v[118:119], v35 offset:10336\n"
    "s_waitcnt lgkmcnt(8)\n"
    "v_mfma_f32_16x16x32_bf16 v[148:151], v[140:143], a[8:11], v[148:151]\n"
    "ds_read_b64_tr_b16 v[120:121], v34 offset:10368\n"
    "v_mfma_f32_16x16x32_bf16 v[152:155], v[140:143], a[12:15], v[152:155]\n"
    "ds_read_b64_tr_b16 v[122:123], v35 offset:10368\n"
    "s_waitcnt lgkmcnt(6)\n"
    "v_mfma_f32_16x16x32_bf16 v[76:79], v[104:107], a[16:19], v[76:79]\n"
    "ds_read_b64_tr_b16 v[124:125], v34 offset:15360\n"
    "v_mfma_f32_16x16x32_bf16 v[80:83], v[104:107], a[20:23], v[80:83]\n"
    "ds_read_b64_tr_b16 v[126:127], v35 offset:15360\n"
    "v_mfma_f32_16x16x32_bf16 v[88:91], v[108:111], a[20:23], v[88:91]\n"
    "ds_read_b64_tr_b16 v[128:129], v34 offset:15392\n"
    "v_mfma_f32_16x16x32_bf16 v[84:87], v[108:111], a[16:19], v[84:87]\n"
    "ds_read_b64_tr_b16 v[130:131], v35 offset:15392\n"
    "s_waitcnt lgkmcnt(6)\n"
    "v_mfma_f32_16x16x32_bf16 v[92:95], v[112:115], a[16:19], v[92:95]\n"
    "ds_read_b64_tr_b16 v[132:133], v34 offset:15424\n"
    "v_mfma_f32_16x16x32_bf16 v[96:99], v[112:115], a[20:23], v[96:99]\n"
    "ds_read_b64_tr_b16 v[134:135], v35 offset:15424\n"
    "v_mfma_f32_16x16x32_bf16 v[144:147], v[116:119], a[20:23], v[144:147]\n"
    "ds_read_b64_tr_b16 v[136:137], v34 offset:15456\n"
    "v_mfma_f32_16x16x32_bf16 v[100:103], v[116:119], a[16:19], v[100:103]\n"
    "ds_read_b64_tr_b16 v[138:139], v35 offset:15456\n"
    "s_waitcnt lgkmcnt(8)\n"
    "v_mfma_f32_16x16x32_bf16 v[148:151], v[120:123], a[16:19], v[148:151]\n"
    "ds_read_b64_tr_b16 v[140:141], v34 offset:15488\n"
    "v_mfma_f32_16x16x32_bf16 v[152:155], v[120:123], a[20:23], v[152:155]\n"
    "ds_read_b64_tr_b16 v[142:143], v35 offset:15488\n"
    "s_waitcnt lgkmcnt(6)\n"
    "v_mfma_f32_16x16x32_bf16 v[76:79], v[124:127], a[24:27], v[76:79]\n"
    "s_waitcnt vmcnt(14)\n"
    "s_barrier\n"
    "ds_read_b64_tr_b16 v[104:105], v34 offset:20480\n"
    "v_mfma_f32_16x16x32_bf16 v[80:83], v[124:127], a[28:31], v[80:83]\n"
    "ds_read_b64_tr_b16 v[106:107], v35 offset:20480\n"
    "v_mfma_f32_16x16x32_bf16 v[88:91], v[128:131], a[28:31], v[88:91]\n"
    "ds_read_b64_tr_b16 v[108:109], v34 offset:20512\n"
    "v_mfma_f32_16x16x32_bf16 v[84:87], v[128:131], a[24:27], v[84:87]\n"
    "ds_read_b64_tr_b16 v[110:111], v35 offset:20512\n"
    "s_waitcnt lgkmcnt(6)\n"
    "v_mfma_f32_16x16x32_bf16 v[92:95], v[132:135], a[24:27], v[92:95]\n"
    "ds_read_b64_tr_b16 v[112:113], v34 offset:20544\n"
    "v_mfma_f32_16x16x32_bf16 v[96:99], v[132:135], a[28:31], v[96:99]\n"
    "ds_read_b64_tr_b16 v[114:115], v35 offset:20544\n"
    "v_mfma_f32_16x16x32_bf16 v[144:147], v[136:139], a[28:31], v[144:147]\n"
    "ds_read_b64_tr_b16 v[116:117], v34 offset:20576\n"
    "v_mfma_f32_16x16x32_bf16 v[100:103], v[136:139], a[24:27], v[100:103]\n"
    "ds_read_b64_tr_b16 v[118:119], v35 offset:20576\n"
    "s_waitcnt lgkmcnt(8)\n"
    "v_mfma_f32_16x16x32_bf16 v[148:151], v[140:143], a[24:27], v[148:151]\n"
    "ds_read_b64_tr_b16 v[120:121], v34 offset:20608\n"
    "v_mfma_f32_16x16x32_bf16 v[152:155], v[140:143], a[28:31], v[152:155]\n"
    "ds_read_b64_tr_b16 v[122:123], v35 offset:20608\n"
    "s_waitcnt lgkmcnt(6)\n"
    "v_mfma_f32_16x16x32_bf16 v[76:79], v[104:107], a[32:35], v[76:79]\n"
    "ds_read_b64_tr_b16 v[124:125], v34 offset:25600\n"
    "v_mfma_f32_16x16x32_bf16 v[80:83], v[104:107], a[36:39], v[80:83]\n"
    "ds_read_b64_tr_b16 v[126:127], v35 offset:25600\n"
    "v_mfma_f32_16x16x32_bf16 v[88:91], v[108:111], a[36:39], v[88:91]\n"
    "ds_read_b64_tr_b16 v[128:129], v34 offset:25632\n"
    "v_mfma_f32_16x16x32_bf16 v[84:87], v[108:111], a[32:35], v[84:87]\n"
    "ds_read_b64_tr_b16 v[130:131], v35 offset:25632\n"
    "s_waitcnt lgkmcnt(6)\n"
    "v_mfma_f32_16x16x32_bf16 v[92:95], v[112:115], a[32:35], v[92:95]\n"
    "ds_read_b64_tr_b16 v[132:133], v34 offset:25664\n"
    "v_mfma_f32_16x16x32_bf16 v[96:99], v[112:115], a[36:39], v[96:99]\n"
    "ds_read_b64_tr_b16 v[134:135], v35 offset:25664\n"
    "v_mfma_f32_16x16x32_bf16 v[144:147], v[116:119], a[36:39], v[144:147]\n"
    "ds_read_b64_tr_b16 v[136:137], v34 offset:25696\n"
    "v_mfma_f32_16x16x32_bf16 v[100:103], v[116:119], a[32:35], v[100:103]\n"
    "ds_read_b64_tr_b16 v[138:139], v35 offset:25696\n"
    "s_waitcnt lgkmcnt(8)\n"
    "v_mfma_f32_16x16x32_bf16 v[148:151], v[120:123], a[32:35], v[148:151]\n"
    "ds_read_b64_tr_b16 v[140:141], v34 offset:25728\n"
    "v_mfma_f32_16x16x32_bf16 v[152:155], v[120:123], a[36:39], v[152:155]\n"
    "ds_read_b64_tr_b16 v[142:143], v35 offset:25728\n"
    "s_waitcnt lgkmcnt(6)\n"
    "v_mfma_f32_16x16x32_bf16 v[76:79], v[124:127], a[40:43], v[76:79]\n"
    "ds_read_b64_tr_b16 v[104:105], v34 offset:30720\n"
    "v_mfma_f32_16x16x32_bf16 v[80:83], v[124:127], a[44:47], v[80:83]\n"
    "ds_read_b64_tr_b16 v[106:107], v35 offset:30720\n"
    "v_mfma_f32_16x16x32_bf16 v[88:91], v[128:131], a[44:47], v[88:91]\n"
    "ds_read_b64_tr_b16 v[108:109], v34 offset:30752\n"
    "s_mul_i32 s21, s62, 122880\n"
    "v_mfma_f32_16x16x32_bf16 v[84:87], v[128:131], a[40:43], v[84:87]\n"
    "ds_read_b64_tr_b16 v[110:111], v35 offset:30752\n"
    "s_waitcnt lgkmcnt(6)\n"
    "v_mfma_f32_16x16x32_bf16 v[92:95], v[132:135], a[40:43], v[92:95]\n"
    "ds_read_b64_tr_b16 v[112:113], v34 offset:30784\n"
    "s_add_u32 m0, s71, 20480\n"
    "v_mfma_f32_16x16x32_bf16 v[96:99], v[132:135], a[44:47], v[96:99]\n"
    "ds_read_b64_tr_b16 v[114:115], v35 offset:30784\n"
    "v_mfma_f32_16x16x32_bf16 v[144:147], v[136:139], a[44:47], v[144:147]\n"
    "ds_read_b64_tr_b16 v[116:117], v34 offset:30816\n"
    "ds_read_b64_tr_b16 v[118:119], v35 offset:30816\n"
    "v_mfma_f32_16x16x32_bf16 v[100:103], v[136:139], a[40:43], v[100:103]\n"
    "buffer_load_dwordx4 v29, s[28:31], s21 offen lds\n"
    "s_waitcnt lgkmcnt(8)\n"
    "v_mfma_f32_16x16x32_bf16 v[148:151], v[140:143], a[40:43], v[148:151]\n"
    "ds_read_b64_tr_b16 v[120:121], v34 offset:30848\n"
    "ds_read_b64_tr_b16 v[122:123], v35 offset:30848\n"
    "v_mfma_f32_16x16x32_bf16 v[152:155], v[140:143], a[44:47], v[152:155]\n"
    "buffer_load_dwordx4 v30, s[28:31], s21 offen offset:1024 lds\n"
    "s_waitcnt lgkmcnt(6)\n"
    "v_mfma_f32_16x16x32_bf16 v[76:79], v[104:107], a[48:51], v[76:79]\n"
    "ds_read_b64_tr_b16 v[124:125], v34 offset:35840\n"
    "v_mfma_f32_16x16x32_bf16 v[80:83], v[104:107], a[52:55], v[80:83]\n"
    "ds_read_b64_tr_b16 v[126:127], v35 offset:35840\n"
    "v_mfma_f32_16x16x32_bf16 v[88:91], v[108:111], a[52:55], v[88:91]\n"
    "ds_read_b64_tr_b16 v[128:129], v34 offset:35872\n"
    "v_mfma_f32_16x16x32_bf16 v[84:87], v[108:111], a[48:51], v[84:87]\n"
    "ds_read_b64_tr_b16 v[130:131], v35 offset:35872\n"
    "s_waitcnt lgkmcnt(6)\n"
    "v_mfma_f32_16x16x32_bf16 v[92:95], v[112:115], a[48:51], v[92:95]\n"
    "ds_read_b64_tr_b16 v[132:133], v34 offset:35904\n"
    "ds_read_b64_tr_b16 v[134:135], v35 offset:35904\n"
    "v_mfma_f32_16x16x32_bf16 v[96:99], v[112:115], a[52:55], v[96:99]\n"
    "buffer_load_dwordx4 v31, s[28:31], s21 offen offset:2048 lds\n"
    "v_mfma_f32_16x16x32_bf16 v[144:147], v[116:119], a[52:55], v[144:147]\n"
    "ds_read_b64_tr_b16 v[136:137], v34 offset:35936\n"
    "v_mfma_f32_16x16x32_bf16 v[100:103], v[116:119], a[48:51], v[100:103]\n"
    "ds_read_b64_tr_b16 v[138:139], v35 offset:35936\n"
    "s_waitcnt lgkmcnt(8)\n"
    "v_mfma_f32_16x16x32_bf16 v[148:151], v[120:123], a[48:51], v[148:151]\n"
    "ds_read_b64_tr_b16 v[140:141], v34 offset:35968\n"
    "ds_read_b64_tr_b16 v[142:143], v35 offset:35968\n"
    "v_mfma_f32_16x16x32_bf16 v[152:155], v[120:123], a[52:55], v[152:155]\n"
    "buffer_load_dwordx4 v32, s[28:31], s21 offen offset:3072 lds\n"
    "s_waitcnt lgkmcnt(6)\n"
    "v_mfma_f32_16x16x32_bf16 v[76:79], v[124:127], a[56:59], v[76:79]\n"
    "s_waitcnt vmcnt(13)\n"
    "s_barrier\n"
    "v_add_u32 v34, 4294877184, v34\n"
    "v_add_u32 v35, 4294877184, v35\n"
    "ds_read_b64_tr_b16 v[104:105], v34 offset:0\n"
    "s_add_u32 m0, s71, 24576\n"
    "v_mfma_f32_16x16x32_bf16 v[80:83], v[124:127], a[60:63], v[80:83]\n"
    "ds_read_b64_tr_b16 v[106:107], v35 offset:0\n"
    "v_mfma_f32_16x16x32_bf16 v[88:91], v[128:131], a[60:63], v[88:91]\n"
    "ds_read_b64_tr_b16 v[108:109], v34 offset:32\n"
    "ds_read_b64_tr_b16 v[110:111], v35 offset:32\n"
    "v_mfma_f32_16x16x32_bf16 v[84:87], v[128:131], a[56:59], v[84:87]\n"
    "buffer_load_dwordx4 v33, s[28:31], s21 offen lds\n"
    "s_waitcnt lgkmcnt(6)\n"
    "v_mfma_f32_16x16x32_bf16 v[92:95], v[132:135], a[56:59], v[92:95]\n"
    "ds_read_b64_tr_b16 v[112:113], v34 offset:64\n"
    "s_add_u32 s62, s62, 4\n"
    "v_mfma_f32_16x16x32_bf16 v[96:99], v[132:135], a[60:63], v[96:99]\n"
    "ds_read_b64_tr_b16 v[114:115], v35 offset:64\n"
    "v_mfma_f32_16x16x32_bf16 v[144:147], v[136:139], a[60:63], v[144:147]\n"
    "ds_read_b64_tr_b16 v[116:117], v34 offset:96\n"
    "s_sub_u32 s67, s62, 20\n"
    "v_mfma_f32_16x16x32_bf16 v[100:103], v[136:139], a[56:59], v[100:103]\n"
    "ds_read_b64_tr_b16 v[118:119], v35 offset:96\n"
    "s_waitcnt lgkmcnt(8)\n"
    "v_mfma_f32_16x16x32_bf16 v[148:151], v[140:143], a[56:59], v[148:151]\n"
    "ds_read_b64_tr_b16 v[120:121], v34 offset:128\n"
    "s_cselect_b32 s62, s62, s67\n"
    "v_mfma_f32_16x16x32_bf16 v[152:155], v[140:143], a[60:63], v[152:155]\n"
    "ds_read_b64_tr_b16 v[122:123], v35 offset:128\n"
    "s_waitcnt lgkmcnt(6)\n"
    "v_mfma_f32_16x16x32_bf16 v[76:79], v[104:107], a[64:67], v[76:79]\n"
    "ds_read_b64_tr_b16 v[124:125], v34 offset:5120\n"
    "v_mfma_f32_16x16x32_bf16 v[80:83], v[104:107], a[68:71], v[80:83]\n"
    "ds_read_b64_tr_b16 v[126:127], v35 offset:5120\n"
    "v_mfma_f32_16x16x32_bf16 v[88:91], v[108:111], a[68:71], v[88:91]\n"
    "ds_read_b64_tr_b16 v[128:129], v34 offset:5152\n"
    "v_mfma_f32_16x16x32_bf16 v[84:87], v[108:111], a[64:67], v[84:87]\n"
    "ds_read_b64_tr_b16 v[130:131], v35 offset:5152\n"
    "s_waitcnt lgkmcnt(6)\n"
    "v_mfma_f32_16x16x32_bf16 v[92:95], v[112:115], a[64:67], v[92:95]\n"
    "ds_read_b64_tr_b16 v[132:133], v34 offset:5184\n"
    "v_mfma_f32_16x16x32_bf16 v[96:99], v[112:115], a[68:71], v[96:99]\n"
    "ds_read_b64_tr_b16 v[134:135], v35 offset:5184\n"
    "v_mfma_f32_16x16x32_bf16 v[144:147], v[116:119], a[68:71], v[144:147]\n"
    "ds_read_b64_tr_b16 v[136:137], v34 offset:5216\n"
    "v_mfma_f32_16x16x32_bf16 v[100:103], v[116:119], a[64:67], v[100:103]\n"
    "ds_read_b64_tr_b16 v[138:139], v35 offset:5216\n"
    "s_waitcnt lgkmcnt(8)\n"
    "v_mfma_f32_16x16x32_bf16 v[148:151], v[120:123], a[64:67], v[148:151]\n"
    "ds_read_b64_tr_b16 v[140:141], v34 offset:5248\n"
    "v_mfma_f32_16x16x32_bf16 v[152:155], v[120:123], a[68:71], v[152:155]\n"
    "ds_read_b64_tr_b16 v[142:143], v35 offset:5248\n"
    "s_waitcnt lgkmcnt(6)\n"
    "v_mfma_f32_16x16x32_bf16 v[76:79], v[124:127], a[72:75], v[76:79]\n"
    "ds_read_b64_tr_b16 v[104:105], v34 offset:10240\n"
    "v_mfma_f32_16x16x32_bf16 v[80:83], v[124:127], a[76:79], v[80:83]\n"
    "ds_read_b64_tr_b16 v[106:107], v35 offset:10240\n"
    "v_mfma_f32_16x16x32_bf16 v[88:91], v[128:131], a[76:79], v[88:91]\n"
    "ds_read_b64_tr_b16 v[108:109], v34 offset:10272\n"
    "s_mul_i32 s21, s62, 122880\n"
    "v_mfma_f32_16x16x32_bf16 v[84:87], v[128:131], a[72:75], v[84:87]\n"
    "ds_read_b64_tr_b16 v[110:111], v35 offset:10272\n"
    "s_waitcnt lgkmcnt(6)\n"
    "v_mfma_f32_16x16x32_bf16 v[92:95], v[132:135], a[72:75], v[92:95]\n"
    "ds_read_b64_tr_b16 v[112:113], v34 offset:10304\n"
    "v_mfma_f32_16x16x32_bf16 v[96:99], v[132:135], a[76:79], v[96:99]\n"
    "ds_read_b64_tr_b16 v[114:115], v35 offset:10304\n"
    "v_mfma_f32_16x16x32_bf16 v[144:147], v[136:139], a[76:79], v[144:147]\n"
    "ds_read_b64_tr_b16 v[116:117], v34 offset:10336\n"
    "s_add_u32 m0, s71, 40960\n"
    "v_mfma_f32_16x16x32_bf16 v[100:103], v[136:139], a[72:75], v[100:103]\n"
    "ds_read_b64_tr_b16 v[118:119], v35 offset:10336\n"
    "s_waitcnt lgkmcnt(8)\n"
    "v_mfma_f32_16x16x32_bf16 v[148:151], v[140:143], a[72:75], v[148:151]\n"
    "ds_read_b64_tr_b16 v[120:121], v34 offset:10368\n"
    "ds_read_b64_tr_b16 v[122:123], v35 offset:10368\n"
    "v_mfma_f32_16x16x32_bf16 v[152:155], v[140:143], a[76:79], v[152:155]\n"
    "buffer_load_dwordx4 v29, s[28:31], s21 offen lds\n"
    "s_waitcnt lgkmcnt(6)\n"
    "v_mfma_f32_16x16x32_bf16 v[76:79], v[104:107], a[80:83], v[76:79]\n"
    "s_lshl_b32 s6, s66, 7\n"
    "s_add_u32 s6, s6, 131072\n"
    "s_waitcnt vmcnt(7)\n"
    "buffer_store_dword v248, v249, s[36:39], s6 offen sc1\n"
    "ds_read_b64_tr_b16 v[124:125], v34 offset:15360\n"
    "v_mfma_f32_16x16x32_bf16 v[80:83], v[104:107], a[84:87], v[80:83]\n"
    "ds_read_b64_tr_b16 v[126:127], v35 offset:15360\n"
    "v_mfma_f32_16x16x32_bf16 v[88:91], v[108:111], a[84:87], v[88:91]\n"
    "ds_read_b64_tr_b16 v[128:129], v34 offset:15392\n"
    "v_mfma_f32_16x16x32_bf16 v[84:87], v[108:111], a[80:83], v[84:87]\n"
    "ds_read_b64_tr_b16 v[130:131], v35 offset:15392\n"
    "s_waitcnt lgkmcnt(6)\n"
    "v_mfma_f32_16x16x32_bf16 v[92:95], v[112:115], a[80:83], v[92:95]\n"
    "ds_read_b64_tr_b16 v[132:133], v34 offset:15424\n"
    "ds_read_b64_tr_b16 v[134:135], v35 offset:15424\n"
    "v_mfma_f32_16x16x32_bf16 v[96:99], v[112:115], a[84:87], v[96:99]\n"
    "buffer_load_dwordx4 v30, s[28:31], s21 offen offset:1024 lds\n"
    "v_mfma_f32_16x16x32_bf16 v[144:147], v[116:119], a[84:87], v[144:147]\n"
    "ds_read_b64_tr_b16 v[136:137], v34 offset:15456\n"
    "v_mfma_f32_16x16x32_bf16 v[100:103], v[116:119], a[80:83], v[100:103]\n"
    "ds_read_b64_tr_b16 v[138:139], v35 offset:15456\n"
    "s_waitcnt lgkmcnt(8)\n"
    "v_mfma_f32_16x16x32_bf16 v[148:151], v[120:123], a[80:83], v[148:151]\n"
    "ds_read_b64_tr_b16 v[140:141], v34 offset:15488\n"
    "ds_read_b64_tr_b16 v[142:143], v35 offset:15488\n"
    "v_mfma_f32_16x16x32_bf16 v[152:155], v[120:123], a[84:87], v[152:155]\n"
    "buffer_load_dwordx4 v31, s[28:31], s21 offen offset:2048 lds\n"
    "s_waitcnt lgkmcnt(6)\n"
    "v_mfma_f32_16x16x32_bf16 v[76:79], v[124:127], a[88:91], v[76:79]\n"
    "s_waitcnt vmcnt(9)\n"
    "v_cmp_ne_u32 vcc, s63, v63\n"
    "s_nop 1\n"
    "s_cmp_eq_u64 vcc, 0\n"
    "s_cbranch_scc0 L_f1q_slow_%=\n"
    "L_f1q_ok_%=:\n"
    "s_waitcnt vmcnt(4)\n"
    "s_barrier\n"
    "ds_read_b64_tr_b16 v[104:105], v34 offset:20480\n"
    "v_mfma_f32_16x16x32_bf16 v[80:83], v[124:127], a[92:95], v[80:83]\n"
    "ds_read_b64_tr_b16 v[106:107], v35 offset:20480\n"
    "v_mfma_f32_16x16x32_bf16 v[88:91], v[128:131], a[92:95], v[88:91]\n"
    "ds_read_b64_tr_b16 v[108:109], v34 offset:20512\n"
    "ds_read_b64_tr_b16 v[110:111], v35 offset:20512\n"
    "v_mfma_f32_16x16x32_bf16 v[84:87], v[128:131], a[88:91], v[84:87]\n"
    "buffer_load_dwordx4 v32, s[28:31], s21 offen offset:3072 lds\n"
    "s_waitcnt lgkmcnt(6)\n"
    "v_mfma_f32_16x16x32_bf16 v[92:95], v[132:135], a[88:91], v[92:95]\n"
    "ds_read_b64_tr_b16 v[112:113], v34 offset:20544\n"
    "v_mfma_f32_16x16x32_bf16 v[96:99], v[132:135], a[92:95], v[96:99]\n"
    "ds_read_b64_tr_b16 v[114:115], v35 offset:20544\n"
    "v_mfma_f32_16x16x32_bf16 v[144:147], v[136:139], a[92:95], v[144:147]\n"
    "ds_read_b64_tr_b16 v[116:117], v34 offset:20576\n"
    "s_add_u32 m0, s71, 45056\n"
    "v_mfma_f32_16x16x32_bf16 v[100:103], v[136:139], a[88:91], v[100:103]\n"
    "ds_read_b64_tr_b16 v[118:119], v35 offset:20576\n"
    "s_waitcnt lgkmcnt(8)\n"
    "v_mfma_f32_16x16x32_bf16 v[148:151], v[140:143], a[88:91], v[148:151]\n"
    "ds_read_b64_tr_b16 v[120:121], v34 offset:20608\n"
    "ds_read_b64_tr_b16 v[122:123], v35 offset:20608\n"
    "v_mfma_f32_16x16x32_bf16 v[152:155], v[140:143], a[92:95], v[152:155]\n"
    "buffer_load_dwordx4 v33, s[28:31], s21 offen lds\n"
    "s_waitcnt lgkmcnt(6)\n"
    "v_mfma_f32_16x16x32_bf16 v[76:79], v[104:107], a[96:99], v[76:79]\n"
    "ds_read_b64_tr_b16 v[124:125], v34 offset:25600\n"
    "v_mfma_f32_16x16x32_bf16 v[80:83], v[104:107], a[100:103], v[80:83]\n"
    "ds_read_b64_tr_b16 v[126:127], v35 offset:25600\n"
    "v_mfma_f32_16x16x32_bf16 v[88:91], v[108:111], a[100:103], v[88:91]\n"
    "ds_read_b64_tr_b16 v[128:129], v34 offset:25632\n"
    "ds_read_b64_tr_b16 v[130:131], v35 offset:25632\n"
    "v_mfma_f32_16x16x32_bf16 v[84:87], v[108:111], a[96:99], v[84:87]\n"
    "buffer_load_dwordx4 a[160:163], v1, s[36:39], s64 offen sc1\n"
    "s_waitcnt lgkmcnt(6)\n"
    "v_mfma_f32_16x16x32_bf16 v[92:95], v[112:115], a[96:99], v[92:95]\n"
    "ds_read_b64_tr_b16 v[132:133], v34 offset:25664\n"
    "ds_read_b64_tr_b16 v[134:135], v35 offset:25664\n"
    "v_mfma_f32_16x16x32_bf16 v[96:99], v[112:115], a[100:103], v[96:99]\n"
    "buffer_load_dwordx4 a[164:167], v1, s[36:39], s64 offen offset:1024 sc1\n"
    "v_mfma_f32_16x16x32_bf16 v[144:147], v[116:119], a[100:103], v[144:147]\n"
    "ds_read_b64_tr_b16 v[136:137], v34 offset:25696\n"
    "ds_read_b64_tr_b16 v[138:139], v35 offset:25696\n"
    "v_mfma_f32_16x16x32_bf16 v[100:103], v[116:119], a[96:99], v[100:103]\n"
    "buffer_load_dwordx4 a[168:171], v1, s[36:39], s64 offen offset:2048 sc1\n"
    "s_waitcnt lgkmcnt(8)\n"
    "v_mfma_f32_16x16x32_bf16 v[148:151], v[120:123], a[96:99], v[148:151]\n"
    "ds_read_b64_tr_b16 v[140:141], v34 offset:25728\n"
    "ds_read_b64_tr_b16 v[142:143], v35 offset:25728\n"
    "v_mfma_f32_16x16x32_bf16 v[152:155], v[120:123], a[100:103], v[152:155]\n"
    "buffer_load_dwordx4 a[172:175], v1, s[36:39], s64 offen offset:3072 sc1\n"
    "s_waitcnt lgkmcnt(6)\n"
    "v_mfma_f32_16x16x32_bf16 v[76:79], v[124:127], a[104:107], v[76:79]\n"
    "ds_read_b64_tr_b16 v[104:105], v34 offset:30720\n"
    "ds_read_b64_tr_b16 v[106:107], v35 offset:30720\n"
    "v_mfma_f32_16x16x32_bf16 v[80:83], v[124:127], a[108:111], v[80:83]\n"
    "buffer_load_dwordx4 a[176:179], v247, s[36:39], s64 offen sc1\n"
    "v_mfma_f32_16x16x32_bf16 v[88:91], v[128:131], a[108:111], v[88:91]\n"
    "ds_read_b64_tr_b16 v[108:109], v34 offset:30752\n"
    "s_add_u32 s64, s64, 5120\n"
    "v_mfma_f32_16x16x32_bf16 v[84:87], v[128:131], a[104:107], v[84:87]\n"
    "ds_read_b64_tr_b16 v[110:111], v35 offset:30752\n"
    "s_waitcnt lgkmcnt(6)\n"
    "v_mfma_f32_16x16x32_bf16 v[92:95], v[132:135], a[104:107], v[92:95]\n"
    "ds_read_b64_tr_b16 v[112:113], v34 offset:30784\n"
    "s_cmp_eq_u32 s64, s72\n"
    "v_mfma_f32_16x16x32_bf16 v[96:99], v[132:135], a[108:111], v[96:99]\n"
    "ds_read_b64_tr_b16 v[114:115], v35 offset:30784\n"
    "v_mfma_f32_16x16x32_bf16 v[144:147], v[136:139], a[108:111], v[144:147]\n"
    "ds_read_b64_tr_b16 v[116:117], v34 offset:30816\n"
    "s_cselect_b32 s67, 20480, 0\n"
    "v_mfma_f32_16x16x32_bf16 v[100:103], v[136:139], a[104:107], v[100:103]\n"
    "ds_read_b64_tr_b16 v[118:119], v35 offset:30816\n"
    "s_waitcnt lgkmcnt(8)\n"
    "v_mfma_f32_16x16x32_bf16 v[148:151], v[140:143], a[104:107], v[148:151]\n"
    "ds_read_b64_tr_b16 v[120:121], v34 offset:30848\n"
    "s_sub_u32 s64, s64, s67\n"
    "v_mfma_f32_16x16x32_bf16 v[152:155], v[140:143], a[108:111], v[152:155]\n"
    "ds_read_b64_tr_b16 v[122:123], v35 offset:30848\n"
    "s_waitcnt lgkmcnt(6)\n"
    "v_mfma_f32_16x16x32_bf16 v[76:79], v[104:107], a[112:115], v[76:79]\n"
    "ds_read_b64_tr_b16 v[124:125], v34 offset:35840\n"
    "v_mfma_f32_16x16x32_bf16 v[80:83], v[104:107], a[116:119], v[80:83]\n"
    "ds_read_b64_tr_b16 v[126:127], v35 offset:35840\n"
    "v_mfma_f32_16x16x32_bf16 v[88:91], v[108:111], a[116:119], v[88:91]\n"
    "ds_read_b64_tr_b16 v[128:129], v34 offset:35872\n"
    "ds_read_b64_tr_b16 v[130:131], v35 offset:35872\n"
    "v_mfma_f32_16x16x32_bf16 v[84:87], v[108:111], a[112:115], v[84:87]\n"
    "buffer_load_dwordx4 a[180:183], v1, s[36:39], s64 offen sc1\n"
    "s_waitcnt lgkmcnt(6)\n"
    "v_mfma_f32_16x16x32_bf16 v[92:95], v[112:115], a[112:115], v[92:95]\n"
    "ds_read_b64_tr_b16 v[132:133], v34 offset:35904\n"
    "ds_read_b64_tr_b16 v[134:135], v35 offset:35904\n"
    "v_mfma_f32_16x16x32_bf16 v[96:99], v[112:115], a[116:119], v[96:99]\n"
    "buffer_load_dwordx4 a[184:187], v1, s[36:39], s64 offen offset:1024 sc1\n"
    "v_mfma_f32_16x16x32_bf16 v[144:147], v[116:119], a[116:119], v[144:147]\n"
    "ds_read_b64_tr_b16 v[136:137], v34 offset:35936\n"
    "ds_read_b64_tr_b16 v[138:139], v35 offset:35936\n"
    "v_mfma_f32_16x16x32_bf16 v[100:103], v[116:119], a[112:115], v[100:103]\n"
    "buffer_load_dwordx4 a[188:191], v1, s[36:39], s64 offen offset:2048 sc1\n"
    "s_waitcnt lgkmcnt(8)\n"
    "v_mfma_f32_16x16x32_bf16 v[148:151], v[120:123], a[112:115], v[148:151]\n"
    "ds_read_b64_tr_b16 v[140:141], v34 offset:35968\n"
    "ds_read_b64_tr_b16 v[142:143], v35 offset:35968\n"
    "v_mfma_f32_16x16x32_bf16 v[152:155], v[120:123], a[116:119], v[152:155]\n"
    "buffer_load_dwordx4 a[192:195], v1, s[36:39], s64 offen offset:3072 sc1\n"
    "s_waitcnt lgkmcnt(6)\n"
    "v_mfma_f32_16x16x32_bf16 v[76:79], v[124:127], a[120:123], v[76:79]\n"
    "s_waitcnt vmcnt(9)\n"
    "s_barrier\n"
    "ds_read_b64_tr_b16 v[104:105], v34 offset:40960\n"
    "ds_read_b64_tr_b16 v[106:107], v35 offset:40960\n"
    "v_mfma_f32_16x16x32_bf16 v[80:83], v[124:127], a[124:127], v[80:83]\n"
    "buffer_load_dwordx4 a[196:199], v247, s[36:39], s64 offen sc1\n"
    "v_mfma_f32_16x16x32_bf16 v[88:91], v[128:131], a[124:127], v[88:91]\n"
    "ds_read_b64_tr_b16 v[108:109], v34 offset:40992\n"
    "s_add_u32 s64, s64, 5120\n"
    "v_mfma_f32_16x16x32_bf16 v[84:87], v[128:131], a[120:123], v[84:87]\n"
    "ds_read_b64_tr_b16 v[110:111], v35 offset:40992\n"
    "s_waitcnt lgkmcnt(6)\n"
    "v_mfma_f32_16x16x32_bf16 v[92:95], v[132:135], a[120:123], v[92:95]\n"
    "ds_read_b64_tr_b16 v[112:113], v34 offset:41024\n"
    "s_cmp_eq_u32 s64, s72\n"
    "v_mfma_f32_16x16x32_bf16 v[96:99], v[132:135], a[124:127], v[96:99]\n"
    "ds_read_b64_tr_b16 v[114:115], v35 offset:41024\n"
    "v_mfma_f32_16x16x32_bf16 v[144:147], v[136:139], a[124:127], v[144:147]\n"
    "ds_read_b64_tr_b16 v[116:117], v34 offset:41056\n"
    "s_cselect_b32 s67, 20480, 0\n"
    "v_mfma_f32_16x16x32_bf16 v[100:103], v[136:139], a[120:123], v[100:103]\n"
    "ds_read_b64_tr_b16 v[118:119], v35 offset:41056\n"
    "s_waitcnt lgkmcnt(8)\n"
    "v_mfma_f32_16x16x32_bf16 v[148:151], v[140:143], a[120:123], v[148:151]\n"
    "ds_read_b64_tr_b16 v[120:121], v34 offset:41088\n"
    "s_sub_u32 s64, s64, s67\n"
    "v_mfma_f32_16x16x32_bf16 v[152:155], v[140:143], a[124:127], v[152:155]\n"
    "ds_read_b64_tr_b16 v[122:123], v35 offset:41088\n"
    "s_waitcnt lgkmcnt(6)\n"
    "v_mfma_f32_16x16x32_bf16 v[76:79], v[104:107], a[128:131], v[76:79]\n"
    "ds_read_b64_tr_b16 v[124:125], v34 offset:46080\n"
    "v_mfma_f32_16x16x32_bf16 v[80:83], v[104:107], a[132:135], v[80:83]\n"
    "ds_read_b64_tr_b16 v[126:127], v35 offset:46080\n"
    "v_mfma_f32_16x16x32_bf16 v[88:91], v[108:111], a[132:135], v[88:91]\n"
    "ds_read_b64_tr_b16 v[128:129], v34 offset:46112\n"
    "ds_read_b64_tr_b16 v[130:131], v35 offset:46112\n"
    "v_mfma_f32_16x16x32_bf16 v[84:87], v[108:111], a[128:131], v[84:87]\n"
    "buffer_load_dwordx4 a[200:203], v1, s[36:39], s64 offen sc1\n"
    "s_waitcnt lgkmcnt(6)\n"
    "v_mfma_f32_16x16x32_bf16 v[92:95], v[112:115], a[128:131], v[92:95]\n"
    "ds_read_b64_tr_b16 v[132:133], v34 offset:46144\n"
    "ds_read_b64_tr_b16 v[134:135], v35 offset:46144\n"
    "v_mfma_f32_16x16x32_bf16 v[96:99], v[112:115], a[132:135], v[96:99]\n"
    "buffer_load_dwordx4 a[204:207], v1, s[36:39], s64 offen offset:1024 sc1\n"
    "v_mfma_f32_16x16x32_bf16 v[144:147], v[116:119], a[132:135], v[144:147]\n"
    "ds_read_b64_tr_b16 v[136:137], v34 offset:46176\n"
    "ds_read_b64_tr_b16 v[138:139], v35 offset:46176\n"
    "v_mfma_f32_16x16x32_bf16 v[100:103], v[116:119], a[128:131], v[100:103]\n"
    "buffer_load_dwordx4 a[208:211], v1, s[36:39], s64 offen offset:2048 sc1\n"
    "s_waitcnt lgkmcnt(8)\n"
    "v_mfma_f32_16x16x32_bf16 v[148:151], v[120:123], a[128:131], v[148:151]\n"
    "ds_read_b64_tr_b16 v[140:141], v34 offset:46208\n"
    "ds_read_b64_tr_b16 v[142:143], v35 offset:46208\n"
    "v_mfma_f32_16x16x32_bf16 v[152:155], v[120:123], a[132:135], v[152:155]\n"
    "buffer_load_dwordx4 a[212:215], v1, s[36:39], s64 offen offset:3072 sc1\n"
    "s_waitcnt lgkmcnt(6)\n"
    "v_mfma_f32_16x16x32_bf16 v[76:79], v[124:127], a[136:139], v[76:79]\n"
    "ds_read_b64_tr_b16 v[104:105], v34 offset:51200\n"
    "ds_read_b64_tr_b16 v[106:107], v35 offset:51200\n"
    "v_mfma_f32_16x16x32_bf16 v[80:83], v[124:127], a[140:143], v[80:83]\n"
    "buffer_load_dwordx4 a[216:219], v247, s[36:39], s64 offen sc1\n"
    "v_mfma_f32_16x16x32_bf16 v[88:91], v[128:131], a[140:143], v[88:91]\n"
    "ds_read_b64_tr_b16 v[108:109], v34 offset:51232\n"
    "s_add_u32 s64, s64, 5120\n"
    "v_mfma_f32_16x16x32_bf16 v[84:87], v[128:131], a[136:139], v[84:87]\n"
    "ds_read_b64_tr_b16 v[110:111], v35 offset:51232\n"
    "s_waitcnt lgkmcnt(6)\n"
    "v_mfma_f32_16x16x32_bf16 v[92:95], v[132:135], a[136:139], v[92:95]\n"
    "ds_read_b64_tr_b16 v[112:113], v34 offset:51264\n"
    "s_cmp_eq_u32 s64, s72\n"
    "v_mfma_f32_16x16x32_bf16 v[96:99], v[132:135], a[140:143], v[96:99]\n"
    "ds_read_b64_tr_b16 v[114:115], v35 offset:51264\n"
    "v_mfma_f32_16x16x32_bf16 v[144:147], v[136:139], a[140:143], v[144:147]\n"
    "ds_read_b64_tr_b16 v[116:117], v34 offset:51296\n"
    "s_cselect_b32 s67, 20480, 0\n"
    "v_mfma_f32_16x16x32_bf16 v[100:103], v[136:139], a[136:139], v[100:103]\n"
    "ds_read_b64_tr_b16 v[118:119], v35 offset:51296\n"
    "s_waitcnt lgkmcnt(8)\n"
    "v_mfma_f32_16x16x32_bf16 v[148:151], v[140:143], a[136:139], v[148:151]\n"
    "ds_read_b64_tr_b16 v[120:121], v34 offset:51328\n"
    "s_sub_u32 s64, s64, s67\n"
    "v_mfma_f32_16x16x32_bf16 v[152:155], v[140:143], a[140:143], v[152:155]\n"
    "ds_read_b64_tr_b16 v[122:123], v35 offset:51328\n"
    "s_waitcnt lgkmcnt(6)\n"
    "v_mfma_f32_16x16x32_bf16 v[76:79], v[104:107], a[144:147], v[76:79]\n"
    "ds_read_b64_tr_b16 v[124:125], v34 offset:56320\n"
    "v_mfma_f32_16x16x32_bf16 v[80:83], v[104:107], a[148:151], v[80:83]\n"
    "ds_read_b64_tr_b16 v[126:127], v35 offset:56320\n"
    "v_mfma_f32_16x16x32_bf16 v[88:91], v[108:111], a[148:151], v[88:91]\n"
    "ds_read_b64_tr_b16 v[128:129], v34 offset:56352\n"
    "ds_read_b64_tr_b16 v[130:131], v35 offset:56352\n"
    "v_mfma_f32_16x16x32_bf16 v[84:87], v[108:111], a[144:147], v[84:87]\n"
    "buffer_load_dwordx4 a[220:223], v1, s[36:39], s64 offen sc1\n"
    "s_waitcnt lgkmcnt(6)\n"
    "v_mfma_f32_16x16x32_bf16 v[92:95], v[112:115], a[144:147], v[92:95]\n"
    "ds_read_b64_tr_b16 v[132:133], v34 offset:56384\n"
    "ds_read_b64_tr_b16 v[134:135], v35 offset:56384\n"
    "v_mfma_f32_16x16x32_bf16 v[96:99], v[112:115], a[148:151], v[96:99]\n"
    "buffer_load_dwordx4 a[224:227], v1, s[36:39], s64 offen offset:1024 sc1\n"
    "v_mfma_f32_16x16x32_bf16 v[144:147], v[116:119], a[148:151], v[144:147]\n"
    "ds_read_b64_tr_b16 v[136:137], v34 offset:56416\n"
    "ds_read_b64_tr_b16 v[138:139], v35 offset:56416\n"
    "v_mfma_f32_16x16x32_bf16 v[100:103], v[116:119], a[144:147], v[100:103]\n"
    "buffer_load_dwordx4 a[228:231], v1, s[36:39], s64 offen offset:2048 sc1\n"
    "s_waitcnt lgkmcnt(8)\n"
    "v_mfma_f32_16x16x32_bf16 v[148:151], v[120:123], a[144:147], v[148:151]\n"
    "ds_read_b64_tr_b16 v[140:141], v34 offset:56448\n"
    "ds_read_b64_tr_b16 v[142:143], v35 offset:56448\n"
    "v_mfma_f32_16x16x32_bf16 v[152:155], v[120:123], a[148:151], v[152:155]\n"
    "buffer_load_dwordx4 a[232:235], v1, s[36:39], s64 offen offset:3072 sc1\n"
    "s_waitcnt lgkmcnt(6)\n"
    "v_mfma_f32_16x16x32_bf16 v[76:79], v[124:127], a[152:155], v[76:79]\n"
    "v_mfma_f32_16x16x32_bf16 v[80:83], v[124:127], a[156:159], v[80:83]\n"
    "buffer_load_dwordx4 a[236:239], v247, s[36:39], s64 offen sc1\n"
    "v_mfma_f32_16x16x32_bf16 v[88:91], v[128:131], a[156:159], v[88:91]\n"
    "s_sub_u32 s72, s72, 20480\n"
    "v_mfma_f32_16x16x32_bf16 v[84:87], v[128:131], a[152:155], v[84:87]\n"
    "s_waitcnt lgkmcnt(2)\n"
    "v_mfma_f32_16x16x32_bf16 v[92:95], v[132:135], a[152:155], v[92:95]\n"
    "s_mul_i32 s64, s47, 5120\n"
    "v_mfma_f32_16x16x32_bf16 v[96:99], v[132:135], a[156:159], v[96:99]\n"
    "v_mfma_f32_16x16x32_bf16 v[144:147], v[136:139], a[156:159], v[144:147]\n"
    "s_add_u32 s64, s64, s72\n"
    "v_mfma_f32_16x16x32_bf16 v[100:103], v[136:139], a[152:155], v[100:103]\n"
    "s_waitcnt lgkmcnt(0)\n"
    "v_mfma_f32_16x16x32_bf16 v[148:151], v[140:143], a[152:155], v[148:151]\n"
    "s_sub_u32 s64, s64, 40960\n"
    "v_mfma_f32_16x16x32_bf16 v[152:155], v[140:143], a[156:159], v[152:155]\n"
    "s_barrier\n"
    "v_mov_b32 v114, 0\n"
    "v_mov_b32 v115, 0\n"
    "buffer_load_dwordx4 a[0:3], v1, s[36:39], s64 offen sc1\n"
    "v_mov_b32 v126, 0\n"
    "v_mov_b32 v127, 0\n"
    "v_pk_fma_f32 v[16:17], v[76:77], s[68:69], s[68:69] op_sel:[0,0,1] op_sel_hi:[1,0,1]\n"
    "v_pk_fma_f32 v[18:19], v[78:79], s[68:69], s[68:69] op_sel:[0,0,1] op_sel_hi:[1,0,1]\n"
    "v_pk_fma_f32 v[20:21], v[84:85], s[68:69], s[68:69] op_sel:[0,0,1] op_sel_hi:[1,0,1]\n"
    "v_pk_fma_f32 v[22:23], v[86:87], s[68:69], s[68:69] op_sel:[0,0,1] op_sel_hi:[1,0,1]\n"
    "v_pk_fma_f32 v[24:25], v[92:93], s[68:69], s[68:69] op_sel:[0,0,1] op_sel_hi:[1,0,1]\n"
    "buffer_load_dwordx4 a[4:7], v1, s[36:39], s64 offen offset:1024 sc1\n"
    "v_pk_fma_f32 v[26:27], v[94:95], s[68:69], s[68:69] op_sel:[0,0,1] op_sel_hi:[1,0,1]\n"
    "v_pk_fma_f32 v[28:29], v[100:101], s[68:69], s[68:69] op_sel:[0,0,1] op_sel_hi:[1,0,1]\n"
    "v_pk_fma_f32 v[30:31], v[102:103], s[68:69], s[68:69] op_sel:[0,0,1] op_sel_hi:[1,0,1]\n"
    "v_pk_fma_f32 v[32:33], v[148:149], s[68:69], s[68:69] op_sel:[0,0,1] op_sel_hi:[1,0,1]\n"
    "v_pk_fma_f32 v[34:35], v[150:151], s[68:69], s[68:69] op_sel:[0,0,1] op_sel_hi:[1,0,1]\n"
    "v_exp_f32_e64 v16, v16 clamp\n"
    "v_exp_f32_e64 v17, v17 clamp\n"
    "buffer_load_dwordx4 a[8:11], v1, s[36:39], s64 offen offset:2048 sc1\n"
    "v_max_f32 v76, s70, v76\n"
    "v_max_f32 v77, s70, v77\n"
    "v_exp_f32_e64 v18, v18 clamp\n"
    "v_exp_f32_e64 v19, v19 clamp\n"
    "v_max_f32 v78, s70, v78\n"
    "v_max_f32 v79, s70, v79\n"
    "v_exp_f32_e64 v20, v20 clamp\n"
    "buffer_load_dwordx4 a[12:15], v1, s[36:39], s64 offen offset:3072 sc1\n"
    "v_exp_f32_e64 v21, v21 clamp\n"
    "v_max_f32 v84, s70, v84\n"
    "v_max_f32 v85, s70, v85\n"
    "v_exp_f32_e64 v22, v22 clamp\n"
    "v_exp_f32_e64 v23, v23 clamp\n"
    "v_max_f32 v86, s70, v86\n"
    "v_max_f32 v87, s70, v87\n"
    "buffer_load_dwordx4 a[16:19], v247, s[36:39], s64 offen sc1\n"
    "v_exp_f32_e64 v24, v24 clamp\n"
    "v_exp_f32_e64 v25, v25 clamp\n"
    "v_max_f32 v92, s70, v92\n"
    "v_max_f32 v93, s70, v93\n"
    "v_exp_f32_e64 v26, v26 clamp\n"
    "v_exp_f32_e64 v27, v27 clamp\n"
    "s_add_u32 s64, s64, 5120\n"
    "v_max_f32 v94, s70, v94\n"
    "v_max_f32 v95, s70, v95\n"
    "v_exp_f32_e64 v28, v28 clamp\n"
    "v_exp_f32_e64 v29, v29 clamp\n"
    "v_max_f32 v100, s70, v100\n"
    "v_max_f32 v101, s70, v101\n"
    "v_exp_f32_e64 v30, v30 clamp\n"
    "s_cmp_eq_u32 s64, s72\n"
    "s_cselect_b32 s67, 40960, 0\n"
    "s_sub_u32 s64, s64, s67\n"
    "v_exp_f32_e64 v31, v31 clamp\n"
    "v_max_f32 v102, s70, v102\n"
    "v_max_f32 v103, s70, v103\n"
    "v_exp_f32_e64 v32, v32 clamp\n"
    "v_exp_f32_e64 v33, v33 clamp\n"
    "v_max_f32 v148, s70, v148\n"
    "v_max_f32 v149, s70, v149\n"
    "buffer_load_dwordx4 a[20:23], v1, s[36:39], s64 offen sc1\n"
    "v_exp_f32_e64 v34, v34 clamp\n"
    "v_exp_f32_e64 v35, v35 clamp\n"
    "v_max_f32 v150, s70, v150\n"
    "v_max_f32 v151, s70, v151\n"
    "v_pk_add_f32 v[76:77], v[76:77], v[16:17]\n"
    "v_pk_add_f32 v[78:79], v[78:79], v[18:19]\n"
    "v_pk_add_f32 v[84:85], v[84:85], v[20:21]\n"
    "buffer_load_dwordx4 a[24:27], v1, s[36:39], s64 offen offset:1024 sc1\n"
    "v_pk_add_f32 v[86:87], v[86:87], v[22:23]\n"
    "v_pk_add_f32 v[92:93], v[92:93], v[24:25]\n"
    "v_pk_add_f32 v[94:95], v[94:95], v[26:27]\n"
    "v_pk_add_f32 v[100:101], v[100:101], v[28:29]\n"
    "v_pk_add_f32 v[102:103], v[102:103], v[30:31]\n"
    "v_pk_add_f32 v[148:149], v[148:149], v[32:33]\n"
    "buffer_load_dwordx4 a[28:31], v1, s[36:39], s64 offen offset:2048 sc1\n"
    "v_pk_add_f32 v[150:151], v[150:151], v[34:35]\n"
    "v_cvt_pk_bf16_f32 v104, v76, v77\n"
    "v_cvt_pk_bf16_f32 v105, v78, v79\n"
    "v_cvt_pk_bf16_f32 v106, v84, v85\n"
    "v_cvt_pk_bf16_f32 v107, v86, v87\n"
    "v_cvt_pk_bf16_f32 v108, v92, v93\n"
    "v_cvt_pk_bf16_f32 v109, v94, v95\n"
    "buffer_load_dwordx4 a[32:35], v1, s[36:39], s64 offen offset:3072 sc1\n"
    "v_cvt_pk_bf16_f32 v110, v100, v101\n"
    "v_cvt_pk_bf16_f32 v111, v102, v103\n"
    "v_cvt_pk_bf16_f32 v112, v148, v149\n"
    "v_cvt_pk_bf16_f32 v113, v150, v151\n"
    "v_pk_fma_f32 v[16:17], v[80:81], s[68:69], s[68:69] op_sel:[0,0,1] op_sel_hi:[1,0,1]\n"
    "v_pk_fma_f32 v[18:19], v[82:83], s[68:69], s[68:69] op_sel:[0,0,1] op_sel_hi:[1,0,1]\n"
    "v_pk_fma_f32 v[20:21], v[88:89], s[68:69], s[68:69] op_sel:[0,0,1] op_sel_hi:[1,0,1]\n"
    "buffer_load_dwordx4 a[36:39], v247, s[36:39], s64 offen sc1\n"
    "v_pk_fma_f32 v[22:23], v[90:91], s[68:69], s[68:69] op_sel:[0,0,1] op_sel_hi:[1,0,1]\n"
    "v_pk_fma_f32 v[24:25], v[96:97], s[68:69], s[68:69] op_sel:[0,0,1] op_sel_hi:[1,0,1]\n"
    "v_pk_fma_f32 v[26:27], v[98:99], s[68:69], s[68:69] op_sel:[0,0,1] op_sel_hi:[1,0,1]\n"
    "v_pk_fma_f32 v[28:29], v[144:145], s[68:69], s[68:69] op_sel:[0,0,1] op_sel_hi:[1,0,1]\n"
    "v_pk_fma_f32 v[30:31], v[146:147], s[68:69], s[68:69] op_sel:[0,0,1] op_sel_hi:[1,0,1]\n"
    "v_pk_fma_f32 v[32:33], v[152:153], s[68:69], s[68:69] op_sel:[0,0,1] op_sel_hi:[1,0,1]\n"
    "v_pk_fma_f32 v[34:35], v[154:155], s[68:69], s[68:69] op_sel:[0,0,1] op_sel_hi:[1,0,1]\n"
    "s_add_u32 s64, s64, 5120\n"
    "v_exp_f32_e64 v16, v16 clamp\n"
    "v_exp_f32_e64 v17, v17 clamp\n"
    "v_max_f32 v80, s70, v80\n"
    "v_max_f32 v81, s70, v81\n"
    "v_exp_f32_e64 v18, v18 clamp\n"
    "v_exp_f32_e64 v19, v19 clamp\n"
    "s_cmp_eq_u32 s64, s72\n"
    "s_cselect_b32 s67, 40960, 0\n"
    "s_sub_u32 s64, s64, s67\n"
    "v_max_f32 v82, s70, v82\n"
    "v_max_f32 v83, s70, v83\n"
    "v_exp_f32_e64 v20, v20 clamp\n"
    "v_exp_f32_e64 v21, v21 clamp\n"
    "v_max_f32 v88, s70, v88\n"
    "v_max_f32 v89, s70, v89\n"
    "v_exp_f32_e64 v22, v22 clamp\n"
    "buffer_load_dwordx4 a[40:43], v1, s[36:39], s64 offen sc1\n"
    "v_exp_f32_e64 v23, v23 clamp\n"
    "v_max_f32 v90, s70, v90\n"
    "v_max_f32 v91, s70, v91\n"
    "v_exp_f32_e64 v24, v24 clamp\n"
    "v_exp_f32_e64 v25, v25 clamp\n"
    "v_max_f32 v96, s70, v96\n"
    "v_max_f32 v97, s70, v97\n"
    "buffer_load_dwordx4 a[44:47], v1, s[36:39], s64 offen offset:1024 sc1\n"
    "v_exp_f32_e64 v26, v26 clamp\n"
    "v_exp_f32_e64 v27, v27 clamp\n"
    "v_max_f32 v98, s70, v98\n"
    "v_max_f32 v99, s70, v99\n"
    "v_exp_f32_e64 v28, v28 clamp\n"
    "v_exp_f32_e64 v29, v29 clamp\n"
    "v_max_f32_e64 v144, s70, v144\n"
    "buffer_load_dwordx4 a[48:51], v1, s[36:39], s64 offen offset:2048 sc1\n"
    "v_max_f32_e64 v145, s70, v145\n"
    "v_exp_f32_e64 v30, v30 clamp\n"
    "v_exp_f32_e64 v31, v31 clamp\n"
    "v_max_f32 v146, s70, v146\n"
    "v_max_f32 v147, s70, v147\n"
    "v_exp_f32_e64 v32, v32 clamp\n"
    "buffer_load_dwordx4 a[52:55], v1, s[36:39], s64 offen offset:3072 sc1\n"
    "v_exp_f32_e64 v33, v33 clamp\n"
    "v_max_f32 v152, s70, v152\n"
    "v_max_f32 v153, s70, v153\n"
    "v_exp_f32_e64 v34, v34 clamp\n"
    "v_exp_f32_e64 v35, v35 clamp\n"
    "v_max_f32 v154, s70, v154\n"
    "v_max_f32 v155, s70, v155\n"
    "buffer_load_dwordx4 a[56:59], v247, s[36:39], s64 offen sc1\n"
    "v_pk_add_f32 v[80:81], v[80:81], v[16:17]\n"
    "v_pk_add_f32 v[82:83], v[82:83], v[18:19]\n"
    "v_pk_add_f32 v[88:89], v[88:89], v[20:21]\n"
    "v_pk_add_f32 v[90:91], v[90:91], v[22:23]\n"
    "v_pk_add_f32 v[96:97], v[96:97], v[24:25]\n"
    "v_pk_add_f32 v[98:99], v[98:99], v[26:27]\n"
    "v_pk_add_f32 v[144:145], v[144:145], v[28:29]\n"
    "s_add_u32 s64, s64, 5120\n"
    "v_pk_add_f32 v[146:147], v[146:147], v[30:31]\n"
    "v_pk_add_f32 v[152:153], v[152:153], v[32:33]\n"
    "v_pk_add_f32 v[154:155], v[154:155], v[34:35]\n"
    "v_cvt_pk_bf16_f32 v116, v80, v81\n"
    "v_cvt_pk_bf16_f32 v117, v82, v83\n"
    "v_cvt_pk_bf16_f32 v118, v88, v89\n"
    "v_cvt_pk_bf16_f32 v119, v90, v91\n"
    "s_cmp_eq_u32 s64, s72\n"
    "s_cselect_b32 s67, 40960, 0\n"
    "s_sub_u32 s64, s64, s67\n"
    "v_cvt_pk_bf16_f32 v120, v96, v97\n"
    "v_cvt_pk_bf16_f32 v121, v98, v99\n"
    "v_cvt_pk_bf16_f32 v122, v144, v145\n"
    "v_cvt_pk_bf16_f32 v123, v146, v147\n"
    "v_cvt_pk_bf16_f32 v124, v152, v153\n"
    "v_cvt_pk_bf16_f32 v125, v154, v155\n"
    "s_lshl2_add_u32 s46, s54, s3\n"
    "s_mul_i32 s91, s46, 49152\n"
    "buffer_load_dwordx4 a[60:63], v1, s[36:39], s64 offen sc1\n"
    "s_lshl_b32 s90, s47, 12\n"
    "s_add_u32 s90, s90, s91\n"
    "s_nop 0\n"
    "s_add_u32 s90, s90, 7110656\n"
    "s_add_u32 s91, s91, 7143424\n"
    "s_mul_i32 s46, s46, 20480\n"
    "s_mul_i32 s6, s55, 160\n"
    "s_add_u32 s46, s46, s6\n"
    "s_nop 0\n"
    "s_mul_i32 s7, s54, 81920\n"
    "s_mul_i32 s6, s3, 5120\n"
    "buffer_load_dwordx4 a[64:67], v1, s[36:39], s64 offen offset:1024 sc1\n"
    "s_add_u32 s7, s7, s6\n"
    "s_add_u32 s7, s7, 4489216\n"
    "s_add_u32 s6, s3, 0\n"
    "s_and_b32 s6, s6, 3\n"
    "v_lshl_add_u32 v2, s6, 10, v1\n"
    "s_mul_i32 s8, s6, 20480\n"
    "s_add_u32 s8, s8, s7\n"
    "s_add_u32 s6, s3, 1\n"
    "s_and_b32 s6, s6, 3\n"
    "buffer_load_dwordx4 a[68:71], v1, s[36:39], s64 offen offset:2048 sc1\n"
    "v_lshl_add_u32 v3, s6, 10, v1\n"
    "s_mul_i32 s9, s6, 20480\n"
    "s_add_u32 s9, s9, s7\n"
    "s_sub_u32 s6, s3, 1\n"
    "s_and_b32 s6, s6, 3\n"
    "s_mul_i32 s6, s6, 25600\n"
    "s_add_u32 s6, s6, 0\n"
    "v_add_u32_e64 v8, s6, v1\n"
    "buffer_load_dwordx4 a[72:75], v1, s[36:39], s64 offen offset:3072 sc1\n"
    "s_add_u32 s6, s3, 2\n"
    "s_and_b32 s6, s6, 3\n"
    "v_lshl_add_u32 v4, s6, 10, v1\n"
    "s_mul_i32 s10, s6, 20480\n"
    "s_add_u32 s10, s10, s7\n"
    "s_sub_u32 s6, s3, 2\n"
    "s_and_b32 s6, s6, 3\n"
    "s_nop 0\n"
    "buffer_load_dwordx4 a[76:79], v247, s[36:39], s64 offen sc1\n"
    "s_mul_i32 s6, s6, 25600\n"
    "s_add_u32 s6, s6, 5120\n"
    "v_add_u32_e64 v9, s6, v1\n"
    "s_add_u32 s6, s3, 3\n"
    "s_and_b32 s6, s6, 3\n"
    "v_lshl_add_u32 v5, s6, 10, v1\n"
    "s_mul_i32 s11, s6, 20480\n"
    "s_add_u32 s11, s11, s7\n"
    "s_add_u32 s64, s64, 5120\n"
    "s_sub_u32 s6, s3, 3\n"
    "s_and_b32 s6, s6, 3\n"
    "s_mul_i32 s6, s6, 25600\n"
    "s_add_u32 s6, s6, 10240\n"
    "v_add_u32 v10, s6, v1\n"
    "v_add_u32 v7, s73, v1\n"
    "v_lshlrev_b32 v11, 3, v0\n"
    "v_add_u32 v11, 4096, v11\n"
    "s_cmp_eq_u32 s64, s72\n"
    "s_cselect_b32 s67, 40960, 0\n"
    "s_sub_u32 s64, s64, s67\n"
    "v_and_b32 v38, 15, v0\n"
    "v_lshrrev_b32 v39, 4, v0\n"
    "s_add_u32 s6, s73, 15360\n"
    "v_mul_u32_u24 v38, 0xb0, v38\n"
    "v_lshl_add_u32 v39, v39, 3, s6\n"
    "v_add_u32 v240, v38, v39\n"
    "v_add_u32 v38, 0, v0\n"
    "v_mul_u32_u24 v39, 0xcd, v38\n"
    "v_lshrrev_b32 v39, 11, v39\n"
    "v_lshlrev_b32 v38, 4, v38\n"
    "buffer_load_dwordx4 a[80:83], v1, s[36:39], s64 offen sc1\n"
    "v_lshl_add_u32 v40, v39, 4, v38\n"
    "v_mul_u32_u24 v39, 0x460, v39\n"
    "v_add_u32 v241, s6, v40\n"
    "v_add_u32 v244, v39, v38\n"
    "v_add_u32_e64 v38, 64, v0\n"
    "v_mul_u32_u24 v39, 0xcd, v38\n"
    "v_lshrrev_b32 v39, 11, v39\n"
    "v_lshlrev_b32 v38, 4, v38\n"
    "buffer_load_dwordx4 a[84:87], v1, s[36:39], s64 offen offset:1024 sc1\n"
    "v_lshl_add_u32 v40, v39, 4, v38\n"
    "v_mul_u32_u24 v39, 0x460, v39\n"
    "v_add_u32 v242, s6, v40\n"
    "v_add_u32 v245, v39, v38\n"
    "v_add_u32 v38, 128, v0\n"
    "v_mul_u32_u24 v39, 0xcd, v38\n"
    "v_lshrrev_b32 v39, 11, v39\n"
    "v_lshlrev_b32 v38, 4, v38\n"
    "buffer_load_dwordx4 a[88:91], v1, s[36:39], s64 offen offset:2048 sc1\n"
    "v_lshl_add_u32 v40, v39, 4, v38\n"
    "v_mul_u32_u24 v39, 0x460, v39\n"
    "v_add_u32 v243, s6, v40\n"
    "v_cmp_gt_u32 vcc, 32, v0\n"
    "v_mov_b32 v40, 2147483648\n"
    "v_add_u32_e64 v246, v39, v38\n"
    "v_cndmask_b32 v246, v40, v246, vcc\n"
    "s_lshl2_add_u32 s88, s4, s3\n"
    "s_mul_i32 s88, s88, 40960\n"
    "s_mul_i32 s6, s5, 2560\n"
    "s_add_u32 s88, s88, s6\n"
    "buffer_load_dwordx4 a[92:95], v1, s[36:39], s64 offen offset:3072 sc1\n"
    "s_and_b32 s6, s5, 1\n"
    "s_lshl_b32 s7, s6, 9\n"
    "s_sub_u32 s88, s88, s7\n"
    "s_add_u32 s88, s88, 4489216\n"
    "s_mul_i32 s6, s6, 1536\n"
    "s_sub_u32 s6, 4096, s6\n"
    "v_lshl_add_u32 v254, v0, 3, s6\n"
    "s_add_u32 s18, s88, 20480\n"
    "s_lshl2_add_u32 s21, s5, s3\n"
    "s_lshl_b32 s21, s21, 10\n"
    "buffer_load_dwordx4 a[96:99], v247, s[36:39], s64 offen sc1\n"
    "s_add_u32 s21, s21, 32768\n"
    "v_lshl_add_u32 v38, v0, 1, 0\n"
    "s_mul_i32 s6, s3, 20\n"
    "v_subrev_u32 v41, 80, v38\n"
    "v_mul_u32_u24 v42, 52, v41\n"
    "v_lshrrev_b32 v42, 8, v42\n"
    "v_mul_u32_u24 v43, 5, v42\n"
    "v_sub_u32 v41, v41, v43\n"
    "v_add_u32 v41, 16, v41\n"
    "s_add_u32 s64, s64, 5120\n"
    "v_cmp_gt_u32 vcc, 80, v38\n"
    "v_and_b32 v39, 15, v38\n"
    "v_lshrrev_b32 v40, 4, v38\n"
    "v_cndmask_b32 v39, v41, v39, vcc\n"
    "v_cndmask_b32 v40, v42, v40, vcc\n"
    "v_add_u32_e64 v41, s6, v39\n"
    "s_add_u32 s6, s3, 80\n"
    "v_cmp_eq_u32 vcc, 20, v39\n"
    "v_mov_b32 v42, s6\n"
    "s_mul_i32 s6, s3, 5\n"
    "v_cndmask_b32 v41, v41, v42, vcc\n"
    "v_add_u32 v40, s6, v40\n"
    "v_lshrrev_b32 v42, 4, v41\n"
    "v_mul_u32_u24_e64 v42, 3, v42\n"
    "v_lshrrev_b32 v43, 3, v40\n"
    "s_cmp_eq_u32 s64, s72\n"
    "s_cselect_b32 s67, 40960, 0\n"
    "s_sub_u32 s64, s64, s67\n"
    "v_add_u32 v42, v42, v43\n"
    "v_and_b32 v41, 15, v41\n"
    "v_lshlrev_b32 v41, 4, v41\n"
    "v_lshl_add_u32 v41, v42, 10, v41\n"
    "v_and_b32_e64 v42, 3, v40\n"
    "v_lshl_add_u32 v41, v42, 8, v41\n"
    "v_bfe_u32 v42, v40, 2, 1\n"
    "v_lshl_add_u32 v41, v42, 3, v41\n"
    "v_cmp_gt_u32 vcc, 105, v38\n"
    "v_add_u32 v41, 143360, v41\n"
    "v_mov_b32 v42, 162816\n"
    "v_cndmask_b32_e64 v250, v42, v41, vcc\n"
    "buffer_load_dwordx4 a[100:103], v1, s[36:39], s64 offen sc1\n"
    "v_lshl_add_u32 v38, v0, 1, 1\n"
    "s_mul_i32 s6, s3, 20\n"
    "v_subrev_u32 v41, 80, v38\n"
    "v_mul_u32_u24 v42, 52, v41\n"
    "v_lshrrev_b32 v42, 8, v42\n"
    "v_mul_u32_u24 v43, 5, v42\n"
    "v_sub_u32 v41, v41, v43\n"
    "v_add_u32 v41, 16, v41\n"
    "v_cmp_gt_u32 vcc, 80, v38\n"
    "v_and_b32 v39, 15, v38\n"
    "v_lshrrev_b32 v40, 4, v38\n"
    "v_cndmask_b32 v39, v41, v39, vcc\n"
    "v_cndmask_b32 v40, v42, v40, vcc\n"
    "buffer_load_dwordx4 a[104:107], v1, s[36:39], s64 offen offset:1024 sc1\n"
    "v_add_u32_e64 v41, s6, v39\n"
    "s_add_u32 s6, s3, 80\n"
    "v_cmp_eq_u32 vcc, 20, v39\n"
    "v_mov_b32 v42, s6\n"
    "s_mul_i32 s6, s3, 5\n"
    "v_cndmask_b32 v41, v41, v42, vcc\n"
    "v_add_u32 v40, s6, v40\n"
    "v_lshrrev_b32 v42, 4, v41\n"
    "v_mul_u32_u24 v42, 3, v42\n"
    "v_lshrrev_b32 v43, 3, v40\n"
    "v_add_u32 v42, v42, v43\n"
    "v_and_b32 v41, 15, v41\n"
    "buffer_load_dwordx4 a[108:111], v1, s[36:39], s64 offen offset:2048 sc1\n"
    "v_lshlrev_b32_e64 v41, 4, v41\n"
    "v_lshl_add_u32 v41, v42, 10, v41\n"
    "v_and_b32_e64 v42, 3, v40\n"
    "v_lshl_add_u32 v41, v42, 8, v41\n"
    "v_bfe_u32 v42, v40, 2, 1\n"
    "v_lshl_add_u32 v41, v42, 3, v41\n"
    "v_cmp_gt_u32 vcc, 105, v38\n"
    "v_add_u32 v41, 143360, v41\n"
    "v_mov_b32 v42, 162816\n"
    "v_cndmask_b32_e64 v251, v42, v41, vcc\n"
    "v_add_u32 v252, 143360, v1\n"
    "v_and_b32 v253, 0x330, v1\n"
    "buffer_load_dwordx4 a[112:115], v1, s[36:39], s64 offen offset:3072 sc1\n"
    "v_add_u32 v253, 158720, v253\n"
    "s_mov_b64 s[84:85], 0xffff\n"
    "s_mov_b32 s86, -1\n"
    "s_mov_b32 s87, 0\n"
    "s_mov_b32 s0, -1\n"
    "s_nop 0\n"
    "s_mov_b32 s1, 0xffff\n"
    "buffer_load_dwordx4 a[116:119], v247, s[36:39], s64 offen sc1\n"
    "s_add_u32 s64, s64, 5120\n"
    "s_cmp_eq_u32 s64, s72\n"
    "s_cselect_b32 s67, 40960, 0\n"
    "s_sub_u32 s64, s64, s67\n"
    "buffer_load_dwordx4 a[120:123], v1, s[36:39], s64 offen sc1\n"
    "buffer_load_dwordx4 a[124:127], v1, s[36:39], s64 offen offset:1024 sc1\n"
    "buffer_load_dwordx4 a[128:131], v1, s[36:39], s64 offen offset:2048 sc1\n"
    "buffer_load_dwordx4 a[132:135], v1, s[36:39], s64 offen offset:3072 sc1\n"
    "buffer_load_dwordx4 a[136:139], v247, s[36:39], s64 offen sc1\n"
    "s_add_u32 s64, s64, 5120\n"
    "s_cmp_eq_u32 s64, s72\n"
    "s_cselect_b32 s67, 40960, 0\n"
    "s_sub_u32 s64, s64, s67\n"
    "buffer_load_dwordx4 a[140:143], v1, s[36:39], s64 offen sc1\n"
    "buffer_load_dwordx4 a[144:147], v1, s[36:39], s64 offen offset:1024 sc1\n"
    "buffer_load_dwordx4 a[148:151], v1, s[36:39], s64 offen offset:2048 sc1\n"
    "buffer_load_dwordx4 a[152:155], v1, s[36:39], s64 offen offset:3072 sc1\n"
    "buffer_load_dwordx4 a[156:159], v247, s[36:39], s64 offen sc1\n"
    "s_add_u32 s64, s72, 20480\n"
    "s_add_u32 m0, s73, 0\n"
    "s_nop 0\n"
    "buffer_load_dwordx4 v1, s[36:39], s64 offen sc1 lds\n"
    "buffer_load_dwordx4 v1, s[36:39], s64 offen offset:1024 sc1 lds\n"
    "buffer_load_dwordx4 v1, s[36:39], s64 offen offset:2048 sc1 lds\n"
    "buffer_load_dwordx4 v1, s[36:39], s64 offen offset:3072 sc1 lds\n"
    "s_add_u32 m0, s73, 4096\n"
    "s_nop 0\n"
    "s_nop 0\n"
    "buffer_load_dwordx4 v247, s[36:39], s64 offen sc1 lds\n"
    "s_add_u32 s64, s64, 5120\n"
    "s_add_u32 m0, s73, 5120\n"
    "s_nop 0\n"
    "s_nop 0\n"
    "buffer_load_dwordx4 v1, s[36:39], s64 offen sc1 lds\n"
    "buffer_load_dwordx4 v1, s[36:39], s64 offen offset:1024 sc1 lds\n"
    "buffer_load_dwordx4 v1, s[36:39], s64 offen offset:2048 sc1 lds\n"
    "buffer_load_dwordx4 v1, s[36:39], s64 offen offset:3072 sc1 lds\n"
    "s_add_u32 m0, s73, 9216\n"
    "s_nop 0\n"
    "s_nop 0\n"
    "buffer_load_dwordx4 v247, s[36:39], s64 offen sc1 lds\n"
    "s_add_u32 s64, s64, 5120\n"
    "s_add_u32 m0, s73, 10240\n"
    "s_nop 0\n"
    "s_nop 0\n"
    "buffer_load_dwordx4 v1, s[36:39], s64 offen sc1 lds\n"
    "buffer_load_dwordx4 v1, s[36:39], s64 offen offset:1024 sc1 lds\n"
    "buffer_load_dwordx4 v1, s[36:39], s64 offen offset:2048 sc1 lds\n"
    "buffer_load_dwordx4 v1, s[36:39], s64 offen offset:3072 sc1 lds\n"
    "s_add_u32 m0, s73, 14336\n"
    "s_nop 0\n"
    "s_nop 0\n"
    "buffer_load_dwordx4 v247, s[36:39], s64 offen sc1 lds\n"
    "s_add_u32 s64, s64, 5120\n"
    "buffer_load_dword v14, v6, s[36:39], s16 offen offset:512 sc1\n"
    "buffer_load_dwordx4 v[80:83], v1, s[36:39], s21 offen sc1\n"
    "s_add_u32 m0, s73, 15360\n"
    "s_nop 0\n"
    "s_nop 0\n"
    "buffer_load_dwordx4 v1, s[36:39], s64 offen sc1 lds\n"
    "buffer_load_dwordx4 v1, s[36:39], s64 offen offset:1024 sc1 lds\n"
    "buffer_load_dwordx4 v1, s[36:39], s64 offen offset:2048 sc1 lds\n"
    "buffer_load_dwordx4 v1, s[36:39], s64 offen offset:3072 sc1 lds\n"
    "s_add_u32 m0, s73, 19456\n"
    "s_nop 0\n"
    "s_nop 0\n"
    "buffer_load_dwordx4 v247, s[36:39], s64 offen sc1 lds\n"
    "s_add_u32 s64, s64, 5120\n"
    "s_add_u32 m0, s73, 20480\n"
    "s_nop 0\n"
    "s_nop 0\n"
    "buffer_load_dwordx4 v1, s[36:39], s64 offen sc1 lds\n"
    "buffer_load_dwordx4 v1, s[36:39], s64 offen offset:1024 sc1 lds\n"
    "buffer_load_dwordx4 v1, s[36:39], s64 offen offset:2048 sc1 lds\n"
    "buffer_load_dwordx4 v1, s[36:39], s64 offen offset:3072 sc1 lds\n"
    "s_add_u32 m0, s73, 24576\n"
    "s_nop 0\n"
    "buffer_load_dwordx4 v247, s[36:39], s64 offen sc1 lds\n"
    "s_add_u32 s64, s64, 5120\n"
    "s_waitcnt vmcnt(11)\n"
    "v_cmp_ne_u32 vcc, s63, v14\n"
    "s_nop 1\n"
    "s_cmp_eq_u64 vcc, 0\n"
    "s_cbranch_scc0 L_hfq_slow_%=\n"
    "L_hfq_ok_%=:\n"
    "s_waitcnt vmcnt(10)\n"
    "v_max_u32 v84, v80, v81\n"
    "v_max_u32_e64 v85, v82, v83\n"
    "v_cmp_eq_u32_e64 s[22:23], v84, -1\n"
    "v_cmp_eq_u32 vcc, -1, v85\n"
    "s_nop 0\n"
    "s_and_b32 s23, s23, 0x1fffff\n"
    "s_and_b32 vcc_hi, vcc_hi, 0xfffff\n"
    "s_or_b64 vcc, vcc, s[22:23]\n"
    "s_cmp_eq_u64 vcc, 0\n"
    "s_cbranch_scc0 L_pb_slow_%=\n"
    "L_pb_ok_%=:\n"
    "s_nop 0\n"
    "ds_write_b64 v250, v[80:81]\n"
    "ds_write_b64 v251, v[82:83]\n"
    "buffer_load_dwordx4 v[16:19], v2, s[36:39], s90 offen sc1\n"
    "buffer_load_dwordx4 v[20:23], v3, s[36:39], s90 offen sc1\n"
    "buffer_load_dwordx4 v[24:27], v4, s[36:39], s90 offen sc1\n"
    "buffer_load_dwordx4 v[28:31], v5, s[36:39], s90 offen sc1\n"
    "s_add_u32 s90, s90, 4096\n"
    "s_cmp_eq_u32 s90, s91\n"
    "s_cselect_b32 s67, 32768, 0\n"
    "s_sub_u32 s90, s90, s67\n"
    "buffer_load_dwordx4 v[32:35], v2, s[36:39], s90 offen sc1\n"
    "buffer_load_dwordx4 v[36:39], v3, s[36:39], s90 offen sc1\n"
    "buffer_load_dwordx4 v[40:43], v4, s[36:39], s90 offen sc1\n"
    "buffer_load_dwordx4 v[44:47], v5, s[36:39], s90 offen sc1\n"
    "s_add_u32 s90, s90, 4096\n"
    "s_cmp_eq_u32 s90, s91\n"
    "s_cselect_b32 s67, 32768, 0\n"
    "s_sub_u32 s90, s90, s67\n"
    "s_waitcnt lgkmcnt(0)\n"
    "s_barrier\n"
    "ds_read_b128 v[160:163], v253 offset:0\n"
    "ds_read_b128 v[164:167], v253 offset:1024\n"
    "ds_read_b128 v[168:171], v253 offset:2048\n"
    "ds_read_b128 v[172:175], v252 offset:0\n"
    "ds_read_b128 v[176:179], v252 offset:3072\n"
    "ds_read_b128 v[180:183], v252 offset:4096\n"
    "ds_read_b128 v[184:187], v252 offset:6144\n"
    "ds_read_b128 v[188:191], v252 offset:7168\n"
    "ds_read_b128 v[192:195], v252 offset:10240\n"
    "ds_read_b128 v[196:199], v252 offset:11264\n"
    "ds_read_b128 v[200:203], v252 offset:13312\n"
    "ds_read_b128 v[204:207], v252 offset:14336\n"
    "s_waitcnt lgkmcnt(9)\n"
    "v_mfma_f32_16x16x32_bf16 v[100:103], v[160:163], v[104:107], 0\n"
    "buffer_load_dwordx4 v[48:51], v2, s[36:39], s90 offen sc1\n"
    "v_mfma_f32_16x16x32_bf16 v[148:151], v[160:163], v[116:119], 0\n"
    "buffer_load_dwordx4 v[52:55], v3, s[36:39], s90 offen sc1\n"
    "v_mfma_f32_16x16x32_bf16 v[100:103], v[164:167], v[108:111], v[100:103]\n"
    "buffer_load_dwordx4 v[56:59], v4, s[36:39], s90 offen sc1\n"
    "v_mfma_f32_16x16x32_bf16 v[148:151], v[164:167], v[120:123], v[148:151]\n"
    "buffer_load_dwordx4 v[60:63], v5, s[36:39], s90 offen sc1\n"
    "v_mfma_f32_16x16x32_bf16 v[100:103], v[168:171], v[112:115], v[100:103]\n"
    "s_add_u32 s90, s90, 4096\n"
    "s_cmp_eq_u32 s90, s91\n"
    "s_cselect_b32 s67, 32768, 0\n"
    "v_mfma_f32_16x16x32_bf16 v[148:151], v[168:171], v[124:127], v[148:151]\n"
    "s_sub_u32 s90, s90, s67\n"
    "s_waitcnt lgkmcnt(6)\n"
    "v_mfma_f32_16x16x32_bf16 v[80:83], v[172:175], v[104:107], 0\n"
    "v_mfma_f32_16x16x32_bf16 v[128:131], v[172:175], v[116:119], 0\n"
    "v_mfma_f32_16x16x32_bf16 v[84:87], v[176:179], v[104:107], 0\n"
    "v_mfma_f32_16x16x32_bf16 v[132:135], v[176:179], v[116:119], 0\n"
    "v_add_f32 v100, s70, v100\n"
    "v_add_f32 v101, s70, v101\n"
    "v_mfma_f32_16x16x32_bf16 v[84:87], v[180:183], v[108:111], v[84:87]\n"
    "v_add_f32 v102, s70, v102\n"
    "v_add_f32 v103, s70, v103\n"
    "v_mfma_f32_16x16x32_bf16 v[132:135], v[180:183], v[120:123], v[132:135]\n"
    "v_add_f32_e64 v148, s70, v148\n"
    "v_add_f32 v149, s70, v149\n"
    "s_waitcnt lgkmcnt(0)\n"
    "v_mfma_f32_16x16x32_bf16 v[88:91], v[184:187], v[104:107], 0\n"
    "v_add_f32 v150, s70, v150\n"
    "v_add_f32 v151, s70, v151\n"
    "v_mfma_f32_16x16x32_bf16 v[136:139], v[184:187], v[116:119], 0\n"
    "v_rcp_f32_e64 v100, v100\n"
    "v_mfma_f32_16x16x32_bf16 v[88:91], v[188:191], v[108:111], v[88:91]\n"
    "v_rcp_f32_e64 v101, v101\n"
    "v_mfma_f32_16x16x32_bf16 v[136:139], v[188:191], v[120:123], v[136:139]\n"
    "v_rcp_f32_e64 v102, v102\n"
    "v_mfma_f32_16x16x32_bf16 v[92:95], v[192:195], v[108:111], 0\n"
    "v_rcp_f32_e64 v103, v103\n"
    "v_mfma_f32_16x16x32_bf16 v[140:143], v[192:195], v[120:123], 0\n"
    "v_rcp_f32_e64 v148, v148\n"
    "v_mfma_f32_16x16x32_bf16 v[92:95], v[196:199], v[112:115], v[92:95]\n"
    "v_rcp_f32_e64 v149, v149\n"
    "v_mfma_f32_16x16x32_bf16 v[140:143], v[196:199], v[124:127], v[140:143]\n"
    "v_rcp_f32_e64 v150, v150\n"
    "v_mfma_f32_16x16x32_bf16 v[96:99], v[200:203], v[108:111], 0\n"
    "v_rcp_f32_e64 v151, v151\n"
    "v_mfma_f32_16x16x32_bf16 v[144:147], v[200:203], v[120:123], 0\n"
    "v_mul_f32 v100, v158, v100\n"
    "v_mul_f32 v101, v158, v101\n"
    "v_mfma_f32_16x16x32_bf16 v[96:99], v[204:207], v[112:115], v[96:99]\n"
    "v_mul_f32 v102, v158, v102\n"
    "v_mul_f32 v103, v158, v103\n"
    "v_mfma_f32_16x16x32_bf16 v[144:147], v[204:207], v[124:127], v[144:147]\n"
    "v_mul_f32 v148, v159, v148\n"
    "v_mul_f32 v149, v159, v149\n"
    "v_mul_f32 v150, v159, v150\n"
    "v_mul_f32 v151, v159, v151\n"
    "v_cndmask_b32_e64 v152, v101, v100, s[84:85]\n"
    "v_cndmask_b32_e64 v154, v102, v101, s[86:87]\n"
    "v_cndmask_b32_e64 v156, v103, v102, s[0:1]\n"
    "v_cndmask_b32_e64 v153, v149, v148, s[84:85]\n"
    "v_cndmask_b32_e64 v155, v150, v149, s[86:87]\n"
    "v_cndmask_b32_e64 v157, v151, v150, s[0:1]\n"
    "v_pk_mul_f32 v[80:81], v[80:81], v[100:101] op_sel_hi:[1,0]\n"
    "v_pk_mul_f32 v[82:83], v[82:83], v[100:101] op_sel_hi:[1,0]\n"
    "v_pk_mul_f32 v[128:129], v[128:129], v[148:149] op_sel_hi:[1,0]\n"
    "v_pk_mul_f32 v[130:131], v[130:131], v[148:149] op_sel_hi:[1,0]\n"
    "v_cvt_pk_bf16_f32 v104, v80, v81\n"
    "v_cvt_pk_bf16_f32 v105, v82, v83\n"
    "v_cvt_pk_bf16_f32 v116, v128, v129\n"
    "v_cvt_pk_bf16_f32 v117, v130, v131\n"
    "v_pk_mul_f32 v[84:85], v[84:85], v[152:153] op_sel_hi:[1,0]\n"
    "v_pk_mul_f32 v[86:87], v[86:87], v[152:153] op_sel_hi:[1,0]\n"
    "v_pk_mul_f32 v[132:133], v[132:133], v[152:153] op_sel:[0,1] op_sel_hi:[1,1]\n"
    "v_pk_mul_f32 v[134:135], v[134:135], v[152:153] op_sel:[0,1] op_sel_hi:[1,1]\n"
    "v_cvt_pk_bf16_f32 v106, v84, v85\n"
    "v_cvt_pk_bf16_f32 v107, v86, v87\n"
    "v_cvt_pk_bf16_f32 v118, v132, v133\n"
    "v_cvt_pk_bf16_f32 v119, v134, v135\n"
    "buffer_store_dwordx4 v[104:107], v1, s[36:39], s88 offen sc1\n"
    "buffer_store_dwordx4 v[116:119], v1, s[36:39], s18 offen sc1\n"
    "v_pk_mul_f32 v[88:89], v[88:89], v[154:155] op_sel_hi:[1,0]\n"
    "v_pk_mul_f32 v[90:91], v[90:91], v[154:155] op_sel_hi:[1,0]\n"
    "v_pk_mul_f32 v[136:137], v[136:137], v[154:155] op_sel:[0,1] op_sel_hi:[1,1]\n"
    "v_pk_mul_f32 v[138:139], v[138:139], v[154:155] op_sel:[0,1] op_sel_hi:[1,1]\n"
    "v_cvt_pk_bf16_f32 v108, v88, v89\n"
    "v_cvt_pk_bf16_f32 v109, v90, v91\n"
    "v_cvt_pk_bf16_f32 v120, v136, v137\n"
    "v_cvt_pk_bf16_f32 v121, v138, v139\n"
    "v_pk_mul_f32 v[92:93], v[92:93], v[156:157] op_sel_hi:[1,0]\n"
    "v_pk_mul_f32 v[94:95], v[94:95], v[156:157] op_sel_hi:[1,0]\n"
    "v_pk_mul_f32 v[140:141], v[140:141], v[156:157] op_sel:[0,1] op_sel_hi:[1,1]\n"
    "v_pk_mul_f32 v[142:143], v[142:143], v[156:157] op_sel:[0,1] op_sel_hi:[1,1]\n"
    "v_cvt_pk_bf16_f32 v110, v92, v93\n"
    "v_cvt_pk_bf16_f32 v111, v94, v95\n"
    "v_cvt_pk_bf16_f32 v122, v140, v141\n"
    "v_cvt_pk_bf16_f32 v123, v142, v143\n"
    "buffer_store_dwordx4 v[108:111], v1, s[36:39], s88 offen offset:1024 sc1\n"
    "buffer_store_dwordx4 v[120:123], v1, s[36:39], s18 offen offset:1024 sc1\n"
    "v_pk_mul_f32 v[96:97], v[96:97], v[102:103] op_sel:[0,1] op_sel_hi:[1,1]\n"
    "v_pk_mul_f32 v[98:99], v[98:99], v[102:103] op_sel:[0,1] op_sel_hi:[1,1]\n"
    "v_pk_mul_f32 v[144:145], v[144:145], v[150:151] op_sel:[0,1] op_sel_hi:[1,1]\n"
    "v_pk_mul_f32 v[146:147], v[146:147], v[150:151] op_sel:[0,1] op_sel_hi:[1,1]\n"
    "v_cvt_pk_bf16_f32 v112, v96, v97\n"
    "v_cvt_pk_bf16_f32 v113, v98, v99\n"
    "v_cvt_pk_bf16_f32 v124, v144, v145\n"
    "v_cvt_pk_bf16_f32 v125, v146, v147\n"
    "buffer_store_dwordx2 v[112:113], v254, s[36:39], s88 offen sc1\n"
    "buffer_store_dwordx2 v[124:125], v254, s[36:39], s18 offen sc1\n"
    "L_hloop_%=:\n"
    "s_bitcmp1_b32 s52, 1\n"
    "s_cbranch_scc1 L_nw0_%=\n"
    "s_waitcnt vmcnt(8)\n"
    "L_nw0_%=:\n"
    "s_waitcnt vmcnt(14)\n"
    "v_mfma_f32_16x16x32_bf16 v[160:163], a[0:3], v[16:19], 0\n"
    "buffer_load_dwordx4 v[64:67], v2, s[36:39], s90 offen sc1\n"
    "v_mfma_f32_16x16x32_bf16 v[180:183], a[0:3], v[20:23], 0\n"
    "v_mfma_f32_16x16x32_bf16 v[200:203], a[0:3], v[24:27], 0\n"
    "v_mfma_f32_16x16x32_bf16 v[220:223], a[0:3], v[28:31], 0\n"
    "buffer_load_dwordx4 v[68:71], v3, s[36:39], s90 offen sc1\n"
    "v_mfma_f32_16x16x32_bf16 v[224:227], a[4:7], v[28:31], 0\n"
    "v_mfma_f32_16x16x32_bf16 v[204:207], a[4:7], v[24:27], 0\n"
    "v_mfma_f32_16x16x32_bf16 v[184:187], a[4:7], v[20:23], 0\n"
    "buffer_load_dwordx4 v[72:75], v4, s[36:39], s90 offen sc1\n"
    "v_mfma_f32_16x16x32_bf16 v[164:167], a[4:7], v[16:19], 0\n"
    "v_mfma_f32_16x16x32_bf16 v[168:171], a[8:11], v[16:19], 0\n"
    "v_mfma_f32_16x16x32_bf16 v[188:191], a[8:11], v[20:23], 0\n"
    "v_mfma_f32_16x16x32_bf16 v[208:211], a[8:11], v[24:27], 0\n"
    "buffer_load_dwordx4 v[76:79], v5, s[36:39], s90 offen sc1\n"
    "v_mfma_f32_16x16x32_bf16 v[228:231], a[8:11], v[28:31], 0\n"
    "v_mfma_f32_16x16x32_bf16 v[232:235], a[12:15], v[28:31], 0\n"
    "v_mfma_f32_16x16x32_bf16 v[212:215], a[12:15], v[24:27], 0\n"
    "v_mfma_f32_16x16x32_bf16 v[192:195], a[12:15], v[20:23], 0\n"
    "s_add_u32 s90, s90, 4096\n"
    "v_mfma_f32_16x16x32_bf16 v[172:175], a[12:15], v[16:19], 0\n"
    "s_cmp_eq_u32 s90, s91\n"
    "v_mfma_f32_16x16x32_bf16 v[176:179], a[16:19], v[16:19], 0\n"
    "s_cselect_b32 s67, 32768, 0\n"
    "v_mfma_f32_16x16x32_bf16 v[196:199], a[16:19], v[20:23], 0\n"
    "v_mfma_f32_16x16x32_bf16 v[216:219], a[16:19], v[24:27], 0\n"
    "s_sub_u32 s90, s90, s67\n"
    "v_mfma_f32_16x16x32_bf16 v[236:239], a[16:19], v[28:31], 0\n"
    "s_bitcmp1_b32 s52, 1\n"
    "s_cbranch_scc1 L_nw1_%=\n"
    "s_waitcnt vmcnt(8)\n"
    "L_nw1_%=:\n"
    "s_waitcnt vmcnt(14)\n"
    "v_mfma_f32_16x16x32_bf16 v[160:163], a[20:23], v[32:35], v[160:163]\n"
    "buffer_load_dwordx4 v[16:19], v2, s[36:39], s90 offen sc1\n"
    "v_mfma_f32_16x16x32_bf16 v[180:183], a[20:23], v[36:39], v[180:183]\n"
    "ds_read_b128 a[0:3], v7 offset:0\n"
    "v_mfma_f32_16x16x32_bf16 v[200:203], a[20:23], v[40:43], v[200:203]\n"
    "v_mfma_f32_16x16x32_bf16 v[220:223], a[20:23], v[44:47], v[220:223]\n"
    "buffer_load_dwordx4 v[20:23], v3, s[36:39], s90 offen sc1\n"
    "v_mfma_f32_16x16x32_bf16 v[224:227], a[24:27], v[44:47], v[224:227]\n"
    "ds_read_b128 a[4:7], v7 offset:1024\n"
    "v_mfma_f32_16x16x32_bf16 v[204:207], a[24:27], v[40:43], v[204:207]\n"
    "v_mfma_f32_16x16x32_bf16 v[184:187], a[24:27], v[36:39], v[184:187]\n"
    "buffer_load_dwordx4 v[24:27], v4, s[36:39], s90 offen sc1\n"
    "v_mfma_f32_16x16x32_bf16 v[164:167], a[24:27], v[32:35], v[164:167]\n"
    "ds_read_b128 a[8:11], v7 offset:2048\n"
    "v_mfma_f32_16x16x32_bf16 v[168:171], a[28:31], v[32:35], v[168:171]\n"
    "v_mfma_f32_16x16x32_bf16 v[188:191], a[28:31], v[36:39], v[188:191]\n"
    "v_mfma_f32_16x16x32_bf16 v[208:211], a[28:31], v[40:43], v[208:211]\n"
    "buffer_load_dwordx4 v[28:31], v5, s[36:39], s90 offen sc1\n"
    "v_mfma_f32_16x16x32_bf16 v[228:231], a[28:31], v[44:47], v[228:231]\n"
    "ds_read_b128 a[12:15], v7 offset:3072\n"
    "v_mfma_f32_16x16x32_bf16 v[232:235], a[32:35], v[44:47], v[232:235]\n"
    "v_mfma_f32_16x16x32_bf16 v[212:215], a[32:35], v[40:43], v[212:215]\n"
    "v_mfma_f32_16x16x32_bf16 v[192:195], a[32:35], v[36:39], v[192:195]\n"
    "ds_read_b128 a[16:19], v7 offset:4096\n"
    "s_add_u32 s90, s90, 4096\n"
    "v_mfma_f32_16x16x32_bf16 v[172:175], a[32:35], v[32:35], v[172:175]\n"
    "s_cmp_eq_u32 s90, s91\n"
    "v_mfma_f32_16x16x32_bf16 v[176:179], a[36:39], v[32:35], v[176:179]\n"
    "s_cselect_b32 s67, 32768, 0\n"
    "v_mfma_f32_16x16x32_bf16 v[196:199], a[36:39], v[36:39], v[196:199]\n"
    "v_mfma_f32_16x16x32_bf16 v[216:219], a[36:39], v[40:43], v[216:219]\n"
    "s_sub_u32 s90, s90, s67\n"
    "v_mfma_f32_16x16x32_bf16 v[236:239], a[36:39], v[44:47], v[236:239]\n"
    "s_bitcmp1_b32 s52, 1\n"
    "s_cbranch_scc1 L_nw2_%=\n"
    "s_waitcnt vmcnt(8)\n"
    "L_nw2_%=:\n"
    "s_waitcnt vmcnt(14)\n"
    "v_mfma_f32_16x16x32_bf16 v[160:163], a[40:43], v[48:51], v[160:163]\n"
    "buffer_load_dwordx4 v[32:35], v2, s[36:39], s90 offen sc1\n"
    "v_mfma_f32_16x16x32_bf16 v[180:183], a[40:43], v[52:55], v[180:183]\n"
    "ds_read_b128 a[20:23], v7 offset:5120\n"
    "v_mfma_f32_16x16x32_bf16 v[200:203], a[40:43], v[56:59], v[200:203]\n"
    "v_mfma_f32_16x16x32_bf16 v[220:223], a[40:43], v[60:63], v[220:223]\n"
    "buffer_load_dwordx4 v[36:39], v3, s[36:39], s90 offen sc1\n"
    "v_mfma_f32_16x16x32_bf16 v[224:227], a[44:47], v[60:63], v[224:227]\n"
    "ds_read_b128 a[24:27], v7 offset:6144\n"
    "v_mfma_f32_16x16x32_bf16 v[204:207], a[44:47], v[56:59], v[204:207]\n"
    "v_mfma_f32_16x16x32_bf16 v[184:187], a[44:47], v[52:55], v[184:187]\n"
    "buffer_load_dwordx4 v[40:43], v4, s[36:39], s90 offen sc1\n"
    "v_mfma_f32_16x16x32_bf16 v[164:167], a[44:47], v[48:51], v[164:167]\n"
    "ds_read_b128 a[28:31], v7 offset:7168\n"
    "v_mfma_f32_16x16x32_bf16 v[168:171], a[48:51], v[48:51], v[168:171]\n"
    "v_mfma_f32_16x16x32_bf16 v[188:191], a[48:51], v[52:55], v[188:191]\n"
    "v_mfma_f32_16x16x32_bf16 v[208:211], a[48:51], v[56:59], v[208:211]\n"
    "buffer_load_dwordx4 v[44:47], v5, s[36:39], s90 offen sc1\n"
    "v_mfma_f32_16x16x32_bf16 v[228:231], a[48:51], v[60:63], v[228:231]\n"
    "ds_read_b128 a[32:35], v7 offset:8192\n"
    "v_mfma_f32_16x16x32_bf16 v[232:235], a[52:55], v[60:63], v[232:235]\n"
    "v_mfma_f32_16x16x32_bf16 v[212:215], a[52:55], v[56:59], v[212:215]\n"
    "v_mfma_f32_16x16x32_bf16 v[192:195], a[52:55], v[52:55], v[192:195]\n"
    "ds_read_b128 a[36:39], v7 offset:9216\n"
    "s_add_u32 s90, s90, 4096\n"
    "v_mfma_f32_16x16x32_bf16 v[172:175], a[52:55], v[48:51], v[172:175]\n"
    "s_cmp_eq_u32 s90, s91\n"
    "v_mfma_f32_16x16x32_bf16 v[176:179], a[56:59], v[48:51], v[176:179]\n"
    "s_cselect_b32 s67, 32768, 0\n"
    "v_mfma_f32_16x16x32_bf16 v[196:199], a[56:59], v[52:55], v[196:199]\n"
    "v_mfma_f32_16x16x32_bf16 v[216:219], a[56:59], v[56:59], v[216:219]\n"
    "s_sub_u32 s90, s90, s67\n"
    "v_mfma_f32_16x16x32_bf16 v[236:239], a[56:59], v[60:63], v[236:239]\n"
    "s_bitcmp1_b32 s52, 1\n"
    "s_cbranch_scc0 L_noof_%=\n"
    "s_lshl_b32 s6, s66, 7\n"
    "s_add_u32 s6, s6, 262144\n"
    "s_waitcnt vmcnt(12)\n"
    "buffer_store_dword v248, v249, s[36:39], s6 offen sc1\n"
    "L_noof_%=:\n"
    "s_waitcnt vmcnt(8)\n"
    "v_mfma_f32_16x16x32_bf16 v[160:163], a[60:63], v[64:67], v[160:163]\n"
    "buffer_load_dword v14, v6, s[36:39], s16 offen sc1\n"
    "v_mfma_f32_16x16x32_bf16 v[180:183], a[60:63], v[68:71], v[180:183]\n"
    "ds_read_b128 a[40:43], v7 offset:10240\n"
    "v_mfma_f32_16x16x32_bf16 v[200:203], a[60:63], v[72:75], v[200:203]\n"
    "buffer_load_dwordx4 v[48:51], v2, s[36:39], s90 offen sc1\n"
    "v_mfma_f32_16x16x32_bf16 v[220:223], a[60:63], v[76:79], v[220:223]\n"
    "v_mfma_f32_16x16x32_bf16 v[224:227], a[64:67], v[76:79], v[224:227]\n"
    "ds_read_b128 a[44:47], v7 offset:11264\n"
    "v_mfma_f32_16x16x32_bf16 v[204:207], a[64:67], v[72:75], v[204:207]\n"
    "buffer_load_dwordx4 v[52:55], v3, s[36:39], s90 offen sc1\n"
    "v_mfma_f32_16x16x32_bf16 v[184:187], a[64:67], v[68:71], v[184:187]\n"
    "v_mfma_f32_16x16x32_bf16 v[164:167], a[64:67], v[64:67], v[164:167]\n"
    "ds_read_b128 a[48:51], v7 offset:12288\n"
    "v_mfma_f32_16x16x32_bf16 v[168:171], a[68:71], v[64:67], v[168:171]\n"
    "buffer_load_dwordx4 v[56:59], v4, s[36:39], s90 offen sc1\n"
    "v_mfma_f32_16x16x32_bf16 v[188:191], a[68:71], v[68:71], v[188:191]\n"
    "v_mfma_f32_16x16x32_bf16 v[208:211], a[68:71], v[72:75], v[208:211]\n"
    "v_mfma_f32_16x16x32_bf16 v[228:231], a[68:71], v[76:79], v[228:231]\n"
    "ds_read_b128 a[52:55], v7 offset:13312\n"
    "v_mfma_f32_16x16x32_bf16 v[232:235], a[72:75], v[76:79], v[232:235]\n"
    "buffer_load_dwordx4 v[60:63], v5, s[36:39], s90 offen sc1\n"
    "v_mfma_f32_16x16x32_bf16 v[212:215], a[72:75], v[72:75], v[212:215]\n"
    "v_mfma_f32_16x16x32_bf16 v[192:195], a[72:75], v[68:71], v[192:195]\n"
    "ds_read_b128 a[56:59], v7 offset:14336\n"
    "v_mfma_f32_16x16x32_bf16 v[172:175], a[72:75], v[64:67], v[172:175]\n"
    "s_add_u32 s90, s90, 4096\n"
    "v_mfma_f32_16x16x32_bf16 v[176:179], a[76:79], v[64:67], v[176:179]\n"
    "s_cmp_eq_u32 s90, s91\n"
    "v_mfma_f32_16x16x32_bf16 v[196:199], a[76:79], v[68:71], v[196:199]\n"
    "s_cselect_b32 s67, 32768, 0\n"
    "v_mfma_f32_16x16x32_bf16 v[216:219], a[76:79], v[72:75], v[216:219]\n"
    "s_sub_u32 s90, s90, s67\n"
    "v_mfma_f32_16x16x32_bf16 v[236:239], a[76:79], v[76:79], v[236:239]\n"
    "s_waitcnt vmcnt(9)\n"
    "v_mfma_f32_16x16x32_bf16 v[160:163], a[80:83], v[16:19], v[160:163]\n"
    "buffer_load_dwordx4 v[64:67], v2, s[36:39], s90 offen sc1\n"
    "v_mfma_f32_16x16x32_bf16 v[180:183], a[80:83], v[20:23], v[180:183]\n"
    "ds_read_b128 a[60:63], v7 offset:15360\n"
    "v_mfma_f32_16x16x32_bf16 v[200:203], a[80:83], v[24:27], v[200:203]\n"
    "v_mfma_f32_16x16x32_bf16 v[220:223], a[80:83], v[28:31], v[220:223]\n"
    "buffer_load_dwordx4 v[68:71], v3, s[36:39], s90 offen sc1\n"
    "v_mfma_f32_16x16x32_bf16 v[224:227], a[84:87], v[28:31], v[224:227]\n"
    "ds_read_b128 a[64:67], v7 offset:16384\n"
    "v_mfma_f32_16x16x32_bf16 v[204:207], a[84:87], v[24:27], v[204:207]\n"
    "v_mfma_f32_16x16x32_bf16 v[184:187], a[84:87], v[20:23], v[184:187]\n"
    "buffer_load_dwordx4 v[72:75], v4, s[36:39], s90 offen sc1\n"
    "v_mfma_f32_16x16x32_bf16 v[164:167], a[84:87], v[16:19], v[164:167]\n"
    "ds_read_b128 a[68:71], v7 offset:17408\n"
    "v_mfma_f32_16x16x32_bf16 v[168:171], a[88:91], v[16:19], v[168:171]\n"
    "v_mfma_f32_16x16x32_bf16 v[188:191], a[88:91], v[20:23], v[188:191]\n"
    "v_mfma_f32_16x16x32_bf16 v[208:211], a[88:91], v[24:27], v[208:211]\n"
    "buffer_load_dwordx4 v[76:79], v5, s[36:39], s90 offen sc1\n"
    "v_mfma_f32_16x16x32_bf16 v[228:231], a[88:91], v[28:31], v[228:231]\n"
    "ds_read_b128 a[72:75], v7 offset:18432\n"
    "v_mfma_f32_16x16x32_bf16 v[232:235], a[92:95], v[28:31], v[232:235]\n"
    "v_mfma_f32_16x16x32_bf16 v[212:215], a[92:95], v[24:27], v[212:215]\n"
    "v_mfma_f32_16x16x32_bf16 v[192:195], a[92:95], v[20:23], v[192:195]\n"
    "ds_read_b128 a[76:79], v7 offset:19456\n"
    "s_lshr_b32 s67, s47, 1\n"
    "v_mfma_f32_16x16x32_bf16 v[172:175], a[92:95], v[16:19], v[172:175]\n"
    "s_lshl_b32 s67, s67, 12\n"
    "v_mfma_f32_16x16x32_bf16 v[176:179], a[96:99], v[16:19], v[176:179]\n"
    "s_add_u32 s90, s91, s67\n"
    "v_mfma_f32_16x16x32_bf16 v[196:199], a[96:99], v[20:23], v[196:199]\n"
    "v_mfma_f32_16x16x32_bf16 v[216:219], a[96:99], v[24:27], v[216:219]\n"
    "s_add_u32 s91, s91, 16384\n"
    "v_mfma_f32_16x16x32_bf16 v[236:239], a[96:99], v[28:31], v[236:239]\n"
    "s_waitcnt vmcnt(9)\n"
    "s_waitcnt vmcnt(8)\n"
    "v_cmp_ne_u32 vcc, s63, v14\n"
    "s_nop 1\n"
    "s_cmp_eq_u64 vcc, 0\n"
    "s_cbranch_scc0 L_hk_slow_%=\n"
    "L_hk_ok_%=:\n"
    "v_mfma_f32_16x16x32_bf16 v[160:163], a[100:103], v[32:35], v[160:163]\n"
    "buffer_load_dwordx4 v[16:19], v2, s[36:39], s90 offen sc1\n"
    "v_mfma_f32_16x16x32_bf16 v[180:183], a[100:103], v[36:39], v[180:183]\n"
    "ds_read_b128 a[80:83], v7 offset:20480\n"
    "v_mfma_f32_16x16x32_bf16 v[200:203], a[100:103], v[40:43], v[200:203]\n"
    "v_mfma_f32_16x16x32_bf16 v[220:223], a[100:103], v[44:47], v[220:223]\n"
    "buffer_load_dwordx4 v[20:23], v3, s[36:39], s90 offen sc1\n"
    "v_mfma_f32_16x16x32_bf16 v[224:227], a[104:107], v[44:47], v[224:227]\n"
    "ds_read_b128 a[84:87], v7 offset:21504\n"
    "v_mfma_f32_16x16x32_bf16 v[204:207], a[104:107], v[40:43], v[204:207]\n"
    "v_mfma_f32_16x16x32_bf16 v[184:187], a[104:107], v[36:39], v[184:187]\n"
    "buffer_load_dwordx4 v[24:27], v4, s[36:39], s90 offen sc1\n"
    "v_mfma_f32_16x16x32_bf16 v[164:167], a[104:107], v[32:35], v[164:167]\n"
    "ds_read_b128 a[88:91], v7 offset:22528\n"
    "v_mfma_f32_16x16x32_bf16 v[168:171], a[108:111], v[32:35], v[168:171]\n"
    "v_mfma_f32_16x16x32_bf16 v[188:191], a[108:111], v[36:39], v[188:191]\n"
    "v_mfma_f32_16x16x32_bf16 v[208:211], a[108:111], v[40:43], v[208:211]\n"
    "buffer_load_dwordx4 v[28:31], v5, s[36:39], s90 offen sc1\n"
    "v_mfma_f32_16x16x32_bf16 v[228:231], a[108:111], v[44:47], v[228:231]\n"
    "ds_read_b128 a[92:95], v7 offset:23552\n"
    "v_mfma_f32_16x16x32_bf16 v[232:235], a[112:115], v[44:47], v[232:235]\n"
    "v_mfma_f32_16x16x32_bf16 v[212:215], a[112:115], v[40:43], v[212:215]\n"
    "v_mfma_f32_16x16x32_bf16 v[192:195], a[112:115], v[36:39], v[192:195]\n"
    "ds_read_b128 a[96:99], v7 offset:24576\n"
    "s_add_u32 s90, s90, 4096\n"
    "v_mfma_f32_16x16x32_bf16 v[172:175], a[112:115], v[32:35], v[172:175]\n"
    "s_cmp_eq_u32 s90, s91\n"
    "v_mfma_f32_16x16x32_bf16 v[176:179], a[116:119], v[32:35], v[176:179]\n"
    "s_cselect_b32 s67, 16384, 0\n"
    "v_mfma_f32_16x16x32_bf16 v[196:199], a[116:119], v[36:39], v[196:199]\n"
    "v_mfma_f32_16x16x32_bf16 v[216:219], a[116:119], v[40:43], v[216:219]\n"
    "s_sub_u32 s90, s90, s67\n"
    "v_mfma_f32_16x16x32_bf16 v[236:239], a[116:119], v[44:47], v[236:239]\n"
    "s_waitcnt vmcnt(8)\n"
    "v_mfma_f32_16x16x32_bf16 v[160:163], a[120:123], v[48:51], v[160:163]\n"
    "buffer_load_dwordx4 v[32:35], v2, s[36:39], s90 offen sc1\n"
    "v_mfma_f32_16x16x32_bf16 v[180:183], a[120:123], v[52:55], v[180:183]\n"
    "v_mfma_f32_16x16x32_bf16 v[200:203], a[120:123], v[56:59], v[200:203]\n"
    "v_mfma_f32_16x16x32_bf16 v[220:223], a[120:123], v[60:63], v[220:223]\n"
    "buffer_load_dwordx4 v[36:39], v3, s[36:39], s90 offen sc1\n"
    "v_mfma_f32_16x16x32_bf16 v[224:227], a[124:127], v[60:63], v[224:227]\n"
    "v_mfma_f32_16x16x32_bf16 v[204:207], a[124:127], v[56:59], v[204:207]\n"
    "v_mfma_f32_16x16x32_bf16 v[184:187], a[124:127], v[52:55], v[184:187]\n"
    "buffer_load_dwordx4 v[40:43], v4, s[36:39], s90 offen sc1\n"
    "v_mfma_f32_16x16x32_bf16 v[164:167], a[124:127], v[48:51], v[164:167]\n"
    "v_mfma_f32_16x16x32_bf16 v[168:171], a[128:131], v[48:51], v[168:171]\n"
    "v_mfma_f32_16x16x32_bf16 v[188:191], a[128:131], v[52:55], v[188:191]\n"
    "v_mfma_f32_16x16x32_bf16 v[208:211], a[128:131], v[56:59], v[208:211]\n"
    "buffer_load_dwordx4 v[44:47], v5, s[36:39], s90 offen sc1\n"
    "v_mfma_f32_16x16x32_bf16 v[228:231], a[128:131], v[60:63], v[228:231]\n"
    "v_mfma_f32_16x16x32_bf16 v[232:235], a[132:135], v[60:63], v[232:235]\n"
    "v_mfma_f32_16x16x32_bf16 v[212:215], a[132:135], v[56:59], v[212:215]\n"
    "v_mfma_f32_16x16x32_bf16 v[192:195], a[132:135], v[52:55], v[192:195]\n"
    "s_add_u32 s90, s90, 4096\n"
    "v_mfma_f32_16x16x32_bf16 v[172:175], a[132:135], v[48:51], v[172:175]\n"
    "s_cmp_eq_u32 s90, s91\n"
    "v_mfma_f32_16x16x32_bf16 v[176:179], a[136:139], v[48:51], v[176:179]\n"
    "s_cselect_b32 s67, 16384, 0\n"
    "v_mfma_f32_16x16x32_bf16 v[196:199], a[136:139], v[52:55], v[196:199]\n"
    "v_mfma_f32_16x16x32_bf16 v[216:219], a[136:139], v[56:59], v[216:219]\n"
    "s_sub_u32 s90, s90, s67\n"
    "v_mfma_f32_16x16x32_bf16 v[236:239], a[136:139], v[60:63], v[236:239]\n"
    "s_waitcnt vmcnt(8)\n"
    "v_mfma_f32_16x16x32_bf16 v[160:163], a[140:143], v[64:67], v[160:163]\n"
    "s_add_u32 s16, s16, 131072\n"
    "v_mfma_f32_16x16x32_bf16 v[180:183], a[140:143], v[68:71], v[180:183]\n"
    "buffer_load_dword v13, v6, s[36:39], s16 offen offset:512 sc1\n"
    "v_mfma_f32_16x16x32_bf16 v[200:203], a[140:143], v[72:75], v[200:203]\n"
    "v_mfma_f32_16x16x32_bf16 v[220:223], a[140:143], v[76:79], v[220:223]\n"
    "buffer_load_dwordx4 v[48:51], v2, s[36:39], s90 offen sc1\n"
    "v_mfma_f32_16x16x32_bf16 v[224:227], a[144:147], v[76:79], v[224:227]\n"
    "v_mfma_f32_16x16x32_bf16 v[204:207], a[144:147], v[72:75], v[204:207]\n"
    "v_mfma_f32_16x16x32_bf16 v[184:187], a[144:147], v[68:71], v[184:187]\n"
    "buffer_load_dwordx4 v[52:55], v3, s[36:39], s90 offen sc1\n"
    "v_mfma_f32_16x16x32_bf16 v[164:167], a[144:147], v[64:67], v[164:167]\n"
    "v_mfma_f32_16x16x32_bf16 v[168:171], a[148:151], v[64:67], v[168:171]\n"
    "v_mfma_f32_16x16x32_bf16 v[188:191], a[148:151], v[68:71], v[188:191]\n"
    "buffer_load_dwordx4 v[56:59], v4, s[36:39], s90 offen sc1\n"
    "v_mfma_f32_16x16x32_bf16 v[208:211], a[148:151], v[72:75], v[208:211]\n"
    "v_mfma_f32_16x16x32_bf16 v[228:231], a[148:151], v[76:79], v[228:231]\n"
    "v_mfma_f32_16x16x32_bf16 v[232:235], a[152:155], v[76:79], v[232:235]\n"
    "buffer_load_dwordx4 v[60:63], v5, s[36:39], s90 offen sc1\n"
    "v_mfma_f32_16x16x32_bf16 v[212:215], a[152:155], v[72:75], v[212:215]\n"
    "v_mfma_f32_16x16x32_bf16 v[192:195], a[152:155], v[68:71], v[192:195]\n"
    "v_mfma_f32_16x16x32_bf16 v[172:175], a[152:155], v[64:67], v[172:175]\n"
    "s_add_u32 s90, s90, 4096\n"
    "v_mfma_f32_16x16x32_bf16 v[176:179], a[156:159], v[64:67], v[176:179]\n"
    "s_cmp_eq_u32 s90, s91\n"
    "v_mfma_f32_16x16x32_bf16 v[196:199], a[156:159], v[68:71], v[196:199]\n"
    "s_cselect_b32 s67, 16384, 0\n"
    "v_mfma_f32_16x16x32_bf16 v[216:219], a[156:159], v[72:75], v[216:219]\n"
    "s_sub_u32 s90, s90, s67\n"
    "v_mfma_f32_16x16x32_bf16 v[236:239], a[156:159], v[76:79], v[236:239]\n"
    "s_waitcnt vmcnt(9)\n"
    "v_mfma_f32_16x16x32_bf16 v[180:183], a[160:163], v[20:23], v[180:183]\n"
    "buffer_load_dword v12, v6, s[36:39], s16 offen offset:512 sc1\n"
    "v_mfma_f32_16x16x32_bf16 v[200:203], a[160:163], v[24:27], v[200:203]\n"
    "v_mfma_f32_16x16x32_bf16 v[220:223], a[160:163], v[28:31], v[220:223]\n"
    "buffer_load_dwordx4 v[64:67], v2, s[36:39], s90 offen sc1\n"
    "v_mfma_f32_16x16x32_bf16 v[224:227], a[164:167], v[28:31], v[224:227]\n"
    "v_mfma_f32_16x16x32_bf16 v[204:207], a[164:167], v[24:27], v[204:207]\n"
    "v_mfma_f32_16x16x32_bf16 v[184:187], a[164:167], v[20:23], v[184:187]\n"
    "buffer_load_dwordx4 v[68:71], v3, s[36:39], s90 offen sc1\n"
    "v_mfma_f32_16x16x32_bf16 v[188:191], a[168:171], v[20:23], v[188:191]\n"
    "v_mfma_f32_16x16x32_bf16 v[208:211], a[168:171], v[24:27], v[208:211]\n"
    "v_mfma_f32_16x16x32_bf16 v[228:231], a[168:171], v[28:31], v[228:231]\n"
    "buffer_load_dwordx4 v[72:75], v4, s[36:39], s90 offen sc1\n"
    "v_mfma_f32_16x16x32_bf16 v[232:235], a[172:175], v[28:31], v[232:235]\n"
    "v_mfma_f32_16x16x32_bf16 v[212:215], a[172:175], v[24:27], v[212:215]\n"
    "v_mfma_f32_16x16x32_bf16 v[192:195], a[172:175], v[20:23], v[192:195]\n"
    "buffer_load_dwordx4 v[76:79], v5, s[36:39], s90 offen sc1\n"
    "v_mfma_f32_16x16x32_bf16 v[196:199], a[176:179], v[20:23], v[196:199]\n"
    "v_mfma_f32_16x16x32_bf16 v[216:219], a[176:179], v[24:27], v[216:219]\n"
    "v_mfma_f32_16x16x32_bf16 v[236:239], a[176:179], v[28:31], v[236:239]\n"
    "s_waitcnt vmcnt(9)\n"
    "v_cmp_ne_u32 vcc, s63, v13\n"
    "s_nop 1\n"
    "s_cmp_eq_u64 vcc, 0\n"
    "s_cbranch_scc0 L_ofn9_%=\n"
    "v_mfma_f32_16x16x32_bf16 v[180:183], a[180:183], v[36:39], v[180:183]\n"
    "buffer_load_dwordx4 v[80:83], v1, s[36:39], s9 offen sc1\n"
    "v_mfma_f32_16x16x32_bf16 v[200:203], a[180:183], v[40:43], v[200:203]\n"
    "v_mfma_f32_16x16x32_bf16 v[220:223], a[180:183], v[44:47], v[220:223]\n"
    "v_mfma_f32_16x16x32_bf16 v[224:227], a[184:187], v[44:47], v[224:227]\n"
    "buffer_load_dwordx4 v[84:87], v1, s[36:39], s9 offen offset:1024 sc1\n"
    "v_mfma_f32_16x16x32_bf16 v[204:207], a[184:187], v[40:43], v[204:207]\n"
    "v_mfma_f32_16x16x32_bf16 v[184:187], a[184:187], v[36:39], v[184:187]\n"
    "v_mfma_f32_16x16x32_bf16 v[188:191], a[188:191], v[36:39], v[188:191]\n"
    "buffer_load_dwordx4 v[88:91], v1, s[36:39], s9 offen offset:2048 sc1\n"
    "v_mfma_f32_16x16x32_bf16 v[208:211], a[188:191], v[40:43], v[208:211]\n"
    "v_mfma_f32_16x16x32_bf16 v[228:231], a[188:191], v[44:47], v[228:231]\n"
    "v_mfma_f32_16x16x32_bf16 v[232:235], a[192:195], v[44:47], v[232:235]\n"
    "v_mfma_f32_16x16x32_bf16 v[212:215], a[192:195], v[40:43], v[212:215]\n"
    "buffer_load_dwordx4 v[92:95], v1, s[36:39], s9 offen offset:3072 sc1\n"
    "v_mfma_f32_16x16x32_bf16 v[192:195], a[192:195], v[36:39], v[192:195]\n"
    "v_mfma_f32_16x16x32_bf16 v[196:199], a[196:199], v[36:39], v[196:199]\n"
    "v_mfma_f32_16x16x32_bf16 v[216:219], a[196:199], v[40:43], v[216:219]\n"
    "v_mfma_f32_16x16x32_bf16 v[236:239], a[196:199], v[44:47], v[236:239]\n"
    "s_waitcnt vmcnt(9)\n"
    "v_mfma_f32_16x16x32_bf16 v[180:183], a[200:203], v[52:55], v[180:183]\n"
    "buffer_load_dwordx2 v[96:97], v11, s[36:39], s9 offen sc1\n"
    "v_mfma_f32_16x16x32_bf16 v[200:203], a[200:203], v[56:59], v[200:203]\n"
    "v_mfma_f32_16x16x32_bf16 v[220:223], a[200:203], v[60:63], v[220:223]\n"
    "v_mfma_f32_16x16x32_bf16 v[224:227], a[204:207], v[60:63], v[224:227]\n"
    "buffer_load_dwordx2 v[98:99], v11, s[36:39], s9 offen offset:512 sc1\n"
    "v_mfma_f32_16x16x32_bf16 v[204:207], a[204:207], v[56:59], v[204:207]\n"
    "v_mfma_f32_16x16x32_bf16 v[184:187], a[204:207], v[52:55], v[184:187]\n"
    "v_mfma_f32_16x16x32_bf16 v[188:191], a[208:211], v[52:55], v[188:191]\n"
    "buffer_load_dwordx4 v[100:103], v1, s[36:39], s10 offen sc1\n"
    "v_mfma_f32_16x16x32_bf16 v[208:211], a[208:211], v[56:59], v[208:211]\n"
    "v_mfma_f32_16x16x32_bf16 v[228:231], a[208:211], v[60:63], v[228:231]\n"
    "v_mfma_f32_16x16x32_bf16 v[232:235], a[212:215], v[60:63], v[232:235]\n"
    "v_mfma_f32_16x16x32_bf16 v[212:215], a[212:215], v[56:59], v[212:215]\n"
    "buffer_load_dwordx4 v[104:107], v1, s[36:39], s10 offen offset:1024 sc1\n"
    "v_mfma_f32_16x16x32_bf16 v[192:195], a[212:215], v[52:55], v[192:195]\n"
    "v_mfma_f32_16x16x32_bf16 v[196:199], a[216:219], v[52:55], v[196:199]\n"
    "v_mfma_f32_16x16x32_bf16 v[216:219], a[216:219], v[56:59], v[216:219]\n"
    "v_mfma_f32_16x16x32_bf16 v[236:239], a[216:219], v[60:63], v[236:239]\n"
    "s_waitcnt vmcnt(8)\n"
    "v_mfma_f32_16x16x32_bf16 v[180:183], a[220:223], v[68:71], v[180:183]\n"
    "buffer_load_dwordx4 v[108:111], v1, s[36:39], s10 offen offset:2048 sc1\n"
    "v_mfma_f32_16x16x32_bf16 v[200:203], a[220:223], v[72:75], v[200:203]\n"
    "v_mfma_f32_16x16x32_bf16 v[220:223], a[220:223], v[76:79], v[220:223]\n"
    "v_mfma_f32_16x16x32_bf16 v[224:227], a[224:227], v[76:79], v[224:227]\n"
    "buffer_load_dwordx4 v[112:115], v1, s[36:39], s10 offen offset:3072 sc1\n"
    "v_mfma_f32_16x16x32_bf16 v[204:207], a[224:227], v[72:75], v[204:207]\n"
    "v_mfma_f32_16x16x32_bf16 v[184:187], a[224:227], v[68:71], v[184:187]\n"
    "v_mfma_f32_16x16x32_bf16 v[188:191], a[228:231], v[68:71], v[188:191]\n"
    "buffer_load_dwordx2 v[116:117], v11, s[36:39], s10 offen sc1\n"
    "v_mfma_f32_16x16x32_bf16 v[208:211], a[228:231], v[72:75], v[208:211]\n"
    "v_mfma_f32_16x16x32_bf16 v[228:231], a[228:231], v[76:79], v[228:231]\n"
    "v_mfma_f32_16x16x32_bf16 v[232:235], a[232:235], v[76:79], v[232:235]\n"
    "v_mfma_f32_16x16x32_bf16 v[212:215], a[232:235], v[72:75], v[212:215]\n"
    "buffer_load_dwordx2 v[118:119], v11, s[36:39], s10 offen offset:512 sc1\n"
    "v_mfma_f32_16x16x32_bf16 v[192:195], a[232:235], v[68:71], v[192:195]\n"
    "v_mfma_f32_16x16x32_bf16 v[196:199], a[236:239], v[68:71], v[196:199]\n"
    "v_mfma_f32_16x16x32_bf16 v[216:219], a[236:239], v[72:75], v[216:219]\n"
    "v_mfma_f32_16x16x32_bf16 v[236:239], a[236:239], v[76:79], v[236:239]\n"
    "L_opart_%=:\n"
    "s_waitcnt lgkmcnt(0)\n"
    "s_waitcnt vmcnt(11)\n"
    "v_mfma_f32_16x16x32_bf16 v[180:183], a[0:3], v[80:83], v[180:183]\n"
    "buffer_load_dwordx4 v[120:123], v1, s[36:39], s11 offen sc1\n"
    "v_mfma_f32_16x16x32_bf16 v[184:187], a[4:7], v[80:83], v[184:187]\n"
    "v_mfma_f32_16x16x32_bf16 v[188:191], a[8:11], v[80:83], v[188:191]\n"
    "v_mfma_f32_16x16x32_bf16 v[192:195], a[12:15], v[80:83], v[192:195]\n"
    "buffer_load_dwordx4 v[124:127], v1, s[36:39], s11 offen offset:1024 sc1\n"
    "v_mfma_f32_16x16x32_bf16 v[196:199], a[16:19], v[80:83], v[196:199]\n"
    "s_waitcnt vmcnt(12)\n"
    "v_mfma_f32_16x16x32_bf16 v[180:183], a[20:23], v[84:87], v[180:183]\n"
    "v_mfma_f32_16x16x32_bf16 v[184:187], a[24:27], v[84:87], v[184:187]\n"
    "buffer_load_dwordx4 v[128:131], v1, s[36:39], s11 offen offset:2048 sc1\n"
    "v_mfma_f32_16x16x32_bf16 v[188:191], a[28:31], v[84:87], v[188:191]\n"
    "v_mfma_f32_16x16x32_bf16 v[192:195], a[32:35], v[84:87], v[192:195]\n"
    "v_mfma_f32_16x16x32_bf16 v[196:199], a[36:39], v[84:87], v[196:199]\n"
    "buffer_load_dwordx4 v[132:135], v1, s[36:39], s11 offen offset:3072 sc1\n"
    "s_waitcnt vmcnt(13)\n"
    "v_mfma_f32_16x16x32_bf16 v[180:183], a[40:43], v[88:91], v[180:183]\n"
    "v_mfma_f32_16x16x32_bf16 v[184:187], a[44:47], v[88:91], v[184:187]\n"
    "v_mfma_f32_16x16x32_bf16 v[188:191], a[48:51], v[88:91], v[188:191]\n"
    "buffer_load_dwordx2 v[136:137], v11, s[36:39], s11 offen sc1\n"
    "v_mfma_f32_16x16x32_bf16 v[192:195], a[52:55], v[88:91], v[192:195]\n"
    "v_mfma_f32_16x16x32_bf16 v[196:199], a[56:59], v[88:91], v[196:199]\n"
    "s_waitcnt vmcnt(13)\n"
    "v_mfma_f32_16x16x32_bf16 v[180:183], a[60:63], v[92:95], v[180:183]\n"
    "buffer_load_dwordx2 v[138:139], v11, s[36:39], s11 offen offset:512 sc1\n"
    "v_mfma_f32_16x16x32_bf16 v[184:187], a[64:67], v[92:95], v[184:187]\n"
    "v_mfma_f32_16x16x32_bf16 v[188:191], a[68:71], v[92:95], v[188:191]\n"
    "v_mfma_f32_16x16x32_bf16 v[192:195], a[72:75], v[92:95], v[192:195]\n"
    "v_mfma_f32_16x16x32_bf16 v[196:199], a[76:79], v[92:95], v[196:199]\n"
    "s_waitcnt vmcnt(12)\n"
    "v_mfma_f32_16x16x32_bf16 v[180:183], a[80:83], v[96:99], v[180:183]\n"
    "v_mfma_f32_16x16x32_bf16 v[184:187], a[84:87], v[96:99], v[184:187]\n"
    "v_mfma_f32_16x16x32_bf16 v[188:191], a[88:91], v[96:99], v[188:191]\n"
    "v_mfma_f32_16x16x32_bf16 v[192:195], a[92:95], v[96:99], v[192:195]\n"
    "v_mfma_f32_16x16x32_bf16 v[196:199], a[96:99], v[96:99], v[196:199]\n"
    "s_waitcnt vmcnt(11)\n"
    "v_mfma_f32_16x16x32_bf16 v[200:203], a[0:3], v[100:103], v[200:203]\n"
    "buffer_load_dwordx4 v[140:143], v1, s[36:39], s8 offen sc1\n"
    "v_mfma_f32_16x16x32_bf16 v[204:207], a[4:7], v[100:103], v[204:207]\n"
    "v_mfma_f32_16x16x32_bf16 v[208:211], a[8:11], v[100:103], v[208:211]\n"
    "ds_write_b128 v7, v[180:183] offset:0\n"
    "v_mfma_f32_16x16x32_bf16 v[212:215], a[12:15], v[100:103], v[212:215]\n"
    "buffer_load_dwordx4 v[144:147], v1, s[36:39], s8 offen offset:1024 sc1\n"
    "v_mfma_f32_16x16x32_bf16 v[216:219], a[16:19], v[100:103], v[216:219]\n"
    "s_waitcnt vmcnt(12)\n"
    "v_mfma_f32_16x16x32_bf16 v[200:203], a[20:23], v[104:107], v[200:203]\n"
    "v_mfma_f32_16x16x32_bf16 v[204:207], a[24:27], v[104:107], v[204:207]\n"
    "ds_write_b128 v7, v[184:187] offset:1024\n"
    "v_mfma_f32_16x16x32_bf16 v[208:211], a[28:31], v[104:107], v[208:211]\n"
    "buffer_load_dwordx4 v[148:151], v1, s[36:39], s8 offen offset:2048 sc1\n"
    "v_mfma_f32_16x16x32_bf16 v[212:215], a[32:35], v[104:107], v[212:215]\n"
    "v_mfma_f32_16x16x32_bf16 v[216:219], a[36:39], v[104:107], v[216:219]\n"
    "s_waitcnt vmcnt(12)\n"
    "v_mfma_f32_16x16x32_bf16 v[200:203], a[40:43], v[108:111], v[200:203]\n"
    "ds_write_b128 v7, v[188:191] offset:2048\n"
    "v_mfma_f32_16x16x32_bf16 v[204:207], a[44:47], v[108:111], v[204:207]\n"
    "buffer_load_dwordx4 v[152:155], v1, s[36:39], s8 offen offset:3072 sc1\n"
    "v_mfma_f32_16x16x32_bf16 v[208:211], a[48:51], v[108:111], v[208:211]\n"
    "v_mfma_f32_16x16x32_bf16 v[212:215], a[52:55], v[108:111], v[212:215]\n"
    "v_mfma_f32_16x16x32_bf16 v[216:219], a[56:59], v[108:111], v[216:219]\n"
    "buffer_load_dwordx2 v[156:157], v11, s[36:39], s8 offen sc1\n"
    "s_waitcnt vmcnt(13)\n"
    "v_mfma_f32_16x16x32_bf16 v[200:203], a[60:63], v[112:115], v[200:203]\n"
    "ds_write_b128 v7, v[192:195] offset:3072\n"
    "v_mfma_f32_16x16x32_bf16 v[204:207], a[64:67], v[112:115], v[204:207]\n"
    "v_mfma_f32_16x16x32_bf16 v[208:211], a[68:71], v[112:115], v[208:211]\n"
    "buffer_load_dwordx2 v[158:159], v11, s[36:39], s8 offen offset:512 sc1\n"
    "v_mfma_f32_16x16x32_bf16 v[212:215], a[72:75], v[112:115], v[212:215]\n"
    "v_mfma_f32_16x16x32_bf16 v[216:219], a[76:79], v[112:115], v[216:219]\n"
    "ds_write_b128 v7, v[196:199] offset:4096\n"
    "s_waitcnt vmcnt(12)\n"
    "v_mfma_f32_16x16x32_bf16 v[200:203], a[80:83], v[116:119], v[200:203]\n"
    "v_mfma_f32_16x16x32_bf16 v[204:207], a[84:87], v[116:119], v[204:207]\n"
    "v_mfma_f32_16x16x32_bf16 v[208:211], a[88:91], v[116:119], v[208:211]\n"
    "v_mfma_f32_16x16x32_bf16 v[212:215], a[92:95], v[116:119], v[212:215]\n"
    "v_mfma_f32_16x16x32_bf16 v[216:219], a[96:99], v[116:119], v[216:219]\n"
    "s_waitcnt vmcnt(11)\n"
    "v_mfma_f32_16x16x32_bf16 v[220:223], a[0:3], v[120:123], v[220:223]\n"
    "v_mfma_f32_16x16x32_bf16 v[224:227], a[4:7], v[120:123], v[224:227]\n"
    "v_mfma_f32_16x16x32_bf16 v[228:231], a[8:11], v[120:123], v[228:231]\n"
    "ds_write_b128 v7, v[200:203] offset:5120\n"
    "v_mfma_f32_16x16x32_bf16 v[232:235], a[12:15], v[120:123], v[232:235]\n"
    "v_mfma_f32_16x16x32_bf16 v[236:239], a[16:19], v[120:123], v[236:239]\n"
    "s_waitcnt vmcnt(10)\n"
    "v_mfma_f32_16x16x32_bf16 v[220:223], a[20:23], v[124:127], v[220:223]\n"
    "v_mfma_f32_16x16x32_bf16 v[224:227], a[24:27], v[124:127], v[224:227]\n"
    "ds_write_b128 v7, v[204:207] offset:6144\n"
    "v_mfma_f32_16x16x32_bf16 v[228:231], a[28:31], v[124:127], v[228:231]\n"
    "v_mfma_f32_16x16x32_bf16 v[232:235], a[32:35], v[124:127], v[232:235]\n"
    "v_mfma_f32_16x16x32_bf16 v[236:239], a[36:39], v[124:127], v[236:239]\n"
    "s_waitcnt vmcnt(9)\n"
    "v_mfma_f32_16x16x32_bf16 v[220:223], a[40:43], v[128:131], v[220:223]\n"
    "ds_write_b128 v7, v[208:211] offset:7168\n"
    "v_mfma_f32_16x16x32_bf16 v[224:227], a[44:47], v[128:131], v[224:227]\n"
    "v_mfma_f32_16x16x32_bf16 v[228:231], a[48:51], v[128:131], v[228:231]\n"
    "v_mfma_f32_16x16x32_bf16 v[232:235], a[52:55], v[128:131], v[232:235]\n"
    "v_mfma_f32_16x16x32_bf16 v[236:239], a[56:59], v[128:131], v[236:239]\n"
    "s_waitcnt vmcnt(8)\n"
    "v_mfma_f32_16x16x32_bf16 v[220:223], a[60:63], v[132:135], v[220:223]\n"
    "ds_write_b128 v7, v[212:215] offset:8192\n"
    "v_mfma_f32_16x16x32_bf16 v[224:227], a[64:67], v[132:135], v[224:227]\n"
    "v_mfma_f32_16x16x32_bf16 v[228:231], a[68:71], v[132:135], v[228:231]\n"
    "v_mfma_f32_16x16x32_bf16 v[232:235], a[72:75], v[132:135], v[232:235]\n"
    "v_mfma_f32_16x16x32_bf16 v[236:239], a[76:79], v[132:135], v[236:239]\n"
    "ds_write_b128 v7, v[216:219] offset:9216\n"
    "s_waitcnt vmcnt(6)\n"
    "v_mfma_f32_16x16x32_bf16 v[220:223], a[80:83], v[136:139], v[220:223]\n"
    "v_mfma_f32_16x16x32_bf16 v[224:227], a[84:87], v[136:139], v[224:227]\n"
    "v_mfma_f32_16x16x32_bf16 v[228:231], a[88:91], v[136:139], v[228:231]\n"
    "v_mfma_f32_16x16x32_bf16 v[232:235], a[92:95], v[136:139], v[232:235]\n"
    "v_mfma_f32_16x16x32_bf16 v[236:239], a[96:99], v[136:139], v[236:239]\n"
    "v_mfma_f32_16x16x32_bf16 v[160:163], a[160:163], v[16:19], v[160:163]\n"
    "v_mfma_f32_16x16x32_bf16 v[164:167], a[164:167], v[16:19], v[164:167]\n"
    "v_mfma_f32_16x16x32_bf16 v[168:171], a[168:171], v[16:19], v[168:171]\n"
    "s_nop 0\n"
    "ds_write_b128 v7, v[220:223] offset:10240\n"
    "v_mfma_f32_16x16x32_bf16 v[172:175], a[172:175], v[16:19], v[172:175]\n"
    "v_mfma_f32_16x16x32_bf16 v[176:179], a[176:179], v[16:19], v[176:179]\n"
    "v_mfma_f32_16x16x32_bf16 v[160:163], a[180:183], v[32:35], v[160:163]\n"
    "ds_write_b128 v7, v[224:227] offset:11264\n"
    "v_mfma_f32_16x16x32_bf16 v[164:167], a[184:187], v[32:35], v[164:167]\n"
    "v_mfma_f32_16x16x32_bf16 v[168:171], a[188:191], v[32:35], v[168:171]\n"
    "v_mfma_f32_16x16x32_bf16 v[172:175], a[192:195], v[32:35], v[172:175]\n"
    "ds_write_b128 v7, v[228:231] offset:12288\n"
    "v_mfma_f32_16x16x32_bf16 v[176:179], a[196:199], v[32:35], v[176:179]\n"
    "v_mfma_f32_16x16x32_bf16 v[160:163], a[200:203], v[48:51], v[160:163]\n"
    "v_mfma_f32_16x16x32_bf16 v[164:167], a[204:207], v[48:51], v[164:167]\n"
    "v_mfma_f32_16x16x32_bf16 v[168:171], a[208:211], v[48:51], v[168:171]\n"
    "ds_write_b128 v7, v[232:235] offset:13312\n"
    "v_mfma_f32_16x16x32_bf16 v[172:175], a[212:215], v[48:51], v[172:175]\n"
    "v_mfma_f32_16x16x32_bf16 v[176:179], a[216:219], v[48:51], v[176:179]\n"
    "v_mfma_f32_16x16x32_bf16 v[160:163], a[220:223], v[64:67], v[160:163]\n"
    "ds_write_b128 v7, v[236:239] offset:14336\n"
    "v_mfma_f32_16x16x32_bf16 v[164:167], a[224:227], v[64:67], v[164:167]\n"
    "v_mfma_f32_16x16x32_bf16 v[168:171], a[228:231], v[64:67], v[168:171]\n"
    "v_mfma_f32_16x16x32_bf16 v[172:175], a[232:235], v[64:67], v[172:175]\n"
    "v_mfma_f32_16x16x32_bf16 v[176:179], a[236:239], v[64:67], v[176:179]\n"
    "s_waitcnt vmcnt(5)\n"
    "v_mfma_f32_16x16x32_bf16 v[160:163], a[0:3], v[140:143], v[160:163]\n"
    "s_waitcnt lgkmcnt(0)\n"
    "s_barrier\n"
    "v_mfma_f32_16x16x32_bf16 v[164:167], a[4:7], v[140:143], v[164:167]\n"
    "ds_read_b128 v[80:83], v8 offset:0\n"
    "v_mfma_f32_16x16x32_bf16 v[168:171], a[8:11], v[140:143], v[168:171]\n"
    "ds_read_b128 v[84:87], v9 offset:0\n"
    "v_mfma_f32_16x16x32_bf16 v[172:175], a[12:15], v[140:143], v[172:175]\n"
    "ds_read_b128 v[88:91], v10 offset:0\n"
    "v_mfma_f32_16x16x32_bf16 v[176:179], a[16:19], v[140:143], v[176:179]\n"
    "s_waitcnt vmcnt(4)\n"
    "ds_read_b128 v[92:95], v8 offset:1024\n"
    "v_mfma_f32_16x16x32_bf16 v[160:163], a[20:23], v[144:147], v[160:163]\n"
    "ds_read_b128 v[96:99], v9 offset:1024\n"
    "v_mfma_f32_16x16x32_bf16 v[164:167], a[24:27], v[144:147], v[164:167]\n"
    "ds_read_b128 v[100:103], v10 offset:1024\n"
    "v_mfma_f32_16x16x32_bf16 v[168:171], a[28:31], v[144:147], v[168:171]\n"
    "ds_read_b128 v[104:107], v8 offset:2048\n"
    "v_mfma_f32_16x16x32_bf16 v[172:175], a[32:35], v[144:147], v[172:175]\n"
    "ds_read_b128 v[108:111], v9 offset:2048\n"
    "v_mfma_f32_16x16x32_bf16 v[176:179], a[36:39], v[144:147], v[176:179]\n"
    "s_waitcnt vmcnt(3)\n"
    "ds_read_b128 v[112:115], v10 offset:2048\n"
    "v_mfma_f32_16x16x32_bf16 v[160:163], a[40:43], v[148:151], v[160:163]\n"
    "ds_read_b128 v[116:119], v8 offset:3072\n"
    "v_mfma_f32_16x16x32_bf16 v[164:167], a[44:47], v[148:151], v[164:167]\n"
    "ds_read_b128 v[120:123], v9 offset:3072\n"
    "v_mfma_f32_16x16x32_bf16 v[168:171], a[48:51], v[148:151], v[168:171]\n"
    "ds_read_b128 v[124:127], v10 offset:3072\n"
    "v_mfma_f32_16x16x32_bf16 v[172:175], a[52:55], v[148:151], v[172:175]\n"
    "ds_read_b128 v[128:131], v8 offset:4096\n"
    "v_mfma_f32_16x16x32_bf16 v[176:179], a[56:59], v[148:151], v[176:179]\n"
    "s_waitcnt vmcnt(2)\n"
    "ds_read_b128 v[132:135], v9 offset:4096\n"
    "v_mfma_f32_16x16x32_bf16 v[160:163], a[60:63], v[152:155], v[160:163]\n"
    "ds_read_b128 v[136:139], v10 offset:4096\n"
    "v_mfma_f32_16x16x32_bf16 v[164:167], a[64:67], v[152:155], v[164:167]\n"
    "s_waitcnt lgkmcnt(9)\n"
    "v_mfma_f32_16x16x32_bf16 v[168:171], a[68:71], v[152:155], v[168:171]\n"
    "v_add_f32_e64 v80, v80, v84\n"
    "v_add_f32_e64 v81, v81, v85\n"
    "v_mfma_f32_16x16x32_bf16 v[172:175], a[72:75], v[152:155], v[172:175]\n"
    "v_add_f32_e64 v82, v82, v86\n"
    "v_add_f32_e64 v83, v83, v87\n"
    "v_mfma_f32_16x16x32_bf16 v[176:179], a[76:79], v[152:155], v[176:179]\n"
    "s_waitcnt vmcnt(0)\n"
    "v_add_f32_e64 v80, v80, v88\n"
    "v_add_f32_e64 v81, v81, v89\n"
    "v_mfma_f32_16x16x32_bf16 v[160:163], a[80:83], v[156:159], v[160:163]\n"
    "v_add_f32_e64 v82, v82, v90\n"
    "v_add_f32_e64 v83, v83, v91\n"
    "v_mfma_f32_16x16x32_bf16 v[164:167], a[84:87], v[156:159], v[164:167]\n"
    "v_add_f32_e64 v92, v92, v96\n"
    "v_add_f32_e64 v93, v93, v97\n"
    "v_mfma_f32_16x16x32_bf16 v[168:171], a[88:91], v[156:159], v[168:171]\n"
    "v_add_f32_e64 v94, v94, v98\n"
    "v_add_f32_e64 v95, v95, v99\n"
    "v_mfma_f32_16x16x32_bf16 v[172:175], a[92:95], v[156:159], v[172:175]\n"
    "v_add_f32_e64 v92, v92, v100\n"
    "v_add_f32_e64 v93, v93, v101\n"
    "v_mfma_f32_16x16x32_bf16 v[176:179], a[96:99], v[156:159], v[176:179]\n"
    "v_add_f32_e64 v94, v94, v102\n"
    "v_add_f32_e64 v95, v95, v103\n"
    "s_waitcnt lgkmcnt(6)\n"
    "s_nop 0\n"
    "v_pk_add_f32 v[104:105], v[104:105], v[108:109]\n"
    "v_pk_add_f32 v[106:107], v[106:107], v[110:111]\n"
    "v_pk_add_f32 v[104:105], v[104:105], v[112:113]\n"
    "v_pk_add_f32 v[106:107], v[106:107], v[114:115]\n"
    "s_waitcnt lgkmcnt(3)\n"
    "s_nop 0\n"
    "v_pk_add_f32 v[116:117], v[116:117], v[120:121]\n"
    "v_pk_add_f32 v[118:119], v[118:119], v[122:123]\n"
    "v_pk_add_f32 v[116:117], v[116:117], v[124:125]\n"
    "v_pk_add_f32 v[118:119], v[118:119], v[126:127]\n"
    "s_waitcnt lgkmcnt(0)\n"
    "s_nop 0\n"
    "v_pk_add_f32 v[128:129], v[128:129], v[132:133]\n"
    "v_pk_add_f32 v[130:131], v[130:131], v[134:135]\n"
    "v_pk_add_f32 v[128:129], v[128:129], v[136:137]\n"
    "v_pk_add_f32 v[130:131], v[130:131], v[138:139]\n"
    "v_pk_add_f32 v[160:161], v[160:161], v[80:81]\n"
    "v_pk_add_f32 v[162:163], v[162:163], v[82:83]\n"
    "v_cvt_pk_bf16_f32 v20, v160, v161\n"
    "v_cvt_pk_bf16_f32 v21, v162, v163\n"
    "ds_write_b64 v240, v[20:21] offset:0\n"
    "v_pk_add_f32 v[164:165], v[164:165], v[92:93]\n"
    "v_pk_add_f32 v[166:167], v[166:167], v[94:95]\n"
    "v_cvt_pk_bf16_f32 v22, v164, v165\n"
    "v_cvt_pk_bf16_f32 v23, v166, v167\n"
    "ds_write_b64 v240, v[22:23] offset:32\n"
    "v_pk_add_f32 v[168:169], v[168:169], v[104:105]\n"
    "v_pk_add_f32 v[170:171], v[170:171], v[106:107]\n"
    "v_cvt_pk_bf16_f32 v24, v168, v169\n"
    "v_cvt_pk_bf16_f32 v25, v170, v171\n"
    "ds_write_b64 v240, v[24:25] offset:64\n"
    "v_pk_add_f32 v[172:173], v[172:173], v[116:117]\n"
    "v_pk_add_f32 v[174:175], v[174:175], v[118:119]\n"
    "v_cvt_pk_bf16_f32 v26, v172, v173\n"
    "v_cvt_pk_bf16_f32 v27, v174, v175\n"
    "ds_write_b64 v240, v[26:27] offset:96\n"
    "v_pk_add_f32 v[176:177], v[176:177], v[128:129]\n"
    "v_pk_add_f32 v[178:179], v[178:179], v[130:131]\n"
    "v_cvt_pk_bf16_f32 v28, v176, v177\n"
    "v_cvt_pk_bf16_f32 v29, v178, v179\n"
    "ds_write_b64 v240, v[28:29] offset:128\n"
    "ds_read_b128 v[96:99], v241\n"
    "ds_read_b128 v[100:103], v242\n"
    "ds_read_b128 v[104:107], v243\n"
    "s_waitcnt lgkmcnt(2)\n"
    "buffer_store_dwordx4 v[96:99], v244, s[40:43], s46 offen\n"
    "s_waitcnt lgkmcnt(1)\n"
    "buffer_store_dwordx4 v[100:103], v245, s[40:43], s46 offen\n"
    "s_waitcnt lgkmcnt(0)\n"
    "buffer_store_dwordx4 v[104:107], v246, s[40:43], s46 offen\n"
    "s_endpgm\n"
    "L_ofn9_%=:\n"
    "v_mfma_f32_16x16x32_bf16 v[180:183], a[180:183], v[36:39], v[180:183]\n"
    "buffer_load_dword v13, v6, s[36:39], s16 offen offset:512 sc1\n"
    "v_mfma_f32_16x16x32_bf16 v[200:203], a[180:183], v[40:43], v[200:203]\n"
    "v_mfma_f32_16x16x32_bf16 v[220:223], a[180:183], v[44:47], v[220:223]\n"
    "v_mfma_f32_16x16x32_bf16 v[224:227], a[184:187], v[44:47], v[224:227]\n"
    "v_mfma_f32_16x16x32_bf16 v[204:207], a[184:187], v[40:43], v[204:207]\n"
    "v_mfma_f32_16x16x32_bf16 v[184:187], a[184:187], v[36:39], v[184:187]\n"
    "v_mfma_f32_16x16x32_bf16 v[188:191], a[188:191], v[36:39], v[188:191]\n"
    "v_mfma_f32_16x16x32_bf16 v[208:211], a[188:191], v[40:43], v[208:211]\n"
    "v_mfma_f32_16x16x32_bf16 v[228:231], a[188:191], v[44:47], v[228:231]\n"
    "v_mfma_f32_16x16x32_bf16 v[232:235], a[192:195], v[44:47], v[232:235]\n"
    "v_mfma_f32_16x16x32_bf16 v[212:215], a[192:195], v[40:43], v[212:215]\n"
    "v_mfma_f32_16x16x32_bf16 v[192:195], a[192:195], v[36:39], v[192:195]\n"
    "v_mfma_f32_16x16x32_bf16 v[196:199], a[196:199], v[36:39], v[196:199]\n"
    "v_mfma_f32_16x16x32_bf16 v[216:219], a[196:199], v[40:43], v[216:219]\n"
    "v_mfma_f32_16x16x32_bf16 v[236:239], a[196:199], v[44:47], v[236:239]\n"
    "s_waitcnt vmcnt(5)\n"
    "v_cmp_ne_u32 vcc, s63, v12\n"
    "s_nop 1\n"
    "s_cmp_eq_u64 vcc, 0\n"
    "s_cbranch_scc0 L_ofn10_%=\n"
    "v_mfma_f32_16x16x32_bf16 v[180:183], a[200:203], v[52:55], v[180:183]\n"
    "buffer_load_dwordx4 v[80:83], v1, s[36:39], s9 offen sc1\n"
    "v_mfma_f32_16x16x32_bf16 v[200:203], a[200:203], v[56:59], v[200:203]\n"
    "buffer_load_dwordx4 v[84:87], v1, s[36:39], s9 offen offset:1024 sc1\n"
    "v_mfma_f32_16x16x32_bf16 v[220:223], a[200:203], v[60:63], v[220:223]\n"
    "v_mfma_f32_16x16x32_bf16 v[224:227], a[204:207], v[60:63], v[224:227]\n"
    "v_mfma_f32_16x16x32_bf16 v[204:207], a[204:207], v[56:59], v[204:207]\n"
    "buffer_load_dwordx4 v[88:91], v1, s[36:39], s9 offen offset:2048 sc1\n"
    "v_mfma_f32_16x16x32_bf16 v[184:187], a[204:207], v[52:55], v[184:187]\n"
    "v_mfma_f32_16x16x32_bf16 v[188:191], a[208:211], v[52:55], v[188:191]\n"
    "buffer_load_dwordx4 v[92:95], v1, s[36:39], s9 offen offset:3072 sc1\n"
    "v_mfma_f32_16x16x32_bf16 v[208:211], a[208:211], v[56:59], v[208:211]\n"
    "v_mfma_f32_16x16x32_bf16 v[228:231], a[208:211], v[60:63], v[228:231]\n"
    "v_mfma_f32_16x16x32_bf16 v[232:235], a[212:215], v[60:63], v[232:235]\n"
    "buffer_load_dwordx2 v[96:97], v11, s[36:39], s9 offen sc1\n"
    "v_mfma_f32_16x16x32_bf16 v[212:215], a[212:215], v[56:59], v[212:215]\n"
    "v_mfma_f32_16x16x32_bf16 v[192:195], a[212:215], v[52:55], v[192:195]\n"
    "buffer_load_dwordx2 v[98:99], v11, s[36:39], s9 offen offset:512 sc1\n"
    "v_mfma_f32_16x16x32_bf16 v[196:199], a[216:219], v[52:55], v[196:199]\n"
    "v_mfma_f32_16x16x32_bf16 v[216:219], a[216:219], v[56:59], v[216:219]\n"
    "v_mfma_f32_16x16x32_bf16 v[236:239], a[216:219], v[60:63], v[236:239]\n"
    "s_waitcnt vmcnt(7)\n"
    "v_mfma_f32_16x16x32_bf16 v[180:183], a[220:223], v[68:71], v[180:183]\n"
    "buffer_load_dwordx4 v[100:103], v1, s[36:39], s10 offen sc1\n"
    "v_mfma_f32_16x16x32_bf16 v[200:203], a[220:223], v[72:75], v[200:203]\n"
    "buffer_load_dwordx4 v[104:107], v1, s[36:39], s10 offen offset:1024 sc1\n"
    "v_mfma_f32_16x16x32_bf16 v[220:223], a[220:223], v[76:79], v[220:223]\n"
    "v_mfma_f32_16x16x32_bf16 v[224:227], a[224:227], v[76:79], v[224:227]\n"
    "v_mfma_f32_16x16x32_bf16 v[204:207], a[224:227], v[72:75], v[204:207]\n"
    "buffer_load_dwordx4 v[108:111], v1, s[36:39], s10 offen offset:2048 sc1\n"
    "v_mfma_f32_16x16x32_bf16 v[184:187], a[224:227], v[68:71], v[184:187]\n"
    "v_mfma_f32_16x16x32_bf16 v[188:191], a[228:231], v[68:71], v[188:191]\n"
    "buffer_load_dwordx4 v[112:115], v1, s[36:39], s10 offen offset:3072 sc1\n"
    "v_mfma_f32_16x16x32_bf16 v[208:211], a[228:231], v[72:75], v[208:211]\n"
    "v_mfma_f32_16x16x32_bf16 v[228:231], a[228:231], v[76:79], v[228:231]\n"
    "v_mfma_f32_16x16x32_bf16 v[232:235], a[232:235], v[76:79], v[232:235]\n"
    "buffer_load_dwordx2 v[116:117], v11, s[36:39], s10 offen sc1\n"
    "v_mfma_f32_16x16x32_bf16 v[212:215], a[232:235], v[72:75], v[212:215]\n"
    "v_mfma_f32_16x16x32_bf16 v[192:195], a[232:235], v[68:71], v[192:195]\n"
    "buffer_load_dwordx2 v[118:119], v11, s[36:39], s10 offen offset:512 sc1\n"
    "v_mfma_f32_16x16x32_bf16 v[196:199], a[236:239], v[68:71], v[196:199]\n"
    "v_mfma_f32_16x16x32_bf16 v[216:219], a[236:239], v[72:75], v[216:219]\n"
    "v_mfma_f32_16x16x32_bf16 v[236:239], a[236:239], v[76:79], v[236:239]\n"
    "s_branch L_opart_%=\n"
    "L_ofn10_%=:\n"
    "v_mfma_f32_16x16x32_bf16 v[180:183], a[200:203], v[52:55], v[180:183]\n"
    "buffer_load_dword v12, v6, s[36:39], s16 offen offset:512 sc1\n"
    "v_mfma_f32_16x16x32_bf16 v[200:203], a[200:203], v[56:59], v[200:203]\n"
    "v_mfma_f32_16x16x32_bf16 v[220:223], a[200:203], v[60:63], v[220:223]\n"
    "v_mfma_f32_16x16x32_bf16 v[224:227], a[204:207], v[60:63], v[224:227]\n"
    "v_mfma_f32_16x16x32_bf16 v[204:207], a[204:207], v[56:59], v[204:207]\n"
    "v_mfma_f32_16x16x32_bf16 v[184:187], a[204:207], v[52:55], v[184:187]\n"
    "v_mfma_f32_16x16x32_bf16 v[188:191], a[208:211], v[52:55], v[188:191]\n"
    "v_mfma_f32_16x16x32_bf16 v[208:211], a[208:211], v[56:59], v[208:211]\n"
    "v_mfma_f32_16x16x32_bf16 v[228:231], a[208:211], v[60:63], v[228:231]\n"
    "v_mfma_f32_16x16x32_bf16 v[232:235], a[212:215], v[60:63], v[232:235]\n"
    "v_mfma_f32_16x16x32_bf16 v[212:215], a[212:215], v[56:59], v[212:215]\n"
    "v_mfma_f32_16x16x32_bf16 v[192:195], a[212:215], v[52:55], v[192:195]\n"
    "v_mfma_f32_16x16x32_bf16 v[196:199], a[216:219], v[52:55], v[196:199]\n"
    "v_mfma_f32_16x16x32_bf16 v[216:219], a[216:219], v[56:59], v[216:219]\n"
    "v_mfma_f32_16x16x32_bf16 v[236:239], a[216:219], v[60:63], v[236:239]\n"
    "s_waitcnt vmcnt(1)\n"
    "v_cmp_ne_u32 vcc, s63, v13\n"
    "s_nop 1\n"
    "s_cmp_eq_u64 vcc, 0\n"
    "s_cbranch_scc0 L_ofn11_%=\n"
    "v_mfma_f32_16x16x32_bf16 v[180:183], a[220:223], v[68:71], v[180:183]\n"
    "buffer_load_dwordx4 v[80:83], v1, s[36:39], s9 offen sc1\n"
    "v_mfma_f32_16x16x32_bf16 v[200:203], a[220:223], v[72:75], v[200:203]\n"
    "buffer_load_dwordx4 v[84:87], v1, s[36:39], s9 offen offset:1024 sc1\n"
    "v_mfma_f32_16x16x32_bf16 v[220:223], a[220:223], v[76:79], v[220:223]\n"
    "buffer_load_dwordx4 v[88:91], v1, s[36:39], s9 offen offset:2048 sc1\n"
    "v_mfma_f32_16x16x32_bf16 v[224:227], a[224:227], v[76:79], v[224:227]\n"
    "buffer_load_dwordx4 v[92:95], v1, s[36:39], s9 offen offset:3072 sc1\n"
    "v_mfma_f32_16x16x32_bf16 v[204:207], a[224:227], v[72:75], v[204:207]\n"
    "buffer_load_dwordx2 v[96:97], v11, s[36:39], s9 offen sc1\n"
    "v_mfma_f32_16x16x32_bf16 v[184:187], a[224:227], v[68:71], v[184:187]\n"
    "buffer_load_dwordx2 v[98:99], v11, s[36:39], s9 offen offset:512 sc1\n"
    "v_mfma_f32_16x16x32_bf16 v[188:191], a[228:231], v[68:71], v[188:191]\n"
    "buffer_load_dwordx4 v[100:103], v1, s[36:39], s10 offen sc1\n"
    "v_mfma_f32_16x16x32_bf16 v[208:211], a[228:231], v[72:75], v[208:211]\n"
    "v_mfma_f32_16x16x32_bf16 v[228:231], a[228:231], v[76:79], v[228:231]\n"
    "buffer_load_dwordx4 v[104:107], v1, s[36:39], s10 offen offset:1024 sc1\n"
    "v_mfma_f32_16x16x32_bf16 v[232:235], a[232:235], v[76:79], v[232:235]\n"
    "buffer_load_dwordx4 v[108:111], v1, s[36:39], s10 offen offset:2048 sc1\n"
    "v_mfma_f32_16x16x32_bf16 v[212:215], a[232:235], v[72:75], v[212:215]\n"
    "buffer_load_dwordx4 v[112:115], v1, s[36:39], s10 offen offset:3072 sc1\n"
    "v_mfma_f32_16x16x32_bf16 v[192:195], a[232:235], v[68:71], v[192:195]\n"
    "buffer_load_dwordx2 v[116:117], v11, s[36:39], s10 offen sc1\n"
    "v_mfma_f32_16x16x32_bf16 v[196:199], a[236:239], v[68:71], v[196:199]\n"
    "v_mfma_f32_16x16x32_bf16 v[216:219], a[236:239], v[72:75], v[216:219]\n"
    "buffer_load_dwordx2 v[118:119], v11, s[36:39], s10 offen offset:512 sc1\n"
    "v_mfma_f32_16x16x32_bf16 v[236:239], a[236:239], v[76:79], v[236:239]\n"
    "s_branch L_opart_%=\n"
    "L_ofn11_%=:\n"
    "v_mfma_f32_16x16x32_bf16 v[180:183], a[220:223], v[68:71], v[180:183]\n"
    "v_mfma_f32_16x16x32_bf16 v[200:203], a[220:223], v[72:75], v[200:203]\n"
    "v_mfma_f32_16x16x32_bf16 v[220:223], a[220:223], v[76:79], v[220:223]\n"
    "v_mfma_f32_16x16x32_bf16 v[224:227], a[224:227], v[76:79], v[224:227]\n"
    "v_mfma_f32_16x16x32_bf16 v[204:207], a[224:227], v[72:75], v[204:207]\n"
    "v_mfma_f32_16x16x32_bf16 v[184:187], a[224:227], v[68:71], v[184:187]\n"
    "v_mfma_f32_16x16x32_bf16 v[188:191], a[228:231], v[68:71], v[188:191]\n"
    "v_mfma_f32_16x16x32_bf16 v[208:211], a[228:231], v[72:75], v[208:211]\n"
    "v_mfma_f32_16x16x32_bf16 v[228:231], a[228:231], v[76:79], v[228:231]\n"
    "v_mfma_f32_16x16x32_bf16 v[232:235], a[232:235], v[76:79], v[232:235]\n"
    "v_mfma_f32_16x16x32_bf16 v[212:215], a[232:235], v[72:75], v[212:215]\n"
    "v_mfma_f32_16x16x32_bf16 v[192:195], a[232:235], v[68:71], v[192:195]\n"
    "v_mfma_f32_16x16x32_bf16 v[196:199], a[236:239], v[68:71], v[196:199]\n"
    "v_mfma_f32_16x16x32_bf16 v[216:219], a[236:239], v[72:75], v[216:219]\n"
    "v_mfma_f32_16x16x32_bf16 v[236:239], a[236:239], v[76:79], v[236:239]\n"
    "s_waitcnt vmcnt(0)\n"
    "v_cmp_ne_u32 vcc, s63, v12\n"
    "s_nop 1\n"
    "s_cmp_eq_u64 vcc, 0\n"
    "s_cbranch_scc0 L_of_slow_%=\n"
    "L_of_ok_%=:\n"
    "buffer_load_dwordx4 v[80:83], v1, s[36:39], s9 offen sc1\n"
    "buffer_load_dwordx4 v[84:87], v1, s[36:39], s9 offen offset:1024 sc1\n"
    "buffer_load_dwordx4 v[88:91], v1, s[36:39], s9 offen offset:2048 sc1\n"
    "buffer_load_dwordx4 v[92:95], v1, s[36:39], s9 offen offset:3072 sc1\n"
    "buffer_load_dwordx2 v[96:97], v11, s[36:39], s9 offen sc1\n"
    "buffer_load_dwordx2 v[98:99], v11, s[36:39], s9 offen offset:512 sc1\n"
    "buffer_load_dwordx4 v[100:103], v1, s[36:39], s10 offen sc1\n"
    "buffer_load_dwordx4 v[104:107], v1, s[36:39], s10 offen offset:1024 sc1\n"
    "buffer_load_dwordx4 v[108:111], v1, s[36:39], s10 offen offset:2048 sc1\n"
    "buffer_load_dwordx4 v[112:115], v1, s[36:39], s10 offen offset:3072 sc1\n"
    "buffer_load_dwordx2 v[116:117], v11, s[36:39], s10 offen sc1\n"
    "buffer_load_dwordx2 v[118:119], v11, s[36:39], s10 offen offset:512 sc1\n"
    "s_branch L_opart_%=\n"
    "L_of_slow_%=:\n"
    "s_movk_i32 s75, 20000\n"
    "L_of_spin_%=:\n"
    "s_sleep 1\n"
    "buffer_load_dword v14, v6, s[36:39], s16 offen offset:512 sc1\n"
    "s_waitcnt vmcnt(0)\n"
    "v_cmp_ne_u32_e64 vcc, s63, v14\n"
    "s_nop 1\n"
    "s_cmp_eq_u64 vcc, 0\n"
    "s_cbranch_scc1 L_of_ok_%=\n"
    "s_sub_u32 s75, s75, 1\n"
    "s_cbranch_scc0 L_of_spin_%=\n"
    "s_branch L_of_ok_%=\n"
    "L_hk_slow_%=:\n"
    "s_movk_i32 s75, 20000\n"
    "L_hk_spin_%=:\n"
    "s_sleep 1\n"
    "buffer_load_dword v14, v6, s[36:39], s16 offen sc1\n"
    "s_waitcnt vmcnt(0)\n"
    "v_cmp_ne_u32 vcc, s63, v14\n"
    "s_nop 1\n"
    "s_cmp_eq_u64 vcc, 0\n"
    "s_cbranch_scc1 L_hk_ok_%=\n"
    "s_sub_u32 s75, s75, 1\n"
    "s_cbranch_scc0 L_hk_spin_%=\n"
    "s_branch L_hk_ok_%=\n"
    "L_f1kv_slow_%=:\n"
    "s_movk_i32 s75, 20000\n"
    "L_f1kv_spin_%=:\n"
    "s_sleep 4\n"
    "buffer_load_dword v63, v47, s[36:39], s44 offen sc1\n"
    "s_waitcnt vmcnt(0)\n"
    "v_cmp_ne_u32 vcc, s63, v63\n"
    "s_nop 1\n"
    "s_cmp_eq_u64 vcc, 0\n"
    "s_cbranch_scc1 L_f1kv_ok_%=\n"
    "s_sub_u32 s75, s75, 1\n"
    "s_cbranch_scc0 L_f1kv_spin_%=\n"
    "s_branch L_f1kv_ok_%=\n"
    "L_gkv_slow_%=:\n"
    "s_movk_i32 s75, 20000\n"
    "L_gkv_spin_%=:\n"
    "s_sleep 4\n"
    "buffer_load_dwordx2 v[64:65], v45, s[36:39], s65 offen offset:0 sc1\n"
    "buffer_load_dwordx2 v[66:67], v45, s[36:39], s65 offen offset:128 sc1\n"
    "s_waitcnt vmcnt(0)\n"
    "v_xor_b32 v44, s63, v65\n"
    "v_xor_b32 v43, s63, v67\n"
    "v_or_b32 v44, v44, v43\n"
    "v_cmp_ne_u32_e64 vcc, 0, v44\n"
    "s_nop 1\n"
    "s_cmp_eq_u64 vcc, 0\n"
    "s_cbranch_scc1 L_gkv_ok_%=\n"
    "s_sub_u32 s75, s75, 1\n"
    "s_cbranch_scc0 L_gkv_spin_%=\n"
    "s_branch L_gkv_ok_%=\n"
    "L_ka_f_%=:\n"
    "buffer_load_dwordx4 v[76:79], v12, s[36:39], s19 offen sc1\n"
    "buffer_load_dwordx4 v[80:83], v12, s[36:39], s20 offen sc1\n"
    "s_branch L_ka_r0_%=\n"
    "L_ka_c0_%=:\n"
    "s_waitcnt vmcnt(6)\n"
    "v_max3_u32 v84, v76, v77, v78\n"
    "v_max3_u32 v85, v79, v80, v81\n"
    "v_max3_u32 v84, v84, v82, v83\n"
    "v_max_u32 v84, v84, v85\n"
    "v_cmp_eq_u32 vcc, -1, v84\n"
    "s_nop 1\n"
    "s_and_b64 vcc, vcc, s[22:23]\n"
    "s_cbranch_scc1 L_ka_f0_%=\n"
    "v_add_f32 v76, v76, v80\n"
    "v_add_f32 v77, v77, v81\n"
    "v_add_f32 v78, v78, v82\n"
    "v_add_f32 v79, v79, v83\n"
    "s_nop 0\n"
    "v_permlane32_swap_b32 v76, v78\n"
    "v_permlane32_swap_b32 v77, v79\n"
    "v_add_f32 v76, v76, v78\n"
    "v_add_f32 v77, v77, v79\n"
    "v_mov_b32 v78, v76\n"
    "v_mov_b32 v79, v77\n"
    "s_nop 0\n"
    "v_permlane16_swap_b32 v76, v78\n"
    "v_permlane16_swap_b32 v77, v79\n"
    "v_add_f32 v76, v76, v78\n"
    "v_add_f32 v77, v77, v79\n"
    "s_nop 0\n"
    "v_add_f32_dpp v76, v76, v76 row_ror:8 row_mask:0xf bank_mask:0xf\n"
    "v_add_f32_dpp v77, v77, v77 row_ror:8 row_mask:0xf bank_mask:0xf\n"
    "v_cvt_pk_bf16_f32 v76, v76, v77\n"
    "v_min_u32_e64 v76, v76, -2\n"
    "buffer_store_dword v76, v13, s[36:39], s88 offen sc1\n"
    "s_mov_b64 s[22:23], 0\n"
    "s_branch L_ka_r1_%=\n"
    "L_ka_f0_%=:\n"
    "buffer_load_dwordx4 v[76:79], v12, s[36:39], s19 offen sc1\n"
    "buffer_load_dwordx4 v[80:83], v12, s[36:39], s20 offen sc1\n"
    "s_branch L_ka_r1_%=\n"
    "L_ka_slow_%=:\n"
    "s_movk_i32 s75, 20000\n"
    "s_branch L_ka_w_%=\n"
    "L_ka_spin_%=:\n"
    "buffer_load_dwordx4 v[76:79], v12, s[36:39], s19 offen sc1\n"
    "buffer_load_dwordx4 v[80:83], v12, s[36:39], s20 offen sc1\n"
    "L_ka_w_%=:\n"
    "s_waitcnt vmcnt(0)\n"
    "v_max3_u32 v84, v76, v77, v78\n"
    "v_max3_u32 v85, v79, v80, v81\n"
    "v_max3_u32 v84, v84, v82, v83\n"
    "v_max_u32 v84, v84, v85\n"
    "v_cmp_eq_u32 vcc, -1, v84\n"
    "s_nop 1\n"
    "s_and_b64 vcc, vcc, s[22:23]\n"
    "s_cbranch_scc0 L_ka_ok_%=\n"
    "s_sub_u32 s75, s75, 1\n"
    "s_cbranch_scc0 L_ka_spin_%=\n"
    "L_ka_ok_%=:\n"
    "v_add_f32 v76, v76, v80\n"
    "v_add_f32 v77, v77, v81\n"
    "v_add_f32 v78, v78, v82\n"
    "v_add_f32 v79, v79, v83\n"
    "s_nop 0\n"
    "v_permlane32_swap_b32 v76, v78\n"
    "v_permlane32_swap_b32 v77, v79\n"
    "v_add_f32 v76, v76, v78\n"
    "v_add_f32 v77, v77, v79\n"
    "v_mov_b32 v78, v76\n"
    "v_mov_b32 v79, v77\n"
    "s_nop 0\n"
    "v_permlane16_swap_b32 v76, v78\n"
    "v_permlane16_swap_b32 v77, v79\n"
    "v_add_f32 v76, v76, v78\n"
    "v_add_f32 v77, v77, v79\n"
    "s_nop 0\n"
    "v_add_f32_dpp v76, v76, v76 row_ror:8 row_mask:0xf bank_mask:0xf\n"
    "v_add_f32_dpp v77, v77, v77 row_ror:8 row_mask:0xf bank_mask:0xf\n"
    "v_cvt_pk_bf16_f32 v76, v76, v77\n"
    "v_min_u32_e64 v76, v76, -2\n"
    "buffer_store_dword v76, v13, s[36:39], s88 offen sc1\n"
    "s_branch L_ka_done_%=\n"
    "L_hfk_slow_%=:\n"
    "s_movk_i32 s75, 20000\n"
    "L_hfk_spin_%=:\n"
    "s_sleep 1\n"
    "buffer_load_dword v14, v6, s[36:39], s16 offen offset:512 sc1\n"
    "s_waitcnt vmcnt(0)\n"
    "v_cmp_ne_u32_e64 vcc, s63, v14\n"
    "s_nop 1\n"
    "s_cmp_eq_u64 vcc, 0\n"
    "s_cbranch_scc1 L_hfk_ok_%=\n"
    "s_sub_u32 s75, s75, 1\n"
    "s_cbranch_scc0 L_hfk_spin_%=\n"
    "s_branch L_hfk_ok_%=\n"
    "L_f1q_slow_%=:\n"
    "s_movk_i32 s75, 20000\n"
    "L_f1q_spin_%=:\n"
    "s_sleep 4\n"
    "buffer_load_dword v63, v47, s[36:39], s44 offen sc1\n"
    "s_waitcnt vmcnt(0)\n"
    "v_cmp_ne_u32 vcc, s63, v63\n"
    "s_nop 1\n"
    "s_cmp_eq_u64 vcc, 0\n"
    "s_cbranch_scc1 L_f1q_ok_%=\n"
    "s_sub_u32 s75, s75, 1\n"
    "s_cbranch_scc0 L_f1q_spin_%=\n"
    "s_branch L_f1q_ok_%=\n"
    "L_gq_slow_%=:\n"
    "s_movk_i32 s75, 20000\n"
    "L_gq_spin_%=:\n"
    "s_sleep 4\n"
    "buffer_load_dwordx2 v[64:65], v45, s[36:39], s65 offen offset:0 sc1\n"
    "buffer_load_dwordx2 v[66:67], v45, s[36:39], s65 offen offset:128 sc1\n"
    "buffer_load_dwordx2 v[68:69], v46, s[36:39], s65 offen offset:0 sc1\n"
    "buffer_load_dwordx2 v[70:71], v46, s[36:39], s65 offen offset:128 sc1\n"
    "s_waitcnt vmcnt(0)\n"
    "v_xor_b32 v44, s63, v65\n"
    "v_xor_b32 v43, s63, v67\n"
    "v_or_b32 v44, v44, v43\n"
    "v_xor_b32 v43, s63, v69\n"
    "v_or_b32 v44, v44, v43\n"
    "v_xor_b32 v43, s63, v71\n"
    "v_or_b32 v44, v44, v43\n"
    "v_cmp_ne_u32_e64 vcc, 0, v44\n"
    "s_nop 1\n"
    "s_cmp_eq_u64 vcc, 0\n"
    "s_cbranch_scc1 L_gq_ok_%=\n"
    "s_sub_u32 s75, s75, 1\n"
    "s_cbranch_scc0 L_gq_spin_%=\n"
    "s_branch L_gq_ok_%=\n"
    "L_hfq_slow_%=:\n"
    "s_movk_i32 s75, 20000\n"
    "L_hfq_spin_%=:\n"
    "s_sleep 1\n"
    "buffer_load_dword v14, v6, s[36:39], s16 offen offset:512 sc1\n"
    "s_waitcnt vmcnt(0)\n"
    "v_cmp_ne_u32 vcc, s63, v14\n"
    "s_nop 1\n"
    "s_cmp_eq_u64 vcc, 0\n"
    "s_cbranch_scc1 L_hfq_ok_%=\n"
    "s_sub_u32 s75, s75, 1\n"
    "s_cbranch_scc0 L_hfq_spin_%=\n"
    "s_branch L_hfq_ok_%=\n"
    "L_pb_slow_%=:\n"
    "s_movk_i32 s75, 20000\n"
    "L_pb_spin_%=:\n"
    "s_sleep 2\n"
    "buffer_load_dwordx4 v[80:83], v1, s[36:39], s21 offen sc1\n"
    "s_waitcnt vmcnt(0)\n"
    "v_max_u32 v84, v80, v81\n"
    "v_max_u32_e64 v85, v82, v83\n"
    "v_cmp_eq_u32_e64 s[22:23], v84, -1\n"
    "v_cmp_eq_u32 vcc, -1, v85\n"
    "s_nop 0\n"
    "s_and_b32 s23, s23, 0x1fffff\n"
    "s_and_b32 vcc_hi, vcc_hi, 0xfffff\n"
    "s_or_b64 vcc, vcc, s[22:23]\n"
    "s_cmp_eq_u64 vcc, 0\n"
    "s_cbranch_scc1 L_pb_ok_%=\n"
    "s_sub_u32 s75, s75, 1\n"
    "s_cbranch_scc0 L_pb_spin_%=\n"
    "s_branch L_pb_ok_%=\n"
    "s_nop 0\n"
    "s_nop 0\n"
    "s_nop 0\n"
    "s_nop 0\n"
    "s_nop 0\n"
    "s_nop 0\n"
    "s_nop 0\n"
    "s_nop 0\n"
    "L_end_%=:\n"
    "L_z1_%=:\n"
    "L_z2_%=:\n"
    : "+{v0}"(tid), "+{s3}"(lds_base)
    : "{s[0:1]}"(__builtin_amdgcn_kernarg_segment_ptr()), "{s2}"(blockIdx.x)
    :
      "v1", "v2", "v3", "v4", "v5", "v6", "v7", "v8", "v9", "v10", "v11", "v12", "v13", "v14", "v15", "v16",
      "v17", "v18", "v19", "v20", "v21", "v22", "v23", "v24", "v25", "v26", "v27", "v28", "v29", "v30", "v31", "v32",
      "v33", "v34", "v35", "v36", "v37", "v38", "v39", "v40", "v41", "v42", "v43", "v44", "v45", "v46", "v47", "v48",
      "v49", "v50", "v51", "v52", "v53", "v54", "v55", "v56", "v57", "v58", "v59", "v60", "v61", "v62", "v63", "v64",
      "v65", "v66", "v67", "v68", "v69", "v70", "v71", "v72", "v73", "v74", "v75", "v76", "v77", "v78", "v79", "v80",
      "v81", "v82", "v83", "v84", "v85", "v86", "v87", "v88", "v89", "v90", "v91", "v92", "v93", "v94", "v95", "v96",
      "v97", "v98", "v99", "v100", "v101", "v102", "v103", "v104", "v105", "v106", "v107", "v108", "v109", "v110", "v111", "v112",
      "v113", "v114", "v115", "v116", "v117", "v118", "v119", "v120", "v121", "v122", "v123", "v124", "v125", "v126", "v127", "v128",
      "v129", "v130", "v131", "v132", "v133", "v134", "v135", "v136", "v137", "v138", "v139", "v140", "v141", "v142", "v143", "v144",
      "v145", "v146", "v147", "v148", "v149", "v150", "v151", "v152", "v153", "v154", "v155", "v156", "v157", "v158", "v159", "v160",
      "v161", "v162", "v163", "v164", "v165", "v166", "v167", "v168", "v169", "v170", "v171", "v172", "v173", "v174", "v175", "v176",
      "v177", "v178", "v179", "v180", "v181", "v182", "v183", "v184", "v185", "v186", "v187", "v188", "v189", "v190", "v191", "v192",
      "v193", "v194", "v195", "v196", "v197", "v198", "v199", "v200", "v201", "v202", "v203", "v204", "v205", "v206", "v207", "v208",
      "v209", "v210", "v211", "v212", "v213", "v214", "v215", "v216", "v217", "v218", "v219", "v220", "v221", "v222", "v223", "v224",
      "v225", "v226", "v227", "v228", "v229", "v230", "v231", "v232", "v233", "v234", "v235", "v236", "v237", "v238", "v239", "v240",
      "v241", "v242", "v243", "v244", "v245", "v246", "v247", "v248", "v249", "v250", "v251", "v252", "v253", "v254", "v255", "a0",
      "a1", "a2", "a3", "a4", "a5", "a6", "a7", "a8", "a9", "a10", "a11", "a12", "a13", "a14", "a15", "a16",
      "a17", "a18", "a19", "a20", "a21", "a22", "a23", "a24", "a25", "a26", "a27", "a28", "a29", "a30", "a31", "a32",
      "a33", "a34", "a35", "a36", "a37", "a38", "a39", "a40", "a41", "a42", "a43", "a44", "a45", "a46", "a47", "a48",
      "a49", "a50", "a51", "a52", "a53", "a54", "a55", "a56", "a57", "a58", "a59", "a60", "a61", "a62", "a63", "a64",
      "a65", "a66", "a67", "a68", "a69", "a70", "a71", "a72", "a73", "a74", "a75", "a76", "a77", "a78", "a79", "a80",
      "a81", "a82", "a83", "a84", "a85", "a86", "a87", "a88", "a89", "a90", "a91", "a92", "a93", "a94", "a95", "a96",
      "a97", "a98", "a99", "a100", "a101", "a102", "a103", "a104", "a105", "a106", "a107", "a108", "a109", "a110", "a111", "a112",
      "a113", "a114", "a115", "a116", "a117", "a118", "a119", "a120", "a121", "a122", "a123", "a124", "a125", "a126", "a127", "a128",
      "a129", "a130", "a131", "a132", "a133", "a134", "a135", "a136", "a137", "a138", "a139", "a140", "a141", "a142", "a143", "a144",
      "a145", "a146", "a147", "a148", "a149", "a150", "a151", "a152", "a153", "a154", "a155", "a156", "a157", "a158", "a159", "a160",
      "a161", "a162", "a163", "a164", "a165", "a166", "a167", "a168", "a169", "a170", "a171", "a172", "a173", "a174", "a175", "a176",
      "a177", "a178", "a179", "a180", "a181", "a182", "a183", "a184", "a185", "a186", "a187", "a188", "a189", "a190", "a191", "a192",
      "a193", "a194", "a195", "a196", "a197", "a198", "a199", "a200", "a201", "a202", "a203", "a204", "a205", "a206", "a207", "a208",
      "a209", "a210", "a211", "a212", "a213", "a214", "a215", "a216", "a217", "a218", "a219", "a220", "a221", "a222", "a223", "a224",
      "a225", "a226", "a227", "a228", "a229", "a230", "a231", "a232", "a233", "a234", "a235", "a236", "a237", "a238", "a239", "a240",
      "a241", "a242", "a243", "a244", "a245", "a246", "a247", "a248", "a249", "a250", "a251", "a252", "a253", "a254", "a255", "s4",
      "s5", "s6", "s7", "s8", "s9", "s10", "s11", "s12", "s13", "s14", "s15", "s16", "s17", "s18", "s19", "s20",
      "s21", "s22", "s23", "s24", "s25", "s26", "s27", "s28", "s29", "s30", "s31", "s32", "s33", "s34", "s35", "s36",
      "s37", "s38", "s39", "s40", "s41", "s42", "s43", "s44", "s45", "s46", "s47", "s48", "s49", "s50", "s51", "s52",
      "s53", "s54", "s55", "s56", "s57", "s58", "s59", "s60", "s61", "s62", "s63", "s64", "s65", "s66", "s67", "s68",
      "s69", "s70", "s71", "s72", "s73", "s74", "s75", "s76", "s77", "s78", "s79", "s80", "s81", "s82", "s83", "s84",
      "s85", "s86", "s87", "s88", "s89", "s90", "s91", "s92", "s93", "s94", "s95", "s96", "s97", "s98", "s99", "s100",
      "s101", "vcc", "scc", "memory"
  );
}

extern "C" void kernel_launch(void* const* d_in, const int* in_sizes, int n_in, void* d_out, int out_size, void* d_ws,
                              size_t ws_size, hipStream_t stream) {
  const unsigned F = 0x20000u;
  gated_mix_fused<<<256, 256, 0, stream>>>(d_in[0], 2621440u, F, d_in[1], 2457600u + 2048u, F, d_in[4], 1966080u + 2048u, F, d_ws,
                                           13402112u, F, d_out, 2621440u, F, d_in[6], 1280u, F, d_in[2], d_in[5]);
}
```
